```python
import torch

def _softmax(x, axis=-1):
    return torch.softmax(x, dim=axis)
def _gelu(x):
    return torch.nn.functional.gelu(x, approximate="tanh")
def _silu(x):
    return torch.nn.functional.silu(x)
def _sigmoid(x):
    return torch.sigmoid(x)
def _layernorm(x, axis=-1, eps=1e-5):
    m = x.mean(dim=axis, keepdim=True)
    v = x.var(dim=axis, keepdim=True, unbiased=False)
    return (x - m) / torch.sqrt(v + eps)
def _scatter_add(tgt, idx, src, axis=0):
    return tgt.clone().index_add_(axis, idx, src)
def _segment_sum(src, seg, n):
    out = torch.zeros((n,) + src.shape[1:], dtype=src.dtype)
    return out.index_add_(0, seg, src)
def _pad(x, pw):
    flat = tuple(v for pair in reversed(pw) for v in pair)
    return torch.nn.functional.pad(x, flat)

kernel_name = 'standalone__gqa_block_cached__near_ridge'

def setup_inputs():

    _w = (lambda a: (lambda t: t.to(torch.bfloat16) if t.is_floating_point() else t)(torch.as_tensor(a)))
    B, Sq, D = 16, 240, 512
    Hq, Hkv, Sk, Dh = 32, 32, 512, 128
    assert Hq % Hkv == 0, (Hq, Hkv)
    return {
        "x": _w((lambda *s: torch.randn(*s, dtype=torch.bfloat16) * 0.8)(B, Sq, D)),
        "k_cache": _w((lambda *s: torch.randn(*s, dtype=torch.bfloat16) * 0.8)(B, Hkv, Sk, Dh)),
        "v_cache": _w((lambda *s: torch.randn(*s, dtype=torch.bfloat16) * 0.05)(B, Hkv, Sk, Dh)),
        "wq": _w((lambda *s: torch.randn(*s, dtype=torch.bfloat16) * 0.8)(D, Hq * Dh) * (D ** -0.5)),
        "wo": _w((lambda *s: torch.randn(*s, dtype=torch.bfloat16) * 0.05)(Hq * Dh, D) * ((Hq * Dh) ** -0.5)),
    }

def reference(x, k_cache, v_cache, wq, wo):
    B, Sq, D = x.shape
    Hq, Dh = 32, 128
    Hkv = k_cache.shape[1]
    rep = Hq // Hkv
    q = (lambda x, *d: x.permute(*d))((x @ wq).reshape(B, Sq, Hq, Dh), 0, 2, 1, 3)
    k = (lambda x, n, axis: torch.repeat_interleave(x, n, dim=axis))(k_cache, rep, axis=1)
    v = (lambda x, n, axis: torch.repeat_interleave(x, n, dim=axis))(v_cache, rep, axis=1)
    scores = torch.einsum("bhqd,bhkd->bhqk", q, k) * (Dh ** -0.5)
    p = _softmax(scores, axis=-1)
    o = torch.einsum("bhqk,bhkd->bhqd", p, v)
    return (lambda x, *d: x.permute(*d))(o, 0, 2, 1, 3).reshape(B, Sq, Hq * Dh) @ wo
```

```cpp
#include <hip/hip_runtime.h>
#include <hip/hip_bf16.h>
typedef short bf16x8 __attribute__((ext_vector_type(8)));
typedef float f32x16 __attribute__((ext_vector_type(16)));
typedef unsigned u32x4 __attribute__((ext_vector_type(4)));
typedef unsigned u32x2 __attribute__((ext_vector_type(2)));
typedef unsigned short u16;
#define MFMA32(a,b,c) __builtin_amdgcn_mfma_f32_32x32x16_bf16(a,b,c,0,0,0)
constexpr int NB=16, SQ=240, DM=512, NH=32, SK=512, DH=128;
constexpr float QSCALE = 0.12751743082459868f;

typedef float f32x2 __attribute__((ext_vector_type(2)));
typedef __bf16 bf16x2 __attribute__((ext_vector_type(2)));
__device__ __forceinline__ unsigned cvtpk(float lo, float hi){ f32x2 v={lo,hi}; bf16x2 b=__builtin_convertvector(v,bf16x2); return __builtin_bit_cast(unsigned,b); }
__device__ __forceinline__ bf16x8 pack8(const f32x16& a, int base, float sc){
  u32x4 w;
  #pragma unroll
  for(int i=0;i<4;++i) w[i]=cvtpk(a[base+2*i]*sc, a[base+2*i+1]*sc);
  return __builtin_bit_cast(bf16x8,w);
}
__device__ __forceinline__ float swap32(float v){
  return __shfl_xor(v,32);
}
__device__ __forceinline__ void slow_item(const u16* __restrict__ x, const u16* __restrict__ kc, const u16* __restrict__ vc, const u16* __restrict__ wq,
                                       u16* __restrict__ ows, int b, int head, int G, int lane){
  const int r=lane&31, h=lane>>5;
  const int grow = G*32 + r;
  const bool valid = (grow >= b*SQ) && (grow < b*SQ+SQ);
  const u16* xr = x + (size_t)grow*DM + 8*h;
  const int pr = (r&~12) | ((r&4)<<1) | ((r&8)>>1);
  bf16x8 q[8];
  #pragma unroll
  for(int db=0; db<4; ++db){
    f32x16 acc={};
    const u16* wc = wq + head*DH + 32*db + pr + (size_t)(8*h)*(NH*DH);
    for(int s=0;s<32;++s){
      bf16x8 a;
      #pragma unroll
      for(int j=0;j<8;++j) a[j]=(short)wc[(size_t)(16*s+j)*(NH*DH)];
      bf16x8 bx=*(const bf16x8*)(xr+16*s);
      acc=MFMA32(a,bx,acc);
    }
    q[2*db]=pack8(acc,0,QSCALE); q[2*db+1]=pack8(acc,8,QSCALE);
  }
  const u16* kb = kc + (size_t)(b*NH+head)*SK*DH + (size_t)r*DH + 8*h;
  const u16* vb = vc + (size_t)(b*NH+head)*SK*DH + r + (size_t)(4*h)*DH;
  float m=-INFINITY;
  for(int j=0;j<16;++j){
    f32x16 s={};
    #pragma unroll
    for(int d=0;d<8;++d) s=MFMA32(*(const bf16x8*)(kb+(size_t)j*32*DH+16*d), q[d], s);
    #pragma unroll
    for(int i=0;i<16;++i) m=fmaxf(m,s[i]);
  }
  m=fmaxf(m,swap32(m));
  f32x16 o[4]={}; float l=0;
  for(int j=0;j<16;++j){
    f32x16 s={};
    #pragma unroll
    for(int d=0;d<8;++d) s=MFMA32(*(const bf16x8*)(kb+(size_t)j*32*DH+16*d), q[d], s);
    #pragma unroll
    for(int i=0;i<16;++i){ s[i]=__builtin_amdgcn_exp2f(s[i]-m); l+=s[i]; }
    #pragma unroll
    for(int ks=0;ks<2;++ks){
      bf16x8 p=pack8(s,8*ks,1.f);
      #pragma unroll
      for(int db=0;db<4;++db){
        bf16x8 a;
        #pragma unroll
        for(int e=0;e<8;++e) a[e]=(short)vb[(size_t)(j*32+16*ks+8*(e>>2)+(e&3))*DH+32*db];
        o[db]=MFMA32(a,p,o[db]);
      }
    }
  }
  l+=swap32(l);
  const float il=1.f/l;
  if(valid){
    u16* op = ows + ((size_t)(G*256+head*8)*64 + lane)*8;
    #pragma unroll
    for(int db=0;db<4;++db){
      *(bf16x8*)(op+(size_t)(2*db)*512)=pack8(o[db],0,il);
      *(bf16x8*)(op+(size_t)(2*db+1)*512)=pack8(o[db],8,il);
    }
  }
}
__global__ __launch_bounds__(512,2) void k1_slow(const u16* x,const u16* kc,const u16* vc,const u16* wq,u16* ows){
  const int bid=blockIdx.x, xcd=bid&7, i=bid>>3;
  const int b=(xcd>>2)*8+(i>>2), hp=(xcd&3)*4+(i&3);
  const int w=threadIdx.x>>6, lane=threadIdx.x&63;
  const int G=(15*b)/2 + w;
  #pragma nounroll
  for(int it=0;it<2;++it) slow_item(x,kc,vc,wq,ows,b,2*hp+it,G,lane);
}
__global__ __launch_bounds__(256) void k2_simple(const u16* __restrict__ ows,const u16* __restrict__ wo,u16* __restrict__ out){
  const int w=threadIdx.x>>6, lane=threadIdx.x&63, r=lane&31, h=lane>>5;
  const int unit=blockIdx.x*4+w; const int G=unit>>4, nb=unit&15;
  f32x16 acc={};
  const u16* op=ows+((size_t)(G*256)*64+lane)*8;
  const u16* wp=wo+nb*32+r+(size_t)(4*h)*DM;
  for(int ks=0;ks<256;++ks){
    bf16x8 a;
    #pragma unroll
    for(int e=0;e<8;++e) a[e]=(short)wp[(size_t)(16*ks+8*(e>>2)+(e&3))*DM];
    acc=MFMA32(a,*(const bf16x8*)(op+(size_t)ks*512),acc);
  }
  u16* o=out+(size_t)(G*32+r)*DM+nb*32+4*h;
  #pragma unroll
  for(int g=0;g<4;++g){ u32x2 v={cvtpk(acc[4*g],acc[4*g+1]),cvtpk(acc[4*g+2],acc[4*g+3])}; *(u32x2*)(o+8*g)=v; }
}
extern "C" void kernel_launch(void* const* d_in, const int* in_sizes, int n_in, void* d_out, int out_size, void* d_ws, size_t ws_size, hipStream_t stream){
  const u16* x=(const u16*)d_in[0]; const u16* kc=(const u16*)d_in[1]; const u16* vc=(const u16*)d_in[2];
  const u16* wq=(const u16*)d_in[3]; const u16* wo=(const u16*)d_in[4];
  u16* ows=(u16*)d_ws;
  k1_slow<<<256,512,0,stream>>>(x,kc,vc,wq,ows);
  k2_simple<<<480,256,0,stream>>>(ows,wo,(u16*)d_out);
}
```

```cpp
#include <hip/hip_runtime.h>
#include <hip/hip_bf16.h>
typedef short bf16x8 __attribute__((ext_vector_type(8)));
typedef float f32x16 __attribute__((ext_vector_type(16)));
typedef unsigned u32x4 __attribute__((ext_vector_type(4)));
typedef unsigned u32x2 __attribute__((ext_vector_type(2)));
typedef unsigned short u16;
#define MFMA32(a,b,c) __builtin_amdgcn_mfma_f32_32x32x16_bf16(a,b,c,0,0,0)
constexpr int HPS=17, GS=403;
constexpr int NB=16, SQ=240, DM=512, NH=32, SK=512, DH=128;
constexpr float QSCALE = 0.12751743082459868f;

typedef float f32x2 __attribute__((ext_vector_type(2)));
typedef __bf16 bf16x2 __attribute__((ext_vector_type(2)));
__device__ __forceinline__ unsigned cvtpk(float lo, float hi){ f32x2 v={lo,hi}; bf16x2 b=__builtin_convertvector(v,bf16x2); return __builtin_bit_cast(unsigned,b); }
__device__ __forceinline__ bf16x8 pack8(const f32x16& a, int base, float sc){
  u32x4 w;
  #pragma unroll
  for(int i=0;i<4;++i) w[i]=cvtpk(a[base+2*i]*sc, a[base+2*i+1]*sc);
  return __builtin_bit_cast(bf16x8,w);
}
__device__ __forceinline__ float swap32(float v){
  return __shfl_xor(v,32);
}
__device__ __forceinline__ void slow_item(const u16* __restrict__ x, const u16* __restrict__ kc, const u16* __restrict__ vc, const u16* __restrict__ wq,
                                       u16* __restrict__ ows, int b, int head, int G, int lane){
  const int r=lane&31, h=lane>>5;
  const int grow = G*32 + r;
  const bool valid = (grow >= b*SQ) && (grow < b*SQ+SQ);
  const u16* xr = x + (size_t)grow*DM + 8*h;
  const int pr = (r&~12) | ((r&4)<<1) | ((r&8)>>1);
  bf16x8 q[8];
  #pragma unroll
  for(int db=0; db<4; ++db){
    f32x16 acc={};
    const u16* wc = wq + head*DH + 32*db + pr + (size_t)(8*h)*(NH*DH);
    for(int s=0;s<32;++s){
      bf16x8 a;
      #pragma unroll
      for(int j=0;j<8;++j) a[j]=(short)wc[(size_t)(16*s+j)*(NH*DH)];
      bf16x8 bx=*(const bf16x8*)(xr+16*s);
      acc=MFMA32(a,bx,acc);
    }
    q[2*db]=pack8(acc,0,QSCALE); q[2*db+1]=pack8(acc,8,QSCALE);
  }
  const u16* kb = kc + (size_t)(b*NH+head)*SK*DH + (size_t)r*DH + 8*h;
  const u16* vb = vc + (size_t)(b*NH+head)*SK*DH + r + (size_t)(4*h)*DH;
  float m=-INFINITY;
  for(int j=0;j<16;++j){
    f32x16 s={};
    #pragma unroll
    for(int d=0;d<8;++d) s=MFMA32(*(const bf16x8*)(kb+(size_t)j*32*DH+16*d), q[d], s);
    #pragma unroll
    for(int i=0;i<16;++i) m=fmaxf(m,s[i]);
  }
  m=fmaxf(m,swap32(m));
  f32x16 o[4]={}; float l=0;
  for(int j=0;j<16;++j){
    f32x16 s={};
    #pragma unroll
    for(int d=0;d<8;++d) s=MFMA32(*(const bf16x8*)(kb+(size_t)j*32*DH+16*d), q[d], s);
    #pragma unroll
    for(int i=0;i<16;++i){ s[i]=__builtin_amdgcn_exp2f(s[i]-m); l+=s[i]; }
    #pragma unroll
    for(int ks=0;ks<2;++ks){
      bf16x8 p=pack8(s,8*ks,1.f);
      #pragma unroll
      for(int db=0;db<4;++db){
        bf16x8 a;
        #pragma unroll
        for(int e=0;e<8;++e) a[e]=(short)vb[(size_t)(j*32+16*ks+8*(e>>2)+(e&3))*DH+32*db];
        o[db]=MFMA32(a,p,o[db]);
      }
    }
  }
  l+=swap32(l);
  const float il=1.f/l;
  if(valid){
    u16* op = ows + (size_t)(G*GS+(head>>1)*HPS+(head&1)*8+(r>>4))*512 + ((r&15)+16*h)*8;
    #pragma unroll
    for(int db=0;db<4;++db)
      #pragma unroll
      for(int i4=0;i4<4;++i4){
        u32x2 v={cvtpk(o[db][4*i4]*il,o[db][4*i4+1]*il),cvtpk(o[db][4*i4+2]*il,o[db][4*i4+3]*il)};
        *(u32x2*)(op + db*1024 + (i4&1)*256 + (i4>>1)*4)=v;
      }
  }
}
typedef unsigned long long u64;
__global__ __launch_bounds__(512,2) __attribute__((aligned(4096))) void gqa_fused(const u16* x,const u16* kc,const u16* vc,const u16* wq,const u16* wo,u16* out,u16* ows,char* flags){
  const int bid=blockIdx.x, g=bid&7, i=bid>>3;
  const int P=g>>1, sd=g&1;
  const int b=4*P+(i>>3), hp=((i&6)<<1)|(sd<<1)|(i&1);
  const int w=__builtin_amdgcn_readfirstlane(threadIdx.x>>6);
  const int G0=(15*b)/2;
  const u16* xp = x + (size_t)G0*32*DM;
  const u16* wp = wq + hp*256;
  const u16* kp = kc + (size_t)(b*NH+2*hp)*SK*DH;
  const u16* vp = vc + (size_t)(b*NH+2*hp)*SK*DH;
  u16* op = ows + (size_t)((G0+w)*GS+hp*HPS)*512;
  const int rowlo=(b&1)?16:0, rowhi=(b&1)?256:240;
  const int gq=2*P+(i>>4), rt=(i>>2)&3, nt=2*(i&3)+sd, qb=w&3, kh=w>>2;
  const int Gl=4*rt+qb, Glc=Gl<15?Gl:14;
  const u16* wop = wo + (nt&~1)*64;
  const u16* ofr = ows + (size_t)((15*gq+Glc)*GS + kh*HPS)*512;
  u16* outp = out + (size_t)(15*gq+Glc)*32*DM + nt*64 + kh*32;
  const unsigned onrec = Gl<15 ? 524288u : 0u;
  const unsigned myfl = P*32768 + ((b>>1)&1)*8192 + (((b&1)?7:0)+w)*256 + hp*16;
  const bool des = (Gl==15 && i==12 && sd==0 && kh==0);
  const unsigned pollfl = P*32768 + (i>>4)*8192 + (des?7:Glc)*256;
  const unsigned bumpo = des ? (unsigned)(P*32768+24576) : 0x40000000u;
  const u16* wopf = wo + bid*8192 + w*1024;
  const unsigned wgi = P*32768 + 16384 + ((i*2+sd)*8+w)*16;
  char* pp = (char*)ows + (size_t)((Gl<15 ? 15*gq+Gl : 120+gq)*GS + 272)*1024 + ((nt>>1)*2+kh)*5120;
  int flag; u64 tag; char* fl1=flags; char* fl2=flags;
  asm volatile(
    "s_setprio 0\n\t"
    "s_waitcnt vmcnt(0) lgkmcnt(0)\n\t"
    "s_mov_b32 s66, %[w]\n\t"
    "s_lshl_b32 s61, s66, 11\n\t"
    "s_and_b32 s41, s41, 0xffff\n\t"
    "s_mov_b32 s42, 4186624\n\t"
    "s_mov_b32 s43, 0x20000\n\t"
    "v_mbcnt_lo_u32_b32 v255, -1, 0\n\t"
    "v_mbcnt_hi_u32_b32 v255, -1, v255\n\t"
    "v_bfe_u32 v253, v255, 4, 1\n\t"
    "v_lshlrev_b32 v253, 2, v253\n\t"
    "v_and_b32 v254, 7, v255\n\t"
    "v_xor_b32 v253, v253, v254\n\t"
    "v_lshlrev_b32 v253, 4, v253\n\t"
    "v_lshrrev_b32 v254, 3, v255\n\t"
    "s_lshl_b32 s65, s66, 3\n\t"
    "v_add_u32 v232, s65, v254\n\t"
    "s_and_b32 s65, s66, 1\n\t"
    "s_lshl_b32 s65, s65, 5\n\t"
    "v_xor_b32 v216, s65, v253\n\t"
    "v_lshl_add_u32 v216, v232, 13, v216\n\t"
    "s_add_u32 m0, s61, 131072\n\t"
    "s_mov_b32 s63, 256\n\t"
    "buffer_load_dwordx4 v216, s[40:43], s63 offen lds\n\t"
    "s_add_u32 m0, s61, 132096\n\t"
    "s_mov_b32 s63, 384\n\t"
    "buffer_load_dwordx4 v216, s[40:43], s63 offen lds\n\t"
    "s_and_b32 s37, s37, 0xffff\n\t"
    "s_mov_b32 s38, 262144\n\t"
    "s_mov_b32 s39, 0x20000\n\t"
    "s_lshl_b32 s65, s66, 5\n\t"
    "s_cmp_ge_u32 s65, %[rowlo]\n\t"
    "s_cselect_b32 s67, 0, 1073741824\n\t"
    "s_sub_u32 s65, s65, %[rowlo]\n\t"
    "s_cmp_lt_i32 s65, 224\n\t"
    "s_cselect_b32 s71, 0, 1073741824\n\t"
    "s_lshl_b32 s62, s66, 12\n\t"
    "v_bfe_u32 v253, v255, 4, 2\n\t"
    "v_and_b32 v232, 7, v255\n\t"
    "v_xor_b32 v253, v253, v232\n\t"
    "v_lshlrev_b32 v253, 4, v253\n\t"
    "s_lshl_b32 s65, s66, 15\n\t"
    "s_add_u32 s65, s65, 0\n\t"
    "s_add_u32 s65, s65, s67\n\t"
    "v_xor_b32 v212, 0, v253\n\t"
    "v_lshl_add_u32 v212, v254, 10, v212\n\t"
    "v_add_u32 v212, s65, v212\n\t"
    "s_add_u32 m0, s62, 32768\n\t"
    "s_mov_b32 s63, 0\n\t"
    "buffer_load_dwordx4 v212, s[36:39], s63 offen lds\n\t"
    "s_lshl_b32 s65, s66, 15\n\t"
    "s_add_u32 s65, s65, 8192\n\t"
    "s_add_u32 s65, s65, s67\n\t"
    "v_xor_b32 v213, 64, v253\n\t"
    "v_lshl_add_u32 v213, v254, 10, v213\n\t"
    "v_add_u32 v213, s65, v213\n\t"
    "s_add_u32 m0, s62, 33792\n\t"
    "s_mov_b32 s63, 0\n\t"
    "buffer_load_dwordx4 v213, s[36:39], s63 offen lds\n\t"
    "s_lshl_b32 s65, s66, 15\n\t"
    "s_add_u32 s65, s65, 16384\n\t"
    "s_add_u32 s65, s65, s71\n\t"
    "v_xor_b32 v214, 0, v253\n\t"
    "v_lshl_add_u32 v214, v254, 10, v214\n\t"
    "v_add_u32 v214, s65, v214\n\t"
    "s_add_u32 m0, s62, 34816\n\t"
    "s_mov_b32 s63, 0\n\t"
    "buffer_load_dwordx4 v214, s[36:39], s63 offen lds\n\t"
    "s_lshl_b32 s65, s66, 15\n\t"
    "s_add_u32 s65, s65, 24576\n\t"
    "s_add_u32 s65, s65, s71\n\t"
    "v_xor_b32 v215, 64, v253\n\t"
    "v_lshl_add_u32 v215, v254, 10, v215\n\t"
    "v_add_u32 v215, s65, v215\n\t"
    "s_add_u32 m0, s62, 35840\n\t"
    "s_mov_b32 s63, 0\n\t"
    "buffer_load_dwordx4 v215, s[36:39], s63 offen lds\n\t"
    "s_and_b32 s65, %[wgi], 0xffff8000\n\t"
    "s_add_u32 s65, s65, 24576\n\t"
    "s_load_dwordx2 s[72:73], s[56:57], s65\n\t"
    "s_and_b32 s45, s45, 0xffff\n\t"
    "s_mov_b32 s46, 262144\n\t"
    "s_mov_b32 s47, 0x20000\n\t"
    "s_and_b32 s49, s49, 0xffff\n\t"
    "s_mov_b32 s50, 262144\n\t"
    "s_mov_b32 s51, 0x20000\n\t"
    "s_and_b32 s53, s53, 0xffff\n\t"
    "s_mov_b32 s54, 262144\n\t"
    "s_mov_b32 s55, 0x20000\n\t"
    "s_and_b32 s57, s57, 0xffff\n\t"
    "s_mov_b32 s58, 131072\n\t"
    "s_mov_b32 s59, 0x20000\n\t"
    "s_lshl_b32 s60, s66, 10\n\t"
    "s_mov_b32 s64, 0\n\t"
    "s_and_b32 s65, s66, 2\n\t"
    "s_lshl_b32 s65, s65, 5\n\t"
    "v_xor_b32 v242, s65, v253\n\t"
    "v_lshl_add_u32 v242, v254, 8, v242\n\t"
    "v_bfe_u32 v253, v255, 4, 1\n\t"
    "v_lshlrev_b32 v253, 2, v253\n\t"
    "v_bfe_u32 v232, v255, 5, 1\n\t"
    "v_lshl_add_u32 v253, v232, 1, v253\n\t"
    "v_and_b32 v232, 7, v255\n\t"
    "v_xor_b32 v253, v253, v232\n\t"
    "v_lshlrev_b32 v253, 4, v253\n\t"
    "v_lshl_add_u32 v243, v254, 8, v253\n\t"
    "s_lshr_b32 s65, s66, 1\n\t"
    "s_lshl_b32 s65, s65, 11\n\t"
    "s_and_b32 s63, s66, 1\n\t"
    "s_lshl_b32 s63, s63, 7\n\t"
    "s_add_u32 s65, s65, s63\n\t"
    "v_add_u32 v242, s65, v242\n\t"
    "v_add_u32 v243, s65, v243\n\t"
    "v_bfe_u32 v253, v255, 1, 3\n\t"
    "v_lshrrev_b32 v254, 4, v255\n\t"
    "v_xor_b32 v253, v253, v254\n\t"
    "v_lshlrev_b32 v253, 4, v253\n\t"
    "v_and_b32 v254, 7, v255\n\t"
    "v_lshl_add_u32 v253, v254, 7, v253\n\t"
    "v_bfe_u32 v254, v255, 3, 1\n\t"
    "v_xor_b32 v232, 0, v253\n\t"
    "v_lshl_add_u32 v208, v254, 10, v232\n\t"
    "v_add_u32 v208, s62, v208\n\t"
    "v_lshl_add_u32 v240, v254, 11, v232\n\t"
    "v_add_u32 v240, 98304, v240\n\t"
    "v_xor_b32 v232, 64, v253\n\t"
    "v_lshl_add_u32 v209, v254, 10, v232\n\t"
    "v_add_u32 v209, s62, v209\n\t"
    "v_lshl_add_u32 v241, v254, 11, v232\n\t"
    "v_add_u32 v241, 98304, v241\n\t"
    "s_add_u32 m0, s61, 147456\n\t"
    "s_mov_b32 s63, 0\n\t"
    "buffer_load_dwordx4 v216, s[40:43], s63 offen lds\n\t"
    "s_add_u32 m0, s61, 148480\n\t"
    "s_mov_b32 s63, 128\n\t"
    "buffer_load_dwordx4 v216, s[40:43], s63 offen lds\n\t"
    "v_bfe_u32 v253, v255, 1, 1\n\t"
    "v_bfe_u32 v254, v255, 3, 1\n\t"
    "v_lshl_add_u32 v253, v254, 2, v253\n\t"
    "v_bfe_u32 v254, v255, 4, 1\n\t"
    "v_lshl_add_u32 v253, v254, 1, v253\n\t"
    "v_lshlrev_b32 v253, 4, v253\n\t"
    "v_bfe_u32 v254, v255, 2, 2\n\t"
    "v_lshlrev_b32 v254, 7, v254\n\t"
    "v_and_b32 v232, 1, v255\n\t"
    "v_lshl_add_u32 v254, v232, 3, v254\n\t"
    "v_bfe_u32 v232, v255, 4, 2\n\t"
    "v_lshl_add_u32 v254, v232, 11, v254\n\t"
    "v_xor_b32 v200, 0, v253\n\t"
    "s_mov_b32 s65, 81920\n\t"
    "v_add3_u32 v200, v200, v254, s65\n\t"
    "v_xor_b32 v201, 32, v253\n\t"
    "s_mov_b32 s65, 81920\n\t"
    "v_add3_u32 v201, v201, v254, s65\n\t"
    "v_xor_b32 v202, 64, v253\n\t"
    "s_mov_b32 s65, 81920\n\t"
    "v_add3_u32 v202, v202, v254, s65\n\t"
    "v_xor_b32 v203, 96, v253\n\t"
    "s_mov_b32 s65, 81920\n\t"
    "v_add3_u32 v203, v203, v254, s65\n\t"
    "v_xor_b32 v204, 0, v253\n\t"
    "s_mov_b32 s65, 131072\n\t"
    "v_add3_u32 v204, v204, v254, s65\n\t"
    "v_xor_b32 v205, 32, v253\n\t"
    "s_mov_b32 s65, 131072\n\t"
    "v_add3_u32 v205, v205, v254, s65\n\t"
    "v_xor_b32 v206, 64, v253\n\t"
    "s_mov_b32 s65, 131072\n\t"
    "v_add3_u32 v206, v206, v254, s65\n\t"
    "v_xor_b32 v207, 96, v253\n\t"
    "s_mov_b32 s65, 131072\n\t"
    "v_add3_u32 v207, v207, v254, s65\n\t"
    "s_add_u32 m0, s61, 81920\n\t"
    "s_mov_b32 s63, 524544\n\t"
    "buffer_load_dwordx4 v216, s[40:43], s63 offen lds\n\t"
    "s_add_u32 m0, s61, 82944\n\t"
    "s_mov_b32 s63, 524672\n\t"
    "buffer_load_dwordx4 v216, s[40:43], s63 offen lds\n\t"
    "s_add_u32 m0, s62, 0\n\t"
    "s_mov_b32 s63, 128\n\t"
    "buffer_load_dwordx4 v212, s[36:39], s63 offen lds\n\t"
    "s_add_u32 m0, s62, 1024\n\t"
    "s_mov_b32 s63, 128\n\t"
    "buffer_load_dwordx4 v213, s[36:39], s63 offen lds\n\t"
    "s_add_u32 m0, s62, 2048\n\t"
    "s_mov_b32 s63, 128\n\t"
    "buffer_load_dwordx4 v214, s[36:39], s63 offen lds\n\t"
    "s_add_u32 m0, s62, 3072\n\t"
    "s_mov_b32 s63, 128\n\t"
    "buffer_load_dwordx4 v215, s[36:39], s63 offen lds\n\t"
    "v_bfe_u32 v253, v255, 1, 1\n\t"
    "v_bfe_u32 v254, v255, 3, 1\n\t"
    "v_lshl_add_u32 v253, v254, 2, v253\n\t"
    "v_bfe_u32 v254, v255, 4, 1\n\t"
    "v_lshl_add_u32 v253, v254, 1, v253\n\t"
    "v_lshlrev_b32 v253, 4, v253\n\t"
    "v_bfe_u32 v254, v255, 2, 2\n\t"
    "v_lshlrev_b32 v254, 7, v254\n\t"
    "v_and_b32 v232, 1, v255\n\t"
    "v_lshl_add_u32 v254, v232, 3, v254\n\t"
    "v_bfe_u32 v232, v255, 5, 1\n\t"
    "v_lshl_add_u32 v254, v232, 11, v254\n\t"
    "v_bfe_u32 v232, v255, 4, 1\n\t"
    "v_lshl_add_u32 v254, v232, 9, v254\n\t"
    "v_xor_b32 v244, 0, v253\n\t"
    "s_mov_b32 s65, 32768\n\t"
    "v_add3_u32 v244, v244, v254, s65\n\t"
    "v_xor_b32 v245, 32, v253\n\t"
    "s_mov_b32 s65, 32768\n\t"
    "v_add3_u32 v245, v245, v254, s65\n\t"
    "v_xor_b32 v246, 64, v253\n\t"
    "s_mov_b32 s65, 32768\n\t"
    "v_add3_u32 v246, v246, v254, s65\n\t"
    "v_xor_b32 v247, 96, v253\n\t"
    "s_mov_b32 s65, 32768\n\t"
    "v_add3_u32 v247, v247, v254, s65\n\t"
    "v_lshlrev_b32 v252, 4, v255\n\t"
    "v_mov_b32 v248, 0\n\t"
    "v_mov_b32 v249, 0\n\t"
    "v_mov_b32 v250, 0\n\t"
    "v_mov_b32 v251, 0\n\t"
    "s_mov_b64 exec, 0\n\t"
    "s_cmp_eq_u32 s66, 0\n\t"
    "s_cbranch_scc1 PAD0_%=\n\t"
    "s_cmp_eq_u32 s66, 1\n\t"
    "s_cbranch_scc1 PAD1_%=\n\t"
    "s_cmp_eq_u32 s66, 2\n\t"
    "s_cbranch_scc1 PAD2_%=\n\t"
    "s_cmp_eq_u32 s66, 3\n\t"
    "s_cbranch_scc1 PAD3_%=\n\t"
    "s_cmp_eq_u32 s66, 4\n\t"
    "s_cbranch_scc1 PAD4_%=\n\t"
    "s_cmp_eq_u32 s66, 5\n\t"
    "s_cbranch_scc1 PAD5_%=\n\t"
    "s_cmp_eq_u32 s66, 6\n\t"
    "s_cbranch_scc1 PAD6_%=\n\t"
    "s_cmp_eq_u32 s66, 7\n\t"
    "s_cbranch_scc1 PAD7_%=\n\t"
    "PADRET_%=:\n\t"
    "s_mov_b64 exec, -1\n\t"
    "s_nop 1\n\t"
    "s_waitcnt vmcnt(8)\n\t"
    "ds_read_b128 v[168:171], v208 offset:32768\n\t"
    "ds_read_b128 v[172:175], v208 offset:34816\n\t"
    "ds_read_b128 v[176:179], v209 offset:32768\n\t"
    "ds_read_b128 v[180:183], v209 offset:34816\n\t"
    "s_waitcnt lgkmcnt(0)\n\t"
    "s_barrier\n\t"
    "s_setprio 3\n\t"
    "ds_read_b64_tr_b16 v[128:129], v204 offset:0\n\t"
    "ds_read_b64_tr_b16 v[130:131], v204 offset:512\n\t"
    "ds_read_b64_tr_b16 v[132:133], v204 offset:8192\n\t"
    "ds_read_b64_tr_b16 v[134:135], v204 offset:8704\n\t"
    "ds_read_b64_tr_b16 v[136:137], v205 offset:0\n\t"
    "ds_read_b64_tr_b16 v[138:139], v205 offset:512\n\t"
    "ds_read_b64_tr_b16 v[140:141], v205 offset:8192\n\t"
    "ds_read_b64_tr_b16 v[142:143], v205 offset:8704\n\t"
    "ds_read_b64_tr_b16 v[144:145], v206 offset:0\n\t"
    "ds_read_b64_tr_b16 v[146:147], v206 offset:512\n\t"
    "ds_read_b64_tr_b16 v[148:149], v206 offset:8192\n\t"
    "ds_read_b64_tr_b16 v[150:151], v206 offset:8704\n\t"
    "ds_read_b64_tr_b16 v[152:153], v207 offset:0\n\t"
    "ds_read_b64_tr_b16 v[154:155], v207 offset:512\n\t"
    "ds_read_b64_tr_b16 v[156:157], v207 offset:8192\n\t"
    "ds_read_b64_tr_b16 v[158:159], v207 offset:8704\n\t"
    "s_waitcnt lgkmcnt(12)\n\t"
    "v_mfma_f32_16x16x32_bf16 v[0:3], v[128:131], v[168:171], 0\n\t"
    "ds_read_b64_tr_b16 v[160:161], v204 offset:1024\n\t"
    "v_mfma_f32_16x16x32_bf16 v[8:11], v[128:131], v[172:175], 0\n\t"
    "ds_read_b64_tr_b16 v[162:163], v204 offset:1536\n\t"
    "s_add_u32 m0, s61, 98304\n\t"
    "s_mov_b32 s63, 524288\n\t"
    "buffer_load_dwordx4 v216, s[40:43], s63 offen lds\n\t"
    "v_mfma_f32_16x16x32_bf16 v[0:3], v[132:135], v[176:179], v[0:3]\n\t"
    "ds_read_b64_tr_b16 v[164:165], v204 offset:9216\n\t"
    "v_mfma_f32_16x16x32_bf16 v[8:11], v[132:135], v[180:183], v[8:11]\n\t"
    "ds_read_b64_tr_b16 v[166:167], v204 offset:9728\n\t"
    "s_add_u32 m0, s61, 99328\n\t"
    "s_mov_b32 s63, 524416\n\t"
    "buffer_load_dwordx4 v216, s[40:43], s63 offen lds\n\t"
    "s_waitcnt lgkmcnt(12)\n\t"
    "v_mfma_f32_16x16x32_bf16 v[4:7], v[136:139], v[168:171], 0\n\t"
    "ds_read_b64_tr_b16 v[128:129], v205 offset:1024\n\t"
    "v_mfma_f32_16x16x32_bf16 v[12:15], v[136:139], v[172:175], 0\n\t"
    "ds_read_b64_tr_b16 v[130:131], v205 offset:1536\n\t"
    "s_add_u32 m0, s62, 32768\n\t"
    "s_mov_b32 s63, 256\n\t"
    "buffer_load_dwordx4 v212, s[36:39], s63 offen lds\n\t"
    "v_mfma_f32_16x16x32_bf16 v[4:7], v[140:143], v[176:179], v[4:7]\n\t"
    "ds_read_b64_tr_b16 v[132:133], v205 offset:9216\n\t"
    "v_mfma_f32_16x16x32_bf16 v[12:15], v[140:143], v[180:183], v[12:15]\n\t"
    "ds_read_b64_tr_b16 v[134:135], v205 offset:9728\n\t"
    "s_add_u32 m0, s62, 33792\n\t"
    "s_mov_b32 s63, 256\n\t"
    "buffer_load_dwordx4 v213, s[36:39], s63 offen lds\n\t"
    "s_waitcnt lgkmcnt(12)\n\t"
    "v_mfma_f32_16x16x32_bf16 v[16:19], v[144:147], v[168:171], 0\n\t"
    "ds_read_b64_tr_b16 v[136:137], v206 offset:1024\n\t"
    "v_mfma_f32_16x16x32_bf16 v[24:27], v[144:147], v[172:175], 0\n\t"
    "ds_read_b64_tr_b16 v[138:139], v206 offset:1536\n\t"
    "s_add_u32 m0, s62, 34816\n\t"
    "s_mov_b32 s63, 256\n\t"
    "buffer_load_dwordx4 v214, s[36:39], s63 offen lds\n\t"
    "v_mfma_f32_16x16x32_bf16 v[16:19], v[148:151], v[176:179], v[16:19]\n\t"
    "ds_read_b64_tr_b16 v[140:141], v206 offset:9216\n\t"
    "v_mfma_f32_16x16x32_bf16 v[24:27], v[148:151], v[180:183], v[24:27]\n\t"
    "ds_read_b64_tr_b16 v[142:143], v206 offset:9728\n\t"
    "s_add_u32 m0, s62, 35840\n\t"
    "s_mov_b32 s63, 256\n\t"
    "buffer_load_dwordx4 v215, s[36:39], s63 offen lds\n\t"
    "s_waitcnt lgkmcnt(12)\n\t"
    "v_mfma_f32_16x16x32_bf16 v[20:23], v[152:155], v[168:171], 0\n\t"
    "ds_read_b64_tr_b16 v[144:145], v207 offset:1024\n\t"
    "s_setprio 2\n\t"
    "v_mfma_f32_16x16x32_bf16 v[28:31], v[152:155], v[172:175], 0\n\t"
    "ds_read_b64_tr_b16 v[146:147], v207 offset:1536\n\t"
    "v_mfma_f32_16x16x32_bf16 v[20:23], v[156:159], v[176:179], v[20:23]\n\t"
    "ds_read_b64_tr_b16 v[148:149], v207 offset:9216\n\t"
    "v_mfma_f32_16x16x32_bf16 v[28:31], v[156:159], v[180:183], v[28:31]\n\t"
    "ds_read_b64_tr_b16 v[150:151], v207 offset:9728\n\t"
    "s_waitcnt lgkmcnt(12)\n\t"
    "v_mfma_f32_16x16x32_bf16 v[32:35], v[160:163], v[168:171], 0\n\t"
    "s_add_u32 s72, s72, 0x7F4A7C15\n\t"
    "s_addc_u32 s73, s73, 0x9E3779B9\n\t"
    "v_mfma_f32_16x16x32_bf16 v[40:43], v[160:163], v[172:175], 0\n\t"
    "v_mfma_f32_16x16x32_bf16 v[32:35], v[164:167], v[176:179], v[32:35]\n\t"
    "v_mfma_f32_16x16x32_bf16 v[40:43], v[164:167], v[180:183], v[40:43]\n\t"
    "s_waitcnt vmcnt(12) lgkmcnt(0)\n\t"
    "s_barrier\n\t"
    "s_setprio 3\n\t"
    "ds_read_b64_tr_b16 v[152:153], v204 offset:16384\n\t"
    "ds_read_b64_tr_b16 v[154:155], v204 offset:16896\n\t"
    "ds_read_b64_tr_b16 v[156:157], v204 offset:24576\n\t"
    "ds_read_b64_tr_b16 v[158:159], v204 offset:25088\n\t"
    "v_mfma_f32_16x16x32_bf16 v[36:39], v[128:131], v[168:171], 0\n\t"
    "ds_read_b64_tr_b16 v[160:161], v205 offset:16384\n\t"
    "s_setprio 1\n\t"
    "v_mfma_f32_16x16x32_bf16 v[44:47], v[128:131], v[172:175], 0\n\t"
    "ds_read_b64_tr_b16 v[162:163], v205 offset:16896\n\t"
    "v_mfma_f32_16x16x32_bf16 v[36:39], v[132:135], v[176:179], v[36:39]\n\t"
    "ds_read_b64_tr_b16 v[164:165], v205 offset:24576\n\t"
    "v_mfma_f32_16x16x32_bf16 v[44:47], v[132:135], v[180:183], v[44:47]\n\t"
    "ds_read_b64_tr_b16 v[166:167], v205 offset:25088\n\t"
    "v_mfma_f32_16x16x32_bf16 v[48:51], v[136:139], v[168:171], 0\n\t"
    "ds_read_b64_tr_b16 v[128:129], v206 offset:16384\n\t"
    "v_mfma_f32_16x16x32_bf16 v[56:59], v[136:139], v[172:175], 0\n\t"
    "ds_read_b64_tr_b16 v[130:131], v206 offset:16896\n\t"
    "v_mfma_f32_16x16x32_bf16 v[48:51], v[140:143], v[176:179], v[48:51]\n\t"
    "ds_read_b64_tr_b16 v[132:133], v206 offset:24576\n\t"
    "s_setprio 0\n\t"
    "v_mfma_f32_16x16x32_bf16 v[56:59], v[140:143], v[180:183], v[56:59]\n\t"
    "ds_read_b64_tr_b16 v[134:135], v206 offset:25088\n\t"
    "v_mfma_f32_16x16x32_bf16 v[52:55], v[144:147], v[168:171], 0\n\t"
    "ds_read_b64_tr_b16 v[136:137], v207 offset:16384\n\t"
    "v_mfma_f32_16x16x32_bf16 v[60:63], v[144:147], v[172:175], 0\n\t"
    "ds_read_b64_tr_b16 v[138:139], v207 offset:16896\n\t"
    "s_add_u32 m0, s61, 131072\n\t"
    "s_mov_b32 s63, 1048832\n\t"
    "buffer_load_dwordx4 v216, s[40:43], s63 offen lds\n\t"
    "v_mfma_f32_16x16x32_bf16 v[52:55], v[148:151], v[176:179], v[52:55]\n\t"
    "ds_read_b64_tr_b16 v[140:141], v207 offset:24576\n\t"
    "v_mfma_f32_16x16x32_bf16 v[60:63], v[148:151], v[180:183], v[60:63]\n\t"
    "ds_read_b64_tr_b16 v[142:143], v207 offset:25088\n\t"
    "s_waitcnt lgkmcnt(12)\n\t"
    "v_mfma_f32_16x16x32_bf16 v[64:67], v[152:155], v[168:171], 0\n\t"
    "ds_read_b64_tr_b16 v[144:145], v204 offset:17408\n\t"
    "s_setprio 2\n\t"
    "v_mfma_f32_16x16x32_bf16 v[72:75], v[152:155], v[172:175], 0\n\t"
    "ds_read_b64_tr_b16 v[146:147], v204 offset:17920\n\t"
    "v_mfma_f32_16x16x32_bf16 v[64:67], v[156:159], v[176:179], v[64:67]\n\t"
    "ds_read_b64_tr_b16 v[148:149], v204 offset:25600\n\t"
    "v_mfma_f32_16x16x32_bf16 v[72:75], v[156:159], v[180:183], v[72:75]\n\t"
    "ds_read_b64_tr_b16 v[150:151], v204 offset:26112\n\t"
    "s_waitcnt lgkmcnt(12)\n\t"
    "v_mfma_f32_16x16x32_bf16 v[68:71], v[160:163], v[168:171], 0\n\t"
    "ds_read_b64_tr_b16 v[152:153], v205 offset:17408\n\t"
    "s_waitcnt vmcnt(7)\n\t"
    "ds_read_b128 v[184:187], v208 offset:0\n\t"
    "v_mfma_f32_16x16x32_bf16 v[76:79], v[160:163], v[172:175], 0\n\t"
    "ds_read_b64_tr_b16 v[154:155], v205 offset:17920\n\t"
    "ds_read_b128 v[188:191], v208 offset:2048\n\t"
    "v_mfma_f32_16x16x32_bf16 v[68:71], v[164:167], v[176:179], v[68:71]\n\t"
    "ds_read_b64_tr_b16 v[156:157], v205 offset:25600\n\t"
    "v_mfma_f32_16x16x32_bf16 v[76:79], v[164:167], v[180:183], v[76:79]\n\t"
    "ds_read_b64_tr_b16 v[158:159], v205 offset:26112\n\t"
    "s_add_u32 m0, s61, 132096\n\t"
    "s_mov_b32 s63, 1048960\n\t"
    "buffer_load_dwordx4 v216, s[40:43], s63 offen lds\n\t"
    "s_waitcnt lgkmcnt(14)\n\t"
    "v_mfma_f32_16x16x32_bf16 v[80:83], v[128:131], v[168:171], 0\n\t"
    "ds_read_b64_tr_b16 v[160:161], v206 offset:17408\n\t"
    "s_setprio 1\n\t"
    "ds_read_b128 v[192:195], v209 offset:0\n\t"
    "v_mfma_f32_16x16x32_bf16 v[88:91], v[128:131], v[172:175], 0\n\t"
    "ds_read_b64_tr_b16 v[162:163], v206 offset:17920\n\t"
    "ds_read_b128 v[196:199], v209 offset:2048\n\t"
    "v_mfma_f32_16x16x32_bf16 v[80:83], v[132:135], v[176:179], v[80:83]\n\t"
    "ds_read_b64_tr_b16 v[164:165], v206 offset:25600\n\t"
    "v_mfma_f32_16x16x32_bf16 v[88:91], v[132:135], v[180:183], v[88:91]\n\t"
    "ds_read_b64_tr_b16 v[166:167], v206 offset:26112\n\t"
    "s_waitcnt lgkmcnt(15)\n\t"
    "v_mfma_f32_16x16x32_bf16 v[84:87], v[136:139], v[168:171], 0\n\t"
    "ds_read_b64_tr_b16 v[128:129], v207 offset:17408\n\t"
    "v_mfma_f32_16x16x32_bf16 v[92:95], v[136:139], v[172:175], 0\n\t"
    "ds_read_b64_tr_b16 v[130:131], v207 offset:17920\n\t"
    "v_mfma_f32_16x16x32_bf16 v[84:87], v[140:143], v[176:179], v[84:87]\n\t"
    "ds_read_b64_tr_b16 v[132:133], v207 offset:25600\n\t"
    "s_setprio 0\n\t"
    "v_mfma_f32_16x16x32_bf16 v[92:95], v[140:143], v[180:183], v[92:95]\n\t"
    "ds_read_b64_tr_b16 v[134:135], v207 offset:26112\n\t"
    "s_waitcnt lgkmcnt(15)\n\t"
    "v_mfma_f32_16x16x32_bf16 v[96:99], v[144:147], v[168:171], 0\n\t"
    "v_mfma_f32_16x16x32_bf16 v[104:107], v[144:147], v[172:175], 0\n\t"
    "v_mfma_f32_16x16x32_bf16 v[96:99], v[148:151], v[176:179], v[96:99]\n\t"
    "v_mfma_f32_16x16x32_bf16 v[104:107], v[148:151], v[180:183], v[104:107]\n\t"
    "s_waitcnt lgkmcnt(0)\n\t"
    "s_barrier\n\t"
    "s_setprio 3\n\t"
    "ds_read_b64_tr_b16 v[136:137], v200 offset:0\n\t"
    "ds_read_b64_tr_b16 v[138:139], v200 offset:512\n\t"
    "ds_read_b64_tr_b16 v[140:141], v200 offset:8192\n\t"
    "ds_read_b64_tr_b16 v[142:143], v200 offset:8704\n\t"
    "v_mfma_f32_16x16x32_bf16 v[100:103], v[152:155], v[168:171], 0\n\t"
    "ds_read_b64_tr_b16 v[144:145], v201 offset:0\n\t"
    "v_mfma_f32_16x16x32_bf16 v[108:111], v[152:155], v[172:175], 0\n\t"
    "ds_read_b64_tr_b16 v[146:147], v201 offset:512\n\t"
    "s_add_u32 m0, s61, 147456\n\t"
    "s_mov_b32 s63, 1048576\n\t"
    "buffer_load_dwordx4 v216, s[40:43], s63 offen lds\n\t"
    "v_mfma_f32_16x16x32_bf16 v[100:103], v[156:159], v[176:179], v[100:103]\n\t"
    "ds_read_b64_tr_b16 v[148:149], v201 offset:8192\n\t"
    "v_mfma_f32_16x16x32_bf16 v[108:111], v[156:159], v[180:183], v[108:111]\n\t"
    "ds_read_b64_tr_b16 v[150:151], v201 offset:8704\n\t"
    "s_add_u32 m0, s61, 148480\n\t"
    "s_mov_b32 s63, 1048704\n\t"
    "buffer_load_dwordx4 v216, s[40:43], s63 offen lds\n\t"
    "v_mfma_f32_16x16x32_bf16 v[112:115], v[160:163], v[168:171], 0\n\t"
    "ds_read_b64_tr_b16 v[152:153], v202 offset:0\n\t"
    "v_mfma_f32_16x16x32_bf16 v[120:123], v[160:163], v[172:175], 0\n\t"
    "ds_read_b64_tr_b16 v[154:155], v202 offset:512\n\t"
    "s_add_u32 m0, s62, 0\n\t"
    "s_mov_b32 s63, 384\n\t"
    "buffer_load_dwordx4 v212, s[36:39], s63 offen lds\n\t"
    "v_mfma_f32_16x16x32_bf16 v[112:115], v[164:167], v[176:179], v[112:115]\n\t"
    "ds_read_b64_tr_b16 v[156:157], v202 offset:8192\n\t"
    "v_mfma_f32_16x16x32_bf16 v[120:123], v[164:167], v[180:183], v[120:123]\n\t"
    "ds_read_b64_tr_b16 v[158:159], v202 offset:8704\n\t"
    "s_add_u32 m0, s62, 1024\n\t"
    "s_mov_b32 s63, 384\n\t"
    "buffer_load_dwordx4 v213, s[36:39], s63 offen lds\n\t"
    "v_mfma_f32_16x16x32_bf16 v[116:119], v[128:131], v[168:171], 0\n\t"
    "ds_read_b64_tr_b16 v[160:161], v203 offset:0\n\t"
    "v_mfma_f32_16x16x32_bf16 v[124:127], v[128:131], v[172:175], 0\n\t"
    "ds_read_b64_tr_b16 v[162:163], v203 offset:512\n\t"
    "s_add_u32 m0, s62, 2048\n\t"
    "s_mov_b32 s63, 384\n\t"
    "buffer_load_dwordx4 v214, s[36:39], s63 offen lds\n\t"
    "v_mfma_f32_16x16x32_bf16 v[116:119], v[132:135], v[176:179], v[116:119]\n\t"
    "ds_read_b64_tr_b16 v[164:165], v203 offset:8192\n\t"
    "v_mfma_f32_16x16x32_bf16 v[124:127], v[132:135], v[180:183], v[124:127]\n\t"
    "ds_read_b64_tr_b16 v[166:167], v203 offset:8704\n\t"
    "s_add_u32 m0, s62, 3072\n\t"
    "s_mov_b32 s63, 384\n\t"
    "buffer_load_dwordx4 v215, s[36:39], s63 offen lds\n\t"
    "s_waitcnt lgkmcnt(12)\n\t"
    "v_mfma_f32_16x16x32_bf16 v[0:3], v[136:139], v[184:187], v[0:3]\n\t"
    "ds_read_b64_tr_b16 v[128:129], v200 offset:1024\n\t"
    "s_setprio 2\n\t"
    "v_mfma_f32_16x16x32_bf16 v[8:11], v[136:139], v[188:191], v[8:11]\n\t"
    "ds_read_b64_tr_b16 v[130:131], v200 offset:1536\n\t"
    "v_mfma_f32_16x16x32_bf16 v[0:3], v[140:143], v[192:195], v[0:3]\n\t"
    "ds_read_b64_tr_b16 v[132:133], v200 offset:9216\n\t"
    "v_mfma_f32_16x16x32_bf16 v[8:11], v[140:143], v[196:199], v[8:11]\n\t"
    "ds_read_b64_tr_b16 v[134:135], v200 offset:9728\n\t"
    "s_waitcnt lgkmcnt(12)\n\t"
    "v_mfma_f32_16x16x32_bf16 v[4:7], v[144:147], v[184:187], v[4:7]\n\t"
    "ds_read_b64_tr_b16 v[136:137], v201 offset:1024\n\t"
    "v_mfma_f32_16x16x32_bf16 v[12:15], v[144:147], v[188:191], v[12:15]\n\t"
    "ds_read_b64_tr_b16 v[138:139], v201 offset:1536\n\t"
    "v_mfma_f32_16x16x32_bf16 v[4:7], v[148:151], v[192:195], v[4:7]\n\t"
    "ds_read_b64_tr_b16 v[140:141], v201 offset:9216\n\t"
    "v_mfma_f32_16x16x32_bf16 v[12:15], v[148:151], v[196:199], v[12:15]\n\t"
    "ds_read_b64_tr_b16 v[142:143], v201 offset:9728\n\t"
    "s_waitcnt lgkmcnt(12)\n\t"
    "v_mfma_f32_16x16x32_bf16 v[16:19], v[152:155], v[184:187], v[16:19]\n\t"
    "ds_read_b64_tr_b16 v[144:145], v202 offset:1024\n\t"
    "s_setprio 1\n\t"
    "v_mfma_f32_16x16x32_bf16 v[24:27], v[152:155], v[188:191], v[24:27]\n\t"
    "ds_read_b64_tr_b16 v[146:147], v202 offset:1536\n\t"
    "v_mfma_f32_16x16x32_bf16 v[16:19], v[156:159], v[192:195], v[16:19]\n\t"
    "ds_read_b64_tr_b16 v[148:149], v202 offset:9216\n\t"
    "v_mfma_f32_16x16x32_bf16 v[24:27], v[156:159], v[196:199], v[24:27]\n\t"
    "ds_read_b64_tr_b16 v[150:151], v202 offset:9728\n\t"
    "s_waitcnt lgkmcnt(12)\n\t"
    "v_mfma_f32_16x16x32_bf16 v[20:23], v[160:163], v[184:187], v[20:23]\n\t"
    "ds_read_b64_tr_b16 v[152:153], v203 offset:1024\n\t"
    "v_mfma_f32_16x16x32_bf16 v[28:31], v[160:163], v[188:191], v[28:31]\n\t"
    "ds_read_b64_tr_b16 v[154:155], v203 offset:1536\n\t"
    "v_mfma_f32_16x16x32_bf16 v[20:23], v[164:167], v[192:195], v[20:23]\n\t"
    "ds_read_b64_tr_b16 v[156:157], v203 offset:9216\n\t"
    "s_setprio 0\n\t"
    "v_mfma_f32_16x16x32_bf16 v[28:31], v[164:167], v[196:199], v[28:31]\n\t"
    "ds_read_b64_tr_b16 v[158:159], v203 offset:9728\n\t"
    "s_waitcnt lgkmcnt(12)\n\t"
    "v_mfma_f32_16x16x32_bf16 v[32:35], v[128:131], v[184:187], v[32:35]\n\t"
    "v_mfma_f32_16x16x32_bf16 v[40:43], v[128:131], v[188:191], v[40:43]\n\t"
    "v_mfma_f32_16x16x32_bf16 v[32:35], v[132:135], v[192:195], v[32:35]\n\t"
    "PAD0_%=: s_cbranch_execz PAD8_%=\n\t"
    "v_mfma_f32_16x16x32_bf16 v[40:43], v[132:135], v[196:199], v[40:43]\n\t"
    "s_waitcnt vmcnt(12) lgkmcnt(0)\n\t"
    "s_barrier\n\t"
    "s_setprio 3\n\t"
    "ds_read_b64_tr_b16 v[160:161], v200 offset:16384\n\t"
    "ds_read_b64_tr_b16 v[162:163], v200 offset:16896\n\t"
    "ds_read_b64_tr_b16 v[164:165], v200 offset:24576\n\t"
    "ds_read_b64_tr_b16 v[166:167], v200 offset:25088\n\t"
    "v_mfma_f32_16x16x32_bf16 v[36:39], v[136:139], v[184:187], v[36:39]\n\t"
    "ds_read_b64_tr_b16 v[128:129], v201 offset:16384\n\t"
    "v_mfma_f32_16x16x32_bf16 v[44:47], v[136:139], v[188:191], v[44:47]\n\t"
    "ds_read_b64_tr_b16 v[130:131], v201 offset:16896\n\t"
    "v_mfma_f32_16x16x32_bf16 v[36:39], v[140:143], v[192:195], v[36:39]\n\t"
    "ds_read_b64_tr_b16 v[132:133], v201 offset:24576\n\t"
    "v_mfma_f32_16x16x32_bf16 v[44:47], v[140:143], v[196:199], v[44:47]\n\t"
    "ds_read_b64_tr_b16 v[134:135], v201 offset:25088\n\t"
    "v_mfma_f32_16x16x32_bf16 v[48:51], v[144:147], v[184:187], v[48:51]\n\t"
    "ds_read_b64_tr_b16 v[136:137], v202 offset:16384\n\t"
    "v_mfma_f32_16x16x32_bf16 v[56:59], v[144:147], v[188:191], v[56:59]\n\t"
    "ds_read_b64_tr_b16 v[138:139], v202 offset:16896\n\t"
    "v_mfma_f32_16x16x32_bf16 v[48:51], v[148:151], v[192:195], v[48:51]\n\t"
    "ds_read_b64_tr_b16 v[140:141], v202 offset:24576\n\t"
    "v_mfma_f32_16x16x32_bf16 v[56:59], v[148:151], v[196:199], v[56:59]\n\t"
    "ds_read_b64_tr_b16 v[142:143], v202 offset:25088\n\t"
    "v_mfma_f32_16x16x32_bf16 v[52:55], v[152:155], v[184:187], v[52:55]\n\t"
    "ds_read_b64_tr_b16 v[144:145], v203 offset:16384\n\t"
    "v_mfma_f32_16x16x32_bf16 v[60:63], v[152:155], v[188:191], v[60:63]\n\t"
    "ds_read_b64_tr_b16 v[146:147], v203 offset:16896\n\t"
    "s_add_u32 m0, s61, 81920\n\t"
    "s_mov_b32 s63, 1573120\n\t"
    "buffer_load_dwordx4 v216, s[40:43], s63 offen lds\n\t"
    "v_mfma_f32_16x16x32_bf16 v[52:55], v[156:159], v[192:195], v[52:55]\n\t"
    "ds_read_b64_tr_b16 v[148:149], v203 offset:24576\n\t"
    "v_mfma_f32_16x16x32_bf16 v[60:63], v[156:159], v[196:199], v[60:63]\n\t"
    "ds_read_b64_tr_b16 v[150:151], v203 offset:25088\n\t"
    "s_waitcnt lgkmcnt(12)\n\t"
    "v_mfma_f32_16x16x32_bf16 v[64:67], v[160:163], v[184:187], v[64:67]\n\t"
    "ds_read_b64_tr_b16 v[152:153], v200 offset:17408\n\t"
    "s_setprio 2\n\t"
    "v_mfma_f32_16x16x32_bf16 v[72:75], v[160:163], v[188:191], v[72:75]\n\t"
    "ds_read_b64_tr_b16 v[154:155], v200 offset:17920\n\t"
    "v_mfma_f32_16x16x32_bf16 v[64:67], v[164:167], v[192:195], v[64:67]\n\t"
    "ds_read_b64_tr_b16 v[156:157], v200 offset:25600\n\t"
    "v_mfma_f32_16x16x32_bf16 v[72:75], v[164:167], v[196:199], v[72:75]\n\t"
    "ds_read_b64_tr_b16 v[158:159], v200 offset:26112\n\t"
    "s_waitcnt lgkmcnt(12)\n\t"
    "v_mfma_f32_16x16x32_bf16 v[68:71], v[128:131], v[184:187], v[68:71]\n\t"
    "ds_read_b64_tr_b16 v[160:161], v201 offset:17408\n\t"
    "s_waitcnt vmcnt(9)\n\t"
    "ds_read_b128 v[168:171], v208 offset:32768\n\t"
    "v_mfma_f32_16x16x32_bf16 v[76:79], v[128:131], v[188:191], v[76:79]\n\t"
    "ds_read_b64_tr_b16 v[162:163], v201 offset:17920\n\t"
    "ds_read_b128 v[172:175], v208 offset:34816\n\t"
    "v_mfma_f32_16x16x32_bf16 v[68:71], v[132:135], v[192:195], v[68:71]\n\t"
    "ds_read_b64_tr_b16 v[164:165], v201 offset:25600\n\t"
    "v_mfma_f32_16x16x32_bf16 v[76:79], v[132:135], v[196:199], v[76:79]\n\t"
    "ds_read_b64_tr_b16 v[166:167], v201 offset:26112\n\t"
    "s_add_u32 m0, s61, 82944\n\t"
    "s_mov_b32 s63, 1573248\n\t"
    "buffer_load_dwordx4 v216, s[40:43], s63 offen lds\n\t"
    "s_waitcnt lgkmcnt(14)\n\t"
    "v_mfma_f32_16x16x32_bf16 v[80:83], v[136:139], v[184:187], v[80:83]\n\t"
    "ds_read_b64_tr_b16 v[128:129], v202 offset:17408\n\t"
    "s_setprio 1\n\t"
    "ds_read_b128 v[176:179], v209 offset:32768\n\t"
    "v_mfma_f32_16x16x32_bf16 v[88:91], v[136:139], v[188:191], v[88:91]\n\t"
    "ds_read_b64_tr_b16 v[130:131], v202 offset:17920\n\t"
    "ds_read_b128 v[180:183], v209 offset:34816\n\t"
    "v_mfma_f32_16x16x32_bf16 v[80:83], v[140:143], v[192:195], v[80:83]\n\t"
    "ds_read_b64_tr_b16 v[132:133], v202 offset:25600\n\t"
    "v_mfma_f32_16x16x32_bf16 v[88:91], v[140:143], v[196:199], v[88:91]\n\t"
    "ds_read_b64_tr_b16 v[134:135], v202 offset:26112\n\t"
    "s_waitcnt lgkmcnt(15)\n\t"
    "v_mfma_f32_16x16x32_bf16 v[84:87], v[144:147], v[184:187], v[84:87]\n\t"
    "ds_read_b64_tr_b16 v[136:137], v203 offset:17408\n\t"
    "v_mfma_f32_16x16x32_bf16 v[92:95], v[144:147], v[188:191], v[92:95]\n\t"
    "ds_read_b64_tr_b16 v[138:139], v203 offset:17920\n\t"
    "v_mfma_f32_16x16x32_bf16 v[84:87], v[148:151], v[192:195], v[84:87]\n\t"
    "ds_read_b64_tr_b16 v[140:141], v203 offset:25600\n\t"
    "s_setprio 0\n\t"
    "v_mfma_f32_16x16x32_bf16 v[92:95], v[148:151], v[196:199], v[92:95]\n\t"
    "ds_read_b64_tr_b16 v[142:143], v203 offset:26112\n\t"
    "s_waitcnt lgkmcnt(15)\n\t"
    "v_mfma_f32_16x16x32_bf16 v[96:99], v[152:155], v[184:187], v[96:99]\n\t"
    "v_mfma_f32_16x16x32_bf16 v[104:107], v[152:155], v[188:191], v[104:107]\n\t"
    "v_mfma_f32_16x16x32_bf16 v[96:99], v[156:159], v[192:195], v[96:99]\n\t"
    "v_mfma_f32_16x16x32_bf16 v[104:107], v[156:159], v[196:199], v[104:107]\n\t"
    "s_waitcnt vmcnt(8) lgkmcnt(0)\n\t"
    "s_barrier\n\t"
    "s_setprio 3\n\t"
    "ds_read_b64_tr_b16 v[144:145], v204 offset:0\n\t"
    "ds_read_b64_tr_b16 v[146:147], v204 offset:512\n\t"
    "ds_read_b64_tr_b16 v[148:149], v204 offset:8192\n\t"
    "ds_read_b64_tr_b16 v[150:151], v204 offset:8704\n\t"
    "v_mfma_f32_16x16x32_bf16 v[100:103], v[160:163], v[184:187], v[100:103]\n\t"
    "ds_read_b64_tr_b16 v[152:153], v205 offset:0\n\t"
    "v_mfma_f32_16x16x32_bf16 v[108:111], v[160:163], v[188:191], v[108:111]\n\t"
    "ds_read_b64_tr_b16 v[154:155], v205 offset:512\n\t"
    "s_add_u32 m0, s61, 98304\n\t"
    "s_mov_b32 s63, 1572864\n\t"
    "buffer_load_dwordx4 v216, s[40:43], s63 offen lds\n\t"
    "v_mfma_f32_16x16x32_bf16 v[100:103], v[164:167], v[192:195], v[100:103]\n\t"
    "ds_read_b64_tr_b16 v[156:157], v205 offset:8192\n\t"
    "v_mfma_f32_16x16x32_bf16 v[108:111], v[164:167], v[196:199], v[108:111]\n\t"
    "ds_read_b64_tr_b16 v[158:159], v205 offset:8704\n\t"
    "s_add_u32 m0, s61, 99328\n\t"
    "s_mov_b32 s63, 1572992\n\t"
    "buffer_load_dwordx4 v216, s[40:43], s63 offen lds\n\t"
    "v_mfma_f32_16x16x32_bf16 v[112:115], v[128:131], v[184:187], v[112:115]\n\t"
    "ds_read_b64_tr_b16 v[160:161], v206 offset:0\n\t"
    "v_mfma_f32_16x16x32_bf16 v[120:123], v[128:131], v[188:191], v[120:123]\n\t"
    "ds_read_b64_tr_b16 v[162:163], v206 offset:512\n\t"
    "s_add_u32 m0, s62, 32768\n\t"
    "s_mov_b32 s63, 512\n\t"
    "buffer_load_dwordx4 v212, s[36:39], s63 offen lds\n\t"
    "v_mfma_f32_16x16x32_bf16 v[112:115], v[132:135], v[192:195], v[112:115]\n\t"
    "ds_read_b64_tr_b16 v[164:165], v206 offset:8192\n\t"
    "v_mfma_f32_16x16x32_bf16 v[120:123], v[132:135], v[196:199], v[120:123]\n\t"
    "ds_read_b64_tr_b16 v[166:167], v206 offset:8704\n\t"
    "s_add_u32 m0, s62, 33792\n\t"
    "s_mov_b32 s63, 512\n\t"
    "buffer_load_dwordx4 v213, s[36:39], s63 offen lds\n\t"
    "v_mfma_f32_16x16x32_bf16 v[116:119], v[136:139], v[184:187], v[116:119]\n\t"
    "ds_read_b64_tr_b16 v[128:129], v207 offset:0\n\t"
    "v_mfma_f32_16x16x32_bf16 v[124:127], v[136:139], v[188:191], v[124:127]\n\t"
    "ds_read_b64_tr_b16 v[130:131], v207 offset:512\n\t"
    "s_add_u32 m0, s62, 34816\n\t"
    "s_mov_b32 s63, 512\n\t"
    "buffer_load_dwordx4 v214, s[36:39], s63 offen lds\n\t"
    "v_mfma_f32_16x16x32_bf16 v[116:119], v[140:143], v[192:195], v[116:119]\n\t"
    "ds_read_b64_tr_b16 v[132:133], v207 offset:8192\n\t"
    "v_mfma_f32_16x16x32_bf16 v[124:127], v[140:143], v[196:199], v[124:127]\n\t"
    "ds_read_b64_tr_b16 v[134:135], v207 offset:8704\n\t"
    "s_add_u32 m0, s62, 35840\n\t"
    "s_mov_b32 s63, 512\n\t"
    "buffer_load_dwordx4 v215, s[36:39], s63 offen lds\n\t"
    "s_waitcnt lgkmcnt(12)\n\t"
    "v_mfma_f32_16x16x32_bf16 v[0:3], v[144:147], v[168:171], v[0:3]\n\t"
    "ds_read_b64_tr_b16 v[136:137], v204 offset:1024\n\t"
    "s_setprio 2\n\t"
    "v_mfma_f32_16x16x32_bf16 v[8:11], v[144:147], v[172:175], v[8:11]\n\t"
    "ds_read_b64_tr_b16 v[138:139], v204 offset:1536\n\t"
    "v_mfma_f32_16x16x32_bf16 v[0:3], v[148:151], v[176:179], v[0:3]\n\t"
    "ds_read_b64_tr_b16 v[140:141], v204 offset:9216\n\t"
    "v_mfma_f32_16x16x32_bf16 v[8:11], v[148:151], v[180:183], v[8:11]\n\t"
    "ds_read_b64_tr_b16 v[142:143], v204 offset:9728\n\t"
    "s_waitcnt lgkmcnt(12)\n\t"
    "v_mfma_f32_16x16x32_bf16 v[4:7], v[152:155], v[168:171], v[4:7]\n\t"
    "ds_read_b64_tr_b16 v[144:145], v205 offset:1024\n\t"
    "v_mfma_f32_16x16x32_bf16 v[12:15], v[152:155], v[172:175], v[12:15]\n\t"
    "ds_read_b64_tr_b16 v[146:147], v205 offset:1536\n\t"
    "v_mfma_f32_16x16x32_bf16 v[4:7], v[156:159], v[176:179], v[4:7]\n\t"
    "ds_read_b64_tr_b16 v[148:149], v205 offset:9216\n\t"
    "v_mfma_f32_16x16x32_bf16 v[12:15], v[156:159], v[180:183], v[12:15]\n\t"
    "ds_read_b64_tr_b16 v[150:151], v205 offset:9728\n\t"
    "s_waitcnt lgkmcnt(12)\n\t"
    "v_mfma_f32_16x16x32_bf16 v[16:19], v[160:163], v[168:171], v[16:19]\n\t"
    "ds_read_b64_tr_b16 v[152:153], v206 offset:1024\n\t"
    "s_setprio 1\n\t"
    "v_mfma_f32_16x16x32_bf16 v[24:27], v[160:163], v[172:175], v[24:27]\n\t"
    "ds_read_b64_tr_b16 v[154:155], v206 offset:1536\n\t"
    "v_mfma_f32_16x16x32_bf16 v[16:19], v[164:167], v[176:179], v[16:19]\n\t"
    "ds_read_b64_tr_b16 v[156:157], v206 offset:9216\n\t"
    "v_mfma_f32_16x16x32_bf16 v[24:27], v[164:167], v[180:183], v[24:27]\n\t"
    "ds_read_b64_tr_b16 v[158:159], v206 offset:9728\n\t"
    "s_waitcnt lgkmcnt(12)\n\t"
    "v_mfma_f32_16x16x32_bf16 v[20:23], v[128:131], v[168:171], v[20:23]\n\t"
    "ds_read_b64_tr_b16 v[160:161], v207 offset:1024\n\t"
    "v_mfma_f32_16x16x32_bf16 v[28:31], v[128:131], v[172:175], v[28:31]\n\t"
    "ds_read_b64_tr_b16 v[162:163], v207 offset:1536\n\t"
    "v_mfma_f32_16x16x32_bf16 v[20:23], v[132:135], v[176:179], v[20:23]\n\t"
    "ds_read_b64_tr_b16 v[164:165], v207 offset:9216\n\t"
    "s_setprio 0\n\t"
    "v_mfma_f32_16x16x32_bf16 v[28:31], v[132:135], v[180:183], v[28:31]\n\t"
    "ds_read_b64_tr_b16 v[166:167], v207 offset:9728\n\t"
    "s_waitcnt lgkmcnt(12)\n\t"
    "v_mfma_f32_16x16x32_bf16 v[32:35], v[136:139], v[168:171], v[32:35]\n\t"
    "v_mfma_f32_16x16x32_bf16 v[40:43], v[136:139], v[172:175], v[40:43]\n\t"
    "v_mfma_f32_16x16x32_bf16 v[32:35], v[140:143], v[176:179], v[32:35]\n\t"
    "v_mfma_f32_16x16x32_bf16 v[40:43], v[140:143], v[180:183], v[40:43]\n\t"
    "s_waitcnt vmcnt(12) lgkmcnt(0)\n\t"
    "s_barrier\n\t"
    "s_setprio 3\n\t"
    "ds_read_b64_tr_b16 v[128:129], v204 offset:16384\n\t"
    "ds_read_b64_tr_b16 v[130:131], v204 offset:16896\n\t"
    "ds_read_b64_tr_b16 v[132:133], v204 offset:24576\n\t"
    "ds_read_b64_tr_b16 v[134:135], v204 offset:25088\n\t"
    "v_mfma_f32_16x16x32_bf16 v[36:39], v[144:147], v[168:171], v[36:39]\n\t"
    "ds_read_b64_tr_b16 v[136:137], v205 offset:16384\n\t"
    "v_mfma_f32_16x16x32_bf16 v[44:47], v[144:147], v[172:175], v[44:47]\n\t"
    "ds_read_b64_tr_b16 v[138:139], v205 offset:16896\n\t"
    "v_mfma_f32_16x16x32_bf16 v[36:39], v[148:151], v[176:179], v[36:39]\n\t"
    "ds_read_b64_tr_b16 v[140:141], v205 offset:24576\n\t"
    "v_mfma_f32_16x16x32_bf16 v[44:47], v[148:151], v[180:183], v[44:47]\n\t"
    "ds_read_b64_tr_b16 v[142:143], v205 offset:25088\n\t"
    "v_mfma_f32_16x16x32_bf16 v[48:51], v[152:155], v[168:171], v[48:51]\n\t"
    "ds_read_b64_tr_b16 v[144:145], v206 offset:16384\n\t"
    "v_mfma_f32_16x16x32_bf16 v[56:59], v[152:155], v[172:175], v[56:59]\n\t"
    "ds_read_b64_tr_b16 v[146:147], v206 offset:16896\n\t"
    "v_mfma_f32_16x16x32_bf16 v[48:51], v[156:159], v[176:179], v[48:51]\n\t"
    "ds_read_b64_tr_b16 v[148:149], v206 offset:24576\n\t"
    "v_mfma_f32_16x16x32_bf16 v[56:59], v[156:159], v[180:183], v[56:59]\n\t"
    "ds_read_b64_tr_b16 v[150:151], v206 offset:25088\n\t"
    "v_mfma_f32_16x16x32_bf16 v[52:55], v[160:163], v[168:171], v[52:55]\n\t"
    "ds_read_b64_tr_b16 v[152:153], v207 offset:16384\n\t"
    "v_mfma_f32_16x16x32_bf16 v[60:63], v[160:163], v[172:175], v[60:63]\n\t"
    "ds_read_b64_tr_b16 v[154:155], v207 offset:16896\n\t"
    "s_add_u32 m0, s61, 131072\n\t"
    "s_mov_b32 s63, 2097408\n\t"
    "buffer_load_dwordx4 v216, s[40:43], s63 offen lds\n\t"
    "v_mfma_f32_16x16x32_bf16 v[52:55], v[164:167], v[176:179], v[52:55]\n\t"
    "ds_read_b64_tr_b16 v[156:157], v207 offset:24576\n\t"
    "v_mfma_f32_16x16x32_bf16 v[60:63], v[164:167], v[180:183], v[60:63]\n\t"
    "ds_read_b64_tr_b16 v[158:159], v207 offset:25088\n\t"
    "s_waitcnt lgkmcnt(12)\n\t"
    "v_mfma_f32_16x16x32_bf16 v[64:67], v[128:131], v[168:171], v[64:67]\n\t"
    "ds_read_b64_tr_b16 v[160:161], v204 offset:17408\n\t"
    "s_setprio 2\n\t"
    "v_mfma_f32_16x16x32_bf16 v[72:75], v[128:131], v[172:175], v[72:75]\n\t"
    "ds_read_b64_tr_b16 v[162:163], v204 offset:17920\n\t"
    "v_mfma_f32_16x16x32_bf16 v[64:67], v[132:135], v[176:179], v[64:67]\n\t"
    "ds_read_b64_tr_b16 v[164:165], v204 offset:25600\n\t"
    "v_mfma_f32_16x16x32_bf16 v[72:75], v[132:135], v[180:183], v[72:75]\n\t"
    "ds_read_b64_tr_b16 v[166:167], v204 offset:26112\n\t"
    "s_waitcnt lgkmcnt(12)\n\t"
    "v_mfma_f32_16x16x32_bf16 v[68:71], v[136:139], v[168:171], v[68:71]\n\t"
    "ds_read_b64_tr_b16 v[128:129], v205 offset:17408\n\t"
    "s_waitcnt vmcnt(9)\n\t"
    "ds_read_b128 v[184:187], v208 offset:0\n\t"
    "v_mfma_f32_16x16x32_bf16 v[76:79], v[136:139], v[172:175], v[76:79]\n\t"
    "ds_read_b64_tr_b16 v[130:131], v205 offset:17920\n\t"
    "ds_read_b128 v[188:191], v208 offset:2048\n\t"
    "v_mfma_f32_16x16x32_bf16 v[68:71], v[140:143], v[176:179], v[68:71]\n\t"
    "ds_read_b64_tr_b16 v[132:133], v205 offset:25600\n\t"
    "v_mfma_f32_16x16x32_bf16 v[76:79], v[140:143], v[180:183], v[76:79]\n\t"
    "ds_read_b64_tr_b16 v[134:135], v205 offset:26112\n\t"
    "s_add_u32 m0, s61, 132096\n\t"
    "s_mov_b32 s63, 2097536\n\t"
    "buffer_load_dwordx4 v216, s[40:43], s63 offen lds\n\t"
    "s_waitcnt lgkmcnt(14)\n\t"
    "v_mfma_f32_16x16x32_bf16 v[80:83], v[144:147], v[168:171], v[80:83]\n\t"
    "ds_read_b64_tr_b16 v[136:137], v206 offset:17408\n\t"
    "s_setprio 1\n\t"
    "ds_read_b128 v[192:195], v209 offset:0\n\t"
    "v_mfma_f32_16x16x32_bf16 v[88:91], v[144:147], v[172:175], v[88:91]\n\t"
    "ds_read_b64_tr_b16 v[138:139], v206 offset:17920\n\t"
    "ds_read_b128 v[196:199], v209 offset:2048\n\t"
    "v_mfma_f32_16x16x32_bf16 v[80:83], v[148:151], v[176:179], v[80:83]\n\t"
    "ds_read_b64_tr_b16 v[140:141], v206 offset:25600\n\t"
    "v_mfma_f32_16x16x32_bf16 v[88:91], v[148:151], v[180:183], v[88:91]\n\t"
    "ds_read_b64_tr_b16 v[142:143], v206 offset:26112\n\t"
    "s_waitcnt lgkmcnt(15)\n\t"
    "v_mfma_f32_16x16x32_bf16 v[84:87], v[152:155], v[168:171], v[84:87]\n\t"
    "ds_read_b64_tr_b16 v[144:145], v207 offset:17408\n\t"
    "v_mfma_f32_16x16x32_bf16 v[92:95], v[152:155], v[172:175], v[92:95]\n\t"
    "ds_read_b64_tr_b16 v[146:147], v207 offset:17920\n\t"
    "v_mfma_f32_16x16x32_bf16 v[84:87], v[156:159], v[176:179], v[84:87]\n\t"
    "ds_read_b64_tr_b16 v[148:149], v207 offset:25600\n\t"
    "s_setprio 0\n\t"
    "v_mfma_f32_16x16x32_bf16 v[92:95], v[156:159], v[180:183], v[92:95]\n\t"
    "ds_read_b64_tr_b16 v[150:151], v207 offset:26112\n\t"
    "s_waitcnt lgkmcnt(15)\n\t"
    "v_mfma_f32_16x16x32_bf16 v[96:99], v[160:163], v[168:171], v[96:99]\n\t"
    "global_load_dwordx4 v[224:227], v252, %[wopf]\n\t"
    "v_mfma_f32_16x16x32_bf16 v[104:107], v[160:163], v[172:175], v[104:107]\n\t"
    "v_mfma_f32_16x16x32_bf16 v[96:99], v[164:167], v[176:179], v[96:99]\n\t"
    "v_mfma_f32_16x16x32_bf16 v[104:107], v[164:167], v[180:183], v[104:107]\n\t"
    "s_waitcnt vmcnt(9) lgkmcnt(0)\n\t"
    "s_barrier\n\t"
    "s_setprio 3\n\t"
    "ds_read_b64_tr_b16 v[152:153], v200 offset:0\n\t"
    "ds_read_b64_tr_b16 v[154:155], v200 offset:512\n\t"
    "ds_read_b64_tr_b16 v[156:157], v200 offset:8192\n\t"
    "ds_read_b64_tr_b16 v[158:159], v200 offset:8704\n\t"
    "v_mfma_f32_16x16x32_bf16 v[100:103], v[128:131], v[168:171], v[100:103]\n\t"
    "ds_read_b64_tr_b16 v[160:161], v201 offset:0\n\t"
    "v_mfma_f32_16x16x32_bf16 v[108:111], v[128:131], v[172:175], v[108:111]\n\t"
    "ds_read_b64_tr_b16 v[162:163], v201 offset:512\n\t"
    "s_add_u32 m0, s61, 147456\n\t"
    "s_mov_b32 s63, 2097152\n\t"
    "buffer_load_dwordx4 v216, s[40:43], s63 offen lds\n\t"
    "v_mfma_f32_16x16x32_bf16 v[100:103], v[132:135], v[176:179], v[100:103]\n\t"
    "ds_read_b64_tr_b16 v[164:165], v201 offset:8192\n\t"
    "v_mfma_f32_16x16x32_bf16 v[108:111], v[132:135], v[180:183], v[108:111]\n\t"
    "ds_read_b64_tr_b16 v[166:167], v201 offset:8704\n\t"
    "s_add_u32 m0, s61, 148480\n\t"
    "s_mov_b32 s63, 2097280\n\t"
    "buffer_load_dwordx4 v216, s[40:43], s63 offen lds\n\t"
    "v_mfma_f32_16x16x32_bf16 v[112:115], v[136:139], v[168:171], v[112:115]\n\t"
    "ds_read_b64_tr_b16 v[128:129], v202 offset:0\n\t"
    "v_mfma_f32_16x16x32_bf16 v[120:123], v[136:139], v[172:175], v[120:123]\n\t"
    "ds_read_b64_tr_b16 v[130:131], v202 offset:512\n\t"
    "s_add_u32 m0, s62, 0\n\t"
    "s_mov_b32 s63, 640\n\t"
    "buffer_load_dwordx4 v212, s[36:39], s63 offen lds\n\t"
    "v_mfma_f32_16x16x32_bf16 v[112:115], v[140:143], v[176:179], v[112:115]\n\t"
    "ds_read_b64_tr_b16 v[132:133], v202 offset:8192\n\t"
    "v_mfma_f32_16x16x32_bf16 v[120:123], v[140:143], v[180:183], v[120:123]\n\t"
    "ds_read_b64_tr_b16 v[134:135], v202 offset:8704\n\t"
    "s_add_u32 m0, s62, 1024\n\t"
    "s_mov_b32 s63, 640\n\t"
    "buffer_load_dwordx4 v213, s[36:39], s63 offen lds\n\t"
    "v_mfma_f32_16x16x32_bf16 v[116:119], v[144:147], v[168:171], v[116:119]\n\t"
    "ds_read_b64_tr_b16 v[136:137], v203 offset:0\n\t"
    "v_mfma_f32_16x16x32_bf16 v[124:127], v[144:147], v[172:175], v[124:127]\n\t"
    "ds_read_b64_tr_b16 v[138:139], v203 offset:512\n\t"
    "s_add_u32 m0, s62, 2048\n\t"
    "s_mov_b32 s63, 640\n\t"
    "buffer_load_dwordx4 v214, s[36:39], s63 offen lds\n\t"
    "v_mfma_f32_16x16x32_bf16 v[116:119], v[148:151], v[176:179], v[116:119]\n\t"
    "ds_read_b64_tr_b16 v[140:141], v203 offset:8192\n\t"
    "v_mfma_f32_16x16x32_bf16 v[124:127], v[148:151], v[180:183], v[124:127]\n\t"
    "ds_read_b64_tr_b16 v[142:143], v203 offset:8704\n\t"
    "s_add_u32 m0, s62, 3072\n\t"
    "s_mov_b32 s63, 640\n\t"
    "buffer_load_dwordx4 v215, s[36:39], s63 offen lds\n\t"
    "s_waitcnt lgkmcnt(12)\n\t"
    "v_mfma_f32_16x16x32_bf16 v[0:3], v[152:155], v[184:187], v[0:3]\n\t"
    "ds_read_b64_tr_b16 v[144:145], v200 offset:1024\n\t"
    "s_setprio 2\n\t"
    "v_mfma_f32_16x16x32_bf16 v[8:11], v[152:155], v[188:191], v[8:11]\n\t"
    "ds_read_b64_tr_b16 v[146:147], v200 offset:1536\n\t"
    "v_mfma_f32_16x16x32_bf16 v[0:3], v[156:159], v[192:195], v[0:3]\n\t"
    "ds_read_b64_tr_b16 v[148:149], v200 offset:9216\n\t"
    "v_mfma_f32_16x16x32_bf16 v[8:11], v[156:159], v[196:199], v[8:11]\n\t"
    "ds_read_b64_tr_b16 v[150:151], v200 offset:9728\n\t"
    "s_waitcnt lgkmcnt(12)\n\t"
    "v_mfma_f32_16x16x32_bf16 v[4:7], v[160:163], v[184:187], v[4:7]\n\t"
    "ds_read_b64_tr_b16 v[152:153], v201 offset:1024\n\t"
    "v_mfma_f32_16x16x32_bf16 v[12:15], v[160:163], v[188:191], v[12:15]\n\t"
    "ds_read_b64_tr_b16 v[154:155], v201 offset:1536\n\t"
    "v_mfma_f32_16x16x32_bf16 v[4:7], v[164:167], v[192:195], v[4:7]\n\t"
    "ds_read_b64_tr_b16 v[156:157], v201 offset:9216\n\t"
    "v_mfma_f32_16x16x32_bf16 v[12:15], v[164:167], v[196:199], v[12:15]\n\t"
    "ds_read_b64_tr_b16 v[158:159], v201 offset:9728\n\t"
    "s_waitcnt lgkmcnt(12)\n\t"
    "v_mfma_f32_16x16x32_bf16 v[16:19], v[128:131], v[184:187], v[16:19]\n\t"
    "ds_read_b64_tr_b16 v[160:161], v202 offset:1024\n\t"
    "s_setprio 1\n\t"
    "v_mfma_f32_16x16x32_bf16 v[24:27], v[128:131], v[188:191], v[24:27]\n\t"
    "ds_read_b64_tr_b16 v[162:163], v202 offset:1536\n\t"
    "v_mfma_f32_16x16x32_bf16 v[16:19], v[132:135], v[192:195], v[16:19]\n\t"
    "ds_read_b64_tr_b16 v[164:165], v202 offset:9216\n\t"
    "v_mfma_f32_16x16x32_bf16 v[24:27], v[132:135], v[196:199], v[24:27]\n\t"
    "ds_read_b64_tr_b16 v[166:167], v202 offset:9728\n\t"
    "s_waitcnt lgkmcnt(12)\n\t"
    "v_mfma_f32_16x16x32_bf16 v[20:23], v[136:139], v[184:187], v[20:23]\n\t"
    "ds_read_b64_tr_b16 v[128:129], v203 offset:1024\n\t"
    "v_mfma_f32_16x16x32_bf16 v[28:31], v[136:139], v[188:191], v[28:31]\n\t"
    "ds_read_b64_tr_b16 v[130:131], v203 offset:1536\n\t"
    "v_mfma_f32_16x16x32_bf16 v[20:23], v[140:143], v[192:195], v[20:23]\n\t"
    "ds_read_b64_tr_b16 v[132:133], v203 offset:9216\n\t"
    "s_setprio 0\n\t"
    "v_mfma_f32_16x16x32_bf16 v[28:31], v[140:143], v[196:199], v[28:31]\n\t"
    "ds_read_b64_tr_b16 v[134:135], v203 offset:9728\n\t"
    "s_waitcnt lgkmcnt(12)\n\t"
    "v_mfma_f32_16x16x32_bf16 v[32:35], v[144:147], v[184:187], v[32:35]\n\t"
    "v_mfma_f32_16x16x32_bf16 v[40:43], v[144:147], v[188:191], v[40:43]\n\t"
    "v_mfma_f32_16x16x32_bf16 v[32:35], v[148:151], v[192:195], v[32:35]\n\t"
    "v_mfma_f32_16x16x32_bf16 v[40:43], v[148:151], v[196:199], v[40:43]\n\t"
    "s_waitcnt vmcnt(13) lgkmcnt(0)\n\t"
    "s_barrier\n\t"
    "s_setprio 3\n\t"
    "ds_read_b64_tr_b16 v[136:137], v200 offset:16384\n\t"
    "ds_read_b64_tr_b16 v[138:139], v200 offset:16896\n\t"
    "ds_read_b64_tr_b16 v[140:141], v200 offset:24576\n\t"
    "ds_read_b64_tr_b16 v[142:143], v200 offset:25088\n\t"
    "v_mfma_f32_16x16x32_bf16 v[36:39], v[152:155], v[184:187], v[36:39]\n\t"
    "ds_read_b64_tr_b16 v[144:145], v201 offset:16384\n\t"
    "v_mfma_f32_16x16x32_bf16 v[44:47], v[152:155], v[188:191], v[44:47]\n\t"
    "ds_read_b64_tr_b16 v[146:147], v201 offset:16896\n\t"
    "v_mfma_f32_16x16x32_bf16 v[36:39], v[156:159], v[192:195], v[36:39]\n\t"
    "ds_read_b64_tr_b16 v[148:149], v201 offset:24576\n\t"
    "v_mfma_f32_16x16x32_bf16 v[44:47], v[156:159], v[196:199], v[44:47]\n\t"
    "ds_read_b64_tr_b16 v[150:151], v201 offset:25088\n\t"
    "s_add_u32 m0, s61, 81920\n\t"
    "s_mov_b32 s63, 2621696\n\t"
    "buffer_load_dwordx4 v216, s[40:43], s63 offen lds\n\t"
    "v_mfma_f32_16x16x32_bf16 v[48:51], v[160:163], v[184:187], v[48:51]\n\t"
    "ds_read_b64_tr_b16 v[152:153], v202 offset:16384\n\t"
    "v_mfma_f32_16x16x32_bf16 v[56:59], v[160:163], v[188:191], v[56:59]\n\t"
    "ds_read_b64_tr_b16 v[154:155], v202 offset:16896\n\t"
    "v_mfma_f32_16x16x32_bf16 v[48:51], v[164:167], v[192:195], v[48:51]\n\t"
    "ds_read_b64_tr_b16 v[156:157], v202 offset:24576\n\t"
    "v_mfma_f32_16x16x32_bf16 v[56:59], v[164:167], v[196:199], v[56:59]\n\t"
    "ds_read_b64_tr_b16 v[158:159], v202 offset:25088\n\t"
    "s_add_u32 m0, s61, 82944\n\t"
    "s_mov_b32 s63, 2621824\n\t"
    "buffer_load_dwordx4 v216, s[40:43], s63 offen lds\n\t"
    "v_mfma_f32_16x16x32_bf16 v[52:55], v[128:131], v[184:187], v[52:55]\n\t"
    "ds_read_b64_tr_b16 v[160:161], v203 offset:16384\n\t"
    "v_mfma_f32_16x16x32_bf16 v[60:63], v[128:131], v[188:191], v[60:63]\n\t"
    "ds_read_b64_tr_b16 v[162:163], v203 offset:16896\n\t"
    "v_mfma_f32_16x16x32_bf16 v[52:55], v[132:135], v[192:195], v[52:55]\n\t"
    "ds_read_b64_tr_b16 v[164:165], v203 offset:24576\n\t"
    "v_mfma_f32_16x16x32_bf16 v[60:63], v[132:135], v[196:199], v[60:63]\n\t"
    "ds_read_b64_tr_b16 v[166:167], v203 offset:25088\n\t"
    "s_add_u32 m0, s60, 114688\n\t"
    "s_mov_b32 s63, 0\n\t"
    "buffer_load_dwordx4 v242, s[44:47], s63 offen nt lds\n\t"
    "s_waitcnt lgkmcnt(12)\n\t"
    "v_mfma_f32_16x16x32_bf16 v[64:67], v[136:139], v[184:187], v[64:67]\n\t"
    "ds_read_b64_tr_b16 v[128:129], v200 offset:17408\n\t"
    "s_setprio 2\n\t"
    "v_mfma_f32_16x16x32_bf16 v[72:75], v[136:139], v[188:191], v[72:75]\n\t"
    "ds_read_b64_tr_b16 v[130:131], v200 offset:17920\n\t"
    "v_mfma_f32_16x16x32_bf16 v[64:67], v[140:143], v[192:195], v[64:67]\n\t"
    "ds_read_b64_tr_b16 v[132:133], v200 offset:25600\n\t"
    "v_mfma_f32_16x16x32_bf16 v[72:75], v[140:143], v[196:199], v[72:75]\n\t"
    "ds_read_b64_tr_b16 v[134:135], v200 offset:26112\n\t"
    "s_add_u32 m0, s60, 122880\n\t"
    "s_mov_b32 s63, 8192\n\t"
    "buffer_load_dwordx4 v242, s[44:47], s63 offen nt lds\n\t"
    "s_waitcnt lgkmcnt(12)\n\t"
    "v_mfma_f32_16x16x32_bf16 v[68:71], v[144:147], v[184:187], v[68:71]\n\t"
    "ds_read_b64_tr_b16 v[136:137], v201 offset:17408\n\t"
    "s_waitcnt vmcnt(13)\n\t"
    "ds_read_b128 v[168:171], v208 offset:32768\n\t"
    "v_mfma_f32_16x16x32_bf16 v[76:79], v[144:147], v[188:191], v[76:79]\n\t"
    "ds_read_b64_tr_b16 v[138:139], v201 offset:17920\n\t"
    "ds_read_b128 v[172:175], v208 offset:34816\n\t"
    "v_mfma_f32_16x16x32_bf16 v[68:71], v[148:151], v[192:195], v[68:71]\n\t"
    "ds_read_b64_tr_b16 v[140:141], v201 offset:25600\n\t"
    "v_mfma_f32_16x16x32_bf16 v[76:79], v[148:151], v[196:199], v[76:79]\n\t"
    "ds_read_b64_tr_b16 v[142:143], v201 offset:26112\n\t"
    "s_waitcnt lgkmcnt(14)\n\t"
    "v_mfma_f32_16x16x32_bf16 v[80:83], v[152:155], v[184:187], v[80:83]\n\t"
    "ds_read_b64_tr_b16 v[144:145], v202 offset:17408\n\t"
    "s_setprio 1\n\t"
    "ds_read_b128 v[176:179], v209 offset:32768\n\t"
    "v_mfma_f32_16x16x32_bf16 v[88:91], v[152:155], v[188:191], v[88:91]\n\t"
    "ds_read_b64_tr_b16 v[146:147], v202 offset:17920\n\t"
    "ds_read_b128 v[180:183], v209 offset:34816\n\t"
    "v_mfma_f32_16x16x32_bf16 v[80:83], v[156:159], v[192:195], v[80:83]\n\t"
    "ds_read_b64_tr_b16 v[148:149], v202 offset:25600\n\t"
    "v_mfma_f32_16x16x32_bf16 v[88:91], v[156:159], v[196:199], v[88:91]\n\t"
    "ds_read_b64_tr_b16 v[150:151], v202 offset:26112\n\t"
    "s_waitcnt lgkmcnt(15)\n\t"
    "v_mfma_f32_16x16x32_bf16 v[84:87], v[160:163], v[184:187], v[84:87]\n\t"
    "ds_read_b64_tr_b16 v[152:153], v203 offset:17408\n\t"
    "v_mfma_f32_16x16x32_bf16 v[92:95], v[160:163], v[188:191], v[92:95]\n\t"
    "ds_read_b64_tr_b16 v[154:155], v203 offset:17920\n\t"
    "v_mfma_f32_16x16x32_bf16 v[84:87], v[164:167], v[192:195], v[84:87]\n\t"
    "ds_read_b64_tr_b16 v[156:157], v203 offset:25600\n\t"
    "s_setprio 0\n\t"
    "v_mfma_f32_16x16x32_bf16 v[92:95], v[164:167], v[196:199], v[92:95]\n\t"
    "ds_read_b64_tr_b16 v[158:159], v203 offset:26112\n\t"
    "s_waitcnt lgkmcnt(15)\n\t"
    "v_mfma_f32_16x16x32_bf16 v[96:99], v[128:131], v[184:187], v[96:99]\n\t"
    "v_mfma_f32_16x16x32_bf16 v[104:107], v[128:131], v[188:191], v[104:107]\n\t"
    "v_mfma_f32_16x16x32_bf16 v[96:99], v[132:135], v[192:195], v[96:99]\n\t"
    "v_mfma_f32_16x16x32_bf16 v[104:107], v[132:135], v[196:199], v[104:107]\n\t"
    "s_waitcnt vmcnt(11) lgkmcnt(0)\n\t"
    "s_barrier\n\t"
    "s_setprio 3\n\t"
    "ds_read_b64_tr_b16 v[160:161], v204 offset:0\n\t"
    "ds_read_b64_tr_b16 v[162:163], v204 offset:512\n\t"
    "ds_read_b64_tr_b16 v[164:165], v204 offset:8192\n\t"
    "ds_read_b64_tr_b16 v[166:167], v204 offset:8704\n\t"
    "v_mfma_f32_16x16x32_bf16 v[100:103], v[136:139], v[184:187], v[100:103]\n\t"
    "ds_read_b64_tr_b16 v[128:129], v205 offset:0\n\t"
    "v_mfma_f32_16x16x32_bf16 v[108:111], v[136:139], v[188:191], v[108:111]\n\t"
    "ds_read_b64_tr_b16 v[130:131], v205 offset:512\n\t"
    "s_add_u32 m0, s61, 98304\n\t"
    "s_mov_b32 s63, 2621440\n\t"
    "buffer_load_dwordx4 v216, s[40:43], s63 offen lds\n\t"
    "v_mfma_f32_16x16x32_bf16 v[100:103], v[140:143], v[192:195], v[100:103]\n\t"
    "ds_read_b64_tr_b16 v[132:133], v205 offset:8192\n\t"
    "v_mfma_f32_16x16x32_bf16 v[108:111], v[140:143], v[196:199], v[108:111]\n\t"
    "ds_read_b64_tr_b16 v[134:135], v205 offset:8704\n\t"
    "s_add_u32 m0, s61, 99328\n\t"
    "s_mov_b32 s63, 2621568\n\t"
    "buffer_load_dwordx4 v216, s[40:43], s63 offen lds\n\t"
    "v_mfma_f32_16x16x32_bf16 v[112:115], v[144:147], v[184:187], v[112:115]\n\t"
    "ds_read_b64_tr_b16 v[136:137], v206 offset:0\n\t"
    "v_mfma_f32_16x16x32_bf16 v[120:123], v[144:147], v[188:191], v[120:123]\n\t"
    "ds_read_b64_tr_b16 v[138:139], v206 offset:512\n\t"
    "s_add_u32 m0, s62, 32768\n\t"
    "s_mov_b32 s63, 768\n\t"
    "buffer_load_dwordx4 v212, s[36:39], s63 offen lds\n\t"
    "v_mfma_f32_16x16x32_bf16 v[112:115], v[148:151], v[192:195], v[112:115]\n\t"
    "ds_read_b64_tr_b16 v[140:141], v206 offset:8192\n\t"
    "v_mfma_f32_16x16x32_bf16 v[120:123], v[148:151], v[196:199], v[120:123]\n\t"
    "ds_read_b64_tr_b16 v[142:143], v206 offset:8704\n\t"
    "s_add_u32 m0, s62, 33792\n\t"
    "s_mov_b32 s63, 768\n\t"
    "buffer_load_dwordx4 v213, s[36:39], s63 offen lds\n\t"
    "v_mfma_f32_16x16x32_bf16 v[116:119], v[152:155], v[184:187], v[116:119]\n\t"
    "ds_read_b64_tr_b16 v[144:145], v207 offset:0\n\t"
    "v_mfma_f32_16x16x32_bf16 v[124:127], v[152:155], v[188:191], v[124:127]\n\t"
    "ds_read_b64_tr_b16 v[146:147], v207 offset:512\n\t"
    "s_add_u32 m0, s62, 34816\n\t"
    "s_mov_b32 s63, 768\n\t"
    "buffer_load_dwordx4 v214, s[36:39], s63 offen lds\n\t"
    "v_mfma_f32_16x16x32_bf16 v[116:119], v[156:159], v[192:195], v[116:119]\n\t"
    "ds_read_b64_tr_b16 v[148:149], v207 offset:8192\n\t"
    "v_mfma_f32_16x16x32_bf16 v[124:127], v[156:159], v[196:199], v[124:127]\n\t"
    "ds_read_b64_tr_b16 v[150:151], v207 offset:8704\n\t"
    "s_add_u32 m0, s62, 35840\n\t"
    "s_mov_b32 s63, 768\n\t"
    "buffer_load_dwordx4 v215, s[36:39], s63 offen lds\n\t"
    "s_waitcnt lgkmcnt(12)\n\t"
    "v_mfma_f32_16x16x32_bf16 v[0:3], v[160:163], v[168:171], v[0:3]\n\t"
    "ds_read_b64_tr_b16 v[152:153], v204 offset:1024\n\t"
    "s_setprio 2\n\t"
    "v_mfma_f32_16x16x32_bf16 v[8:11], v[160:163], v[172:175], v[8:11]\n\t"
    "ds_read_b64_tr_b16 v[154:155], v204 offset:1536\n\t"
    "v_mfma_f32_16x16x32_bf16 v[0:3], v[164:167], v[176:179], v[0:3]\n\t"
    "ds_read_b64_tr_b16 v[156:157], v204 offset:9216\n\t"
    "v_mfma_f32_16x16x32_bf16 v[8:11], v[164:167], v[180:183], v[8:11]\n\t"
    "ds_read_b64_tr_b16 v[158:159], v204 offset:9728\n\t"
    "s_waitcnt lgkmcnt(12)\n\t"
    "v_mfma_f32_16x16x32_bf16 v[4:7], v[128:131], v[168:171], v[4:7]\n\t"
    "ds_read_b64_tr_b16 v[160:161], v205 offset:1024\n\t"
    "v_mfma_f32_16x16x32_bf16 v[12:15], v[128:131], v[172:175], v[12:15]\n\t"
    "ds_read_b64_tr_b16 v[162:163], v205 offset:1536\n\t"
    "v_mfma_f32_16x16x32_bf16 v[4:7], v[132:135], v[176:179], v[4:7]\n\t"
    "ds_read_b64_tr_b16 v[164:165], v205 offset:9216\n\t"
    "v_mfma_f32_16x16x32_bf16 v[12:15], v[132:135], v[180:183], v[12:15]\n\t"
    "ds_read_b64_tr_b16 v[166:167], v205 offset:9728\n\t"
    "s_waitcnt lgkmcnt(12)\n\t"
    "v_mfma_f32_16x16x32_bf16 v[16:19], v[136:139], v[168:171], v[16:19]\n\t"
    "ds_read_b64_tr_b16 v[128:129], v206 offset:1024\n\t"
    "s_setprio 1\n\t"
    "v_mfma_f32_16x16x32_bf16 v[24:27], v[136:139], v[172:175], v[24:27]\n\t"
    "ds_read_b64_tr_b16 v[130:131], v206 offset:1536\n\t"
    "v_mfma_f32_16x16x32_bf16 v[16:19], v[140:143], v[176:179], v[16:19]\n\t"
    "ds_read_b64_tr_b16 v[132:133], v206 offset:9216\n\t"
    "v_mfma_f32_16x16x32_bf16 v[24:27], v[140:143], v[180:183], v[24:27]\n\t"
    "ds_read_b64_tr_b16 v[134:135], v206 offset:9728\n\t"
    "s_waitcnt lgkmcnt(12)\n\t"
    "v_mfma_f32_16x16x32_bf16 v[20:23], v[144:147], v[168:171], v[20:23]\n\t"
    "ds_read_b64_tr_b16 v[136:137], v207 offset:1024\n\t"
    "v_mfma_f32_16x16x32_bf16 v[28:31], v[144:147], v[172:175], v[28:31]\n\t"
    "ds_read_b64_tr_b16 v[138:139], v207 offset:1536\n\t"
    "v_mfma_f32_16x16x32_bf16 v[20:23], v[148:151], v[176:179], v[20:23]\n\t"
    "ds_read_b64_tr_b16 v[140:141], v207 offset:9216\n\t"
    "s_setprio 0\n\t"
    "v_mfma_f32_16x16x32_bf16 v[28:31], v[148:151], v[180:183], v[28:31]\n\t"
    "ds_read_b64_tr_b16 v[142:143], v207 offset:9728\n\t"
    "s_waitcnt lgkmcnt(12)\n\t"
    "v_mfma_f32_16x16x32_bf16 v[32:35], v[152:155], v[168:171], v[32:35]\n\t"
    "v_mfma_f32_16x16x32_bf16 v[40:43], v[152:155], v[172:175], v[40:43]\n\t"
    "v_mfma_f32_16x16x32_bf16 v[32:35], v[156:159], v[176:179], v[32:35]\n\t"
    "v_mfma_f32_16x16x32_bf16 v[40:43], v[156:159], v[180:183], v[40:43]\n\t"
    "s_waitcnt vmcnt(14) lgkmcnt(0)\n\t"
    "s_barrier\n\t"
    "s_setprio 3\n\t"
    "PAD1_%=: s_cbranch_execz PAD9_%=\n\t"
    "ds_read_b64_tr_b16 v[144:145], v204 offset:16384\n\t"
    "ds_read_b64_tr_b16 v[146:147], v204 offset:16896\n\t"
    "ds_read_b64_tr_b16 v[148:149], v204 offset:24576\n\t"
    "ds_read_b64_tr_b16 v[150:151], v204 offset:25088\n\t"
    "v_mfma_f32_16x16x32_bf16 v[36:39], v[160:163], v[168:171], v[36:39]\n\t"
    "ds_read_b64_tr_b16 v[152:153], v205 offset:16384\n\t"
    "v_mfma_f32_16x16x32_bf16 v[44:47], v[160:163], v[172:175], v[44:47]\n\t"
    "ds_read_b64_tr_b16 v[154:155], v205 offset:16896\n\t"
    "v_mfma_f32_16x16x32_bf16 v[36:39], v[164:167], v[176:179], v[36:39]\n\t"
    "ds_read_b64_tr_b16 v[156:157], v205 offset:24576\n\t"
    "v_mfma_f32_16x16x32_bf16 v[44:47], v[164:167], v[180:183], v[44:47]\n\t"
    "ds_read_b64_tr_b16 v[158:159], v205 offset:25088\n\t"
    "s_add_u32 m0, s61, 131072\n\t"
    "s_mov_b32 s63, 3145984\n\t"
    "buffer_load_dwordx4 v216, s[40:43], s63 offen lds\n\t"
    "v_mfma_f32_16x16x32_bf16 v[48:51], v[128:131], v[168:171], v[48:51]\n\t"
    "ds_read_b64_tr_b16 v[160:161], v206 offset:16384\n\t"
    "v_mfma_f32_16x16x32_bf16 v[56:59], v[128:131], v[172:175], v[56:59]\n\t"
    "ds_read_b64_tr_b16 v[162:163], v206 offset:16896\n\t"
    "v_mfma_f32_16x16x32_bf16 v[48:51], v[132:135], v[176:179], v[48:51]\n\t"
    "ds_read_b64_tr_b16 v[164:165], v206 offset:24576\n\t"
    "v_mfma_f32_16x16x32_bf16 v[56:59], v[132:135], v[180:183], v[56:59]\n\t"
    "ds_read_b64_tr_b16 v[166:167], v206 offset:25088\n\t"
    "s_add_u32 m0, s61, 132096\n\t"
    "s_mov_b32 s63, 3146112\n\t"
    "buffer_load_dwordx4 v216, s[40:43], s63 offen lds\n\t"
    "v_mfma_f32_16x16x32_bf16 v[52:55], v[136:139], v[168:171], v[52:55]\n\t"
    "ds_read_b64_tr_b16 v[128:129], v207 offset:16384\n\t"
    "v_mfma_f32_16x16x32_bf16 v[60:63], v[136:139], v[172:175], v[60:63]\n\t"
    "ds_read_b64_tr_b16 v[130:131], v207 offset:16896\n\t"
    "v_mfma_f32_16x16x32_bf16 v[52:55], v[140:143], v[176:179], v[52:55]\n\t"
    "ds_read_b64_tr_b16 v[132:133], v207 offset:24576\n\t"
    "v_mfma_f32_16x16x32_bf16 v[60:63], v[140:143], v[180:183], v[60:63]\n\t"
    "ds_read_b64_tr_b16 v[134:135], v207 offset:25088\n\t"
    "s_add_u32 m0, s60, 65536\n\t"
    "s_mov_b32 s63, 0\n\t"
    "buffer_load_dwordx4 v243, s[48:51], s63 offen nt lds\n\t"
    "s_waitcnt lgkmcnt(12)\n\t"
    "v_mfma_f32_16x16x32_bf16 v[64:67], v[144:147], v[168:171], v[64:67]\n\t"
    "ds_read_b64_tr_b16 v[136:137], v204 offset:17408\n\t"
    "s_setprio 2\n\t"
    "v_mfma_f32_16x16x32_bf16 v[72:75], v[144:147], v[172:175], v[72:75]\n\t"
    "ds_read_b64_tr_b16 v[138:139], v204 offset:17920\n\t"
    "v_mfma_f32_16x16x32_bf16 v[64:67], v[148:151], v[176:179], v[64:67]\n\t"
    "ds_read_b64_tr_b16 v[140:141], v204 offset:25600\n\t"
    "v_mfma_f32_16x16x32_bf16 v[72:75], v[148:151], v[180:183], v[72:75]\n\t"
    "ds_read_b64_tr_b16 v[142:143], v204 offset:26112\n\t"
    "s_add_u32 m0, s60, 73728\n\t"
    "s_mov_b32 s63, 8192\n\t"
    "buffer_load_dwordx4 v243, s[48:51], s63 offen nt lds\n\t"
    "s_waitcnt lgkmcnt(12)\n\t"
    "v_mfma_f32_16x16x32_bf16 v[68:71], v[152:155], v[168:171], v[68:71]\n\t"
    "ds_read_b64_tr_b16 v[144:145], v205 offset:17408\n\t"
    "s_waitcnt vmcnt(14)\n\t"
    "ds_read_b128 v[184:187], v208 offset:0\n\t"
    "v_mfma_f32_16x16x32_bf16 v[76:79], v[152:155], v[172:175], v[76:79]\n\t"
    "ds_read_b64_tr_b16 v[146:147], v205 offset:17920\n\t"
    "ds_read_b128 v[188:191], v208 offset:2048\n\t"
    "v_mfma_f32_16x16x32_bf16 v[68:71], v[156:159], v[176:179], v[68:71]\n\t"
    "ds_read_b64_tr_b16 v[148:149], v205 offset:25600\n\t"
    "v_mfma_f32_16x16x32_bf16 v[76:79], v[156:159], v[180:183], v[76:79]\n\t"
    "ds_read_b64_tr_b16 v[150:151], v205 offset:26112\n\t"
    "s_waitcnt lgkmcnt(14)\n\t"
    "v_mfma_f32_16x16x32_bf16 v[80:83], v[160:163], v[168:171], v[80:83]\n\t"
    "ds_read_b64_tr_b16 v[152:153], v206 offset:17408\n\t"
    "s_setprio 1\n\t"
    "ds_read_b128 v[192:195], v209 offset:0\n\t"
    "v_mfma_f32_16x16x32_bf16 v[88:91], v[160:163], v[172:175], v[88:91]\n\t"
    "ds_read_b64_tr_b16 v[154:155], v206 offset:17920\n\t"
    "ds_read_b128 v[196:199], v209 offset:2048\n\t"
    "v_mfma_f32_16x16x32_bf16 v[80:83], v[164:167], v[176:179], v[80:83]\n\t"
    "ds_read_b64_tr_b16 v[156:157], v206 offset:25600\n\t"
    "v_mfma_f32_16x16x32_bf16 v[88:91], v[164:167], v[180:183], v[88:91]\n\t"
    "ds_read_b64_tr_b16 v[158:159], v206 offset:26112\n\t"
    "s_waitcnt lgkmcnt(15)\n\t"
    "v_mfma_f32_16x16x32_bf16 v[84:87], v[128:131], v[168:171], v[84:87]\n\t"
    "ds_read_b64_tr_b16 v[160:161], v207 offset:17408\n\t"
    "v_mfma_f32_16x16x32_bf16 v[92:95], v[128:131], v[172:175], v[92:95]\n\t"
    "ds_read_b64_tr_b16 v[162:163], v207 offset:17920\n\t"
    "v_mfma_f32_16x16x32_bf16 v[84:87], v[132:135], v[176:179], v[84:87]\n\t"
    "ds_read_b64_tr_b16 v[164:165], v207 offset:25600\n\t"
    "s_setprio 0\n\t"
    "v_mfma_f32_16x16x32_bf16 v[92:95], v[132:135], v[180:183], v[92:95]\n\t"
    "ds_read_b64_tr_b16 v[166:167], v207 offset:26112\n\t"
    "s_waitcnt lgkmcnt(15)\n\t"
    "v_mfma_f32_16x16x32_bf16 v[96:99], v[136:139], v[168:171], v[96:99]\n\t"
    "v_mfma_f32_16x16x32_bf16 v[104:107], v[136:139], v[172:175], v[104:107]\n\t"
    "v_mfma_f32_16x16x32_bf16 v[96:99], v[140:143], v[176:179], v[96:99]\n\t"
    "v_mfma_f32_16x16x32_bf16 v[104:107], v[140:143], v[180:183], v[104:107]\n\t"
    "s_waitcnt vmcnt(12) lgkmcnt(0)\n\t"
    "s_barrier\n\t"
    "s_setprio 3\n\t"
    "ds_read_b64_tr_b16 v[128:129], v200 offset:0\n\t"
    "ds_read_b64_tr_b16 v[130:131], v200 offset:512\n\t"
    "ds_read_b64_tr_b16 v[132:133], v200 offset:8192\n\t"
    "ds_read_b64_tr_b16 v[134:135], v200 offset:8704\n\t"
    "v_mfma_f32_16x16x32_bf16 v[100:103], v[144:147], v[168:171], v[100:103]\n\t"
    "ds_read_b64_tr_b16 v[136:137], v201 offset:0\n\t"
    "v_mfma_f32_16x16x32_bf16 v[108:111], v[144:147], v[172:175], v[108:111]\n\t"
    "ds_read_b64_tr_b16 v[138:139], v201 offset:512\n\t"
    "s_add_u32 m0, s61, 147456\n\t"
    "s_mov_b32 s63, 3145728\n\t"
    "buffer_load_dwordx4 v216, s[40:43], s63 offen lds\n\t"
    "v_mfma_f32_16x16x32_bf16 v[100:103], v[148:151], v[176:179], v[100:103]\n\t"
    "ds_read_b64_tr_b16 v[140:141], v201 offset:8192\n\t"
    "v_mfma_f32_16x16x32_bf16 v[108:111], v[148:151], v[180:183], v[108:111]\n\t"
    "ds_read_b64_tr_b16 v[142:143], v201 offset:8704\n\t"
    "s_add_u32 m0, s61, 148480\n\t"
    "s_mov_b32 s63, 3145856\n\t"
    "buffer_load_dwordx4 v216, s[40:43], s63 offen lds\n\t"
    "v_mfma_f32_16x16x32_bf16 v[112:115], v[152:155], v[168:171], v[112:115]\n\t"
    "ds_read_b64_tr_b16 v[144:145], v202 offset:0\n\t"
    "v_mfma_f32_16x16x32_bf16 v[120:123], v[152:155], v[172:175], v[120:123]\n\t"
    "ds_read_b64_tr_b16 v[146:147], v202 offset:512\n\t"
    "s_add_u32 m0, s62, 0\n\t"
    "s_mov_b32 s63, 896\n\t"
    "buffer_load_dwordx4 v212, s[36:39], s63 offen lds\n\t"
    "v_mfma_f32_16x16x32_bf16 v[112:115], v[156:159], v[176:179], v[112:115]\n\t"
    "ds_read_b64_tr_b16 v[148:149], v202 offset:8192\n\t"
    "v_mfma_f32_16x16x32_bf16 v[120:123], v[156:159], v[180:183], v[120:123]\n\t"
    "ds_read_b64_tr_b16 v[150:151], v202 offset:8704\n\t"
    "s_add_u32 m0, s62, 1024\n\t"
    "s_mov_b32 s63, 896\n\t"
    "buffer_load_dwordx4 v213, s[36:39], s63 offen lds\n\t"
    "v_mfma_f32_16x16x32_bf16 v[116:119], v[160:163], v[168:171], v[116:119]\n\t"
    "ds_read_b64_tr_b16 v[152:153], v203 offset:0\n\t"
    "v_mfma_f32_16x16x32_bf16 v[124:127], v[160:163], v[172:175], v[124:127]\n\t"
    "ds_read_b64_tr_b16 v[154:155], v203 offset:512\n\t"
    "s_add_u32 m0, s62, 2048\n\t"
    "s_mov_b32 s63, 896\n\t"
    "buffer_load_dwordx4 v214, s[36:39], s63 offen lds\n\t"
    "v_mfma_f32_16x16x32_bf16 v[116:119], v[164:167], v[176:179], v[116:119]\n\t"
    "ds_read_b64_tr_b16 v[156:157], v203 offset:8192\n\t"
    "v_mfma_f32_16x16x32_bf16 v[124:127], v[164:167], v[180:183], v[124:127]\n\t"
    "ds_read_b64_tr_b16 v[158:159], v203 offset:8704\n\t"
    "s_add_u32 m0, s62, 3072\n\t"
    "s_mov_b32 s63, 896\n\t"
    "buffer_load_dwordx4 v215, s[36:39], s63 offen lds\n\t"
    "s_waitcnt lgkmcnt(12)\n\t"
    "v_mfma_f32_16x16x32_bf16 v[0:3], v[128:131], v[184:187], v[0:3]\n\t"
    "ds_read_b64_tr_b16 v[160:161], v200 offset:1024\n\t"
    "s_setprio 2\n\t"
    "v_mfma_f32_16x16x32_bf16 v[8:11], v[128:131], v[188:191], v[8:11]\n\t"
    "ds_read_b64_tr_b16 v[162:163], v200 offset:1536\n\t"
    "v_mfma_f32_16x16x32_bf16 v[0:3], v[132:135], v[192:195], v[0:3]\n\t"
    "ds_read_b64_tr_b16 v[164:165], v200 offset:9216\n\t"
    "v_mfma_f32_16x16x32_bf16 v[8:11], v[132:135], v[196:199], v[8:11]\n\t"
    "ds_read_b64_tr_b16 v[166:167], v200 offset:9728\n\t"
    "s_waitcnt lgkmcnt(12)\n\t"
    "v_mfma_f32_16x16x32_bf16 v[4:7], v[136:139], v[184:187], v[4:7]\n\t"
    "ds_read_b64_tr_b16 v[128:129], v201 offset:1024\n\t"
    "v_mfma_f32_16x16x32_bf16 v[12:15], v[136:139], v[188:191], v[12:15]\n\t"
    "ds_read_b64_tr_b16 v[130:131], v201 offset:1536\n\t"
    "v_mfma_f32_16x16x32_bf16 v[4:7], v[140:143], v[192:195], v[4:7]\n\t"
    "ds_read_b64_tr_b16 v[132:133], v201 offset:9216\n\t"
    "v_mfma_f32_16x16x32_bf16 v[12:15], v[140:143], v[196:199], v[12:15]\n\t"
    "ds_read_b64_tr_b16 v[134:135], v201 offset:9728\n\t"
    "s_waitcnt lgkmcnt(12)\n\t"
    "v_mfma_f32_16x16x32_bf16 v[16:19], v[144:147], v[184:187], v[16:19]\n\t"
    "ds_read_b64_tr_b16 v[136:137], v202 offset:1024\n\t"
    "s_setprio 1\n\t"
    "v_mfma_f32_16x16x32_bf16 v[24:27], v[144:147], v[188:191], v[24:27]\n\t"
    "ds_read_b64_tr_b16 v[138:139], v202 offset:1536\n\t"
    "v_mfma_f32_16x16x32_bf16 v[16:19], v[148:151], v[192:195], v[16:19]\n\t"
    "ds_read_b64_tr_b16 v[140:141], v202 offset:9216\n\t"
    "v_mfma_f32_16x16x32_bf16 v[24:27], v[148:151], v[196:199], v[24:27]\n\t"
    "ds_read_b64_tr_b16 v[142:143], v202 offset:9728\n\t"
    "s_waitcnt lgkmcnt(12)\n\t"
    "v_mfma_f32_16x16x32_bf16 v[20:23], v[152:155], v[184:187], v[20:23]\n\t"
    "ds_read_b64_tr_b16 v[144:145], v203 offset:1024\n\t"
    "v_mfma_f32_16x16x32_bf16 v[28:31], v[152:155], v[188:191], v[28:31]\n\t"
    "ds_read_b64_tr_b16 v[146:147], v203 offset:1536\n\t"
    "v_mfma_f32_16x16x32_bf16 v[20:23], v[156:159], v[192:195], v[20:23]\n\t"
    "ds_read_b64_tr_b16 v[148:149], v203 offset:9216\n\t"
    "s_setprio 0\n\t"
    "v_mfma_f32_16x16x32_bf16 v[28:31], v[156:159], v[196:199], v[28:31]\n\t"
    "ds_read_b64_tr_b16 v[150:151], v203 offset:9728\n\t"
    "s_waitcnt lgkmcnt(12)\n\t"
    "v_mfma_f32_16x16x32_bf16 v[32:35], v[160:163], v[184:187], v[32:35]\n\t"
    "v_mfma_f32_16x16x32_bf16 v[40:43], v[160:163], v[188:191], v[40:43]\n\t"
    "v_mfma_f32_16x16x32_bf16 v[32:35], v[164:167], v[192:195], v[32:35]\n\t"
    "v_mfma_f32_16x16x32_bf16 v[40:43], v[164:167], v[196:199], v[40:43]\n\t"
    "s_waitcnt vmcnt(14) lgkmcnt(0)\n\t"
    "s_barrier\n\t"
    "s_setprio 3\n\t"
    "ds_read_b64_tr_b16 v[152:153], v200 offset:16384\n\t"
    "ds_read_b64_tr_b16 v[154:155], v200 offset:16896\n\t"
    "ds_read_b64_tr_b16 v[156:157], v200 offset:24576\n\t"
    "ds_read_b64_tr_b16 v[158:159], v200 offset:25088\n\t"
    "v_mfma_f32_16x16x32_bf16 v[36:39], v[128:131], v[184:187], v[36:39]\n\t"
    "ds_read_b64_tr_b16 v[160:161], v201 offset:16384\n\t"
    "v_mfma_f32_16x16x32_bf16 v[44:47], v[128:131], v[188:191], v[44:47]\n\t"
    "ds_read_b64_tr_b16 v[162:163], v201 offset:16896\n\t"
    "v_mfma_f32_16x16x32_bf16 v[36:39], v[132:135], v[192:195], v[36:39]\n\t"
    "ds_read_b64_tr_b16 v[164:165], v201 offset:24576\n\t"
    "v_mfma_f32_16x16x32_bf16 v[44:47], v[132:135], v[196:199], v[44:47]\n\t"
    "ds_read_b64_tr_b16 v[166:167], v201 offset:25088\n\t"
    "v_mfma_f32_16x16x32_bf16 v[48:51], v[136:139], v[184:187], v[48:51]\n\t"
    "ds_read_b64_tr_b16 v[128:129], v202 offset:16384\n\t"
    "v_mfma_f32_16x16x32_bf16 v[56:59], v[136:139], v[188:191], v[56:59]\n\t"
    "ds_read_b64_tr_b16 v[130:131], v202 offset:16896\n\t"
    "v_mfma_f32_16x16x32_bf16 v[48:51], v[140:143], v[192:195], v[48:51]\n\t"
    "ds_read_b64_tr_b16 v[132:133], v202 offset:24576\n\t"
    "v_mfma_f32_16x16x32_bf16 v[56:59], v[140:143], v[196:199], v[56:59]\n\t"
    "ds_read_b64_tr_b16 v[134:135], v202 offset:25088\n\t"
    "v_mfma_f32_16x16x32_bf16 v[52:55], v[144:147], v[184:187], v[52:55]\n\t"
    "ds_read_b64_tr_b16 v[136:137], v203 offset:16384\n\t"
    "v_mfma_f32_16x16x32_bf16 v[60:63], v[144:147], v[188:191], v[60:63]\n\t"
    "ds_read_b64_tr_b16 v[138:139], v203 offset:16896\n\t"
    "s_add_u32 m0, s61, 81920\n\t"
    "s_mov_b32 s63, 3670272\n\t"
    "buffer_load_dwordx4 v216, s[40:43], s63 offen lds\n\t"
    "v_mfma_f32_16x16x32_bf16 v[52:55], v[148:151], v[192:195], v[52:55]\n\t"
    "ds_read_b64_tr_b16 v[140:141], v203 offset:24576\n\t"
    "v_mfma_f32_16x16x32_bf16 v[60:63], v[148:151], v[196:199], v[60:63]\n\t"
    "ds_read_b64_tr_b16 v[142:143], v203 offset:25088\n\t"
    "s_waitcnt lgkmcnt(12)\n\t"
    "v_mfma_f32_16x16x32_bf16 v[64:67], v[152:155], v[184:187], v[64:67]\n\t"
    "ds_read_b64_tr_b16 v[144:145], v200 offset:17408\n\t"
    "s_setprio 2\n\t"
    "v_mfma_f32_16x16x32_bf16 v[72:75], v[152:155], v[188:191], v[72:75]\n\t"
    "ds_read_b64_tr_b16 v[146:147], v200 offset:17920\n\t"
    "v_mfma_f32_16x16x32_bf16 v[64:67], v[156:159], v[192:195], v[64:67]\n\t"
    "ds_read_b64_tr_b16 v[148:149], v200 offset:25600\n\t"
    "v_mfma_f32_16x16x32_bf16 v[72:75], v[156:159], v[196:199], v[72:75]\n\t"
    "ds_read_b64_tr_b16 v[150:151], v200 offset:26112\n\t"
    "s_waitcnt lgkmcnt(12)\n\t"
    "v_mfma_f32_16x16x32_bf16 v[68:71], v[160:163], v[184:187], v[68:71]\n\t"
    "ds_read_b64_tr_b16 v[152:153], v201 offset:17408\n\t"
    "s_waitcnt vmcnt(11)\n\t"
    "ds_read_b128 v[168:171], v208 offset:32768\n\t"
    "v_mfma_f32_16x16x32_bf16 v[76:79], v[160:163], v[188:191], v[76:79]\n\t"
    "ds_read_b64_tr_b16 v[154:155], v201 offset:17920\n\t"
    "ds_read_b128 v[172:175], v208 offset:34816\n\t"
    "v_mfma_f32_16x16x32_bf16 v[68:71], v[164:167], v[192:195], v[68:71]\n\t"
    "ds_read_b64_tr_b16 v[156:157], v201 offset:25600\n\t"
    "v_mfma_f32_16x16x32_bf16 v[76:79], v[164:167], v[196:199], v[76:79]\n\t"
    "ds_read_b64_tr_b16 v[158:159], v201 offset:26112\n\t"
    "s_add_u32 m0, s61, 82944\n\t"
    "s_mov_b32 s63, 3670400\n\t"
    "buffer_load_dwordx4 v216, s[40:43], s63 offen lds\n\t"
    "s_waitcnt lgkmcnt(14)\n\t"
    "v_mfma_f32_16x16x32_bf16 v[80:83], v[128:131], v[184:187], v[80:83]\n\t"
    "ds_read_b64_tr_b16 v[160:161], v202 offset:17408\n\t"
    "s_setprio 1\n\t"
    "ds_read_b128 v[176:179], v209 offset:32768\n\t"
    "v_mfma_f32_16x16x32_bf16 v[88:91], v[128:131], v[188:191], v[88:91]\n\t"
    "ds_read_b64_tr_b16 v[162:163], v202 offset:17920\n\t"
    "ds_read_b128 v[180:183], v209 offset:34816\n\t"
    "v_mfma_f32_16x16x32_bf16 v[80:83], v[132:135], v[192:195], v[80:83]\n\t"
    "ds_read_b64_tr_b16 v[164:165], v202 offset:25600\n\t"
    "v_mfma_f32_16x16x32_bf16 v[88:91], v[132:135], v[196:199], v[88:91]\n\t"
    "ds_read_b64_tr_b16 v[166:167], v202 offset:26112\n\t"
    "s_waitcnt lgkmcnt(15)\n\t"
    "v_mfma_f32_16x16x32_bf16 v[84:87], v[136:139], v[184:187], v[84:87]\n\t"
    "ds_read_b64_tr_b16 v[128:129], v203 offset:17408\n\t"
    "v_mfma_f32_16x16x32_bf16 v[92:95], v[136:139], v[188:191], v[92:95]\n\t"
    "ds_read_b64_tr_b16 v[130:131], v203 offset:17920\n\t"
    "v_mfma_f32_16x16x32_bf16 v[84:87], v[140:143], v[192:195], v[84:87]\n\t"
    "ds_read_b64_tr_b16 v[132:133], v203 offset:25600\n\t"
    "s_setprio 0\n\t"
    "v_mfma_f32_16x16x32_bf16 v[92:95], v[140:143], v[196:199], v[92:95]\n\t"
    "ds_read_b64_tr_b16 v[134:135], v203 offset:26112\n\t"
    "s_waitcnt lgkmcnt(15)\n\t"
    "v_mfma_f32_16x16x32_bf16 v[96:99], v[144:147], v[184:187], v[96:99]\n\t"
    "global_load_dwordx4 v[228:231], v252, %[wopf] offset:1024\n\t"
    "v_mfma_f32_16x16x32_bf16 v[104:107], v[144:147], v[188:191], v[104:107]\n\t"
    "v_mfma_f32_16x16x32_bf16 v[96:99], v[148:151], v[192:195], v[96:99]\n\t"
    "v_mfma_f32_16x16x32_bf16 v[104:107], v[148:151], v[196:199], v[104:107]\n\t"
    "s_waitcnt vmcnt(11) lgkmcnt(0)\n\t"
    "s_barrier\n\t"
    "s_setprio 3\n\t"
    "ds_read_b64_tr_b16 v[136:137], v204 offset:0\n\t"
    "ds_read_b64_tr_b16 v[138:139], v204 offset:512\n\t"
    "ds_read_b64_tr_b16 v[140:141], v204 offset:8192\n\t"
    "ds_read_b64_tr_b16 v[142:143], v204 offset:8704\n\t"
    "v_mfma_f32_16x16x32_bf16 v[100:103], v[152:155], v[184:187], v[100:103]\n\t"
    "ds_read_b64_tr_b16 v[144:145], v205 offset:0\n\t"
    "v_mfma_f32_16x16x32_bf16 v[108:111], v[152:155], v[188:191], v[108:111]\n\t"
    "ds_read_b64_tr_b16 v[146:147], v205 offset:512\n\t"
    "s_add_u32 m0, s61, 98304\n\t"
    "s_mov_b32 s63, 3670016\n\t"
    "buffer_load_dwordx4 v216, s[40:43], s63 offen lds\n\t"
    "v_mfma_f32_16x16x32_bf16 v[100:103], v[156:159], v[192:195], v[100:103]\n\t"
    "ds_read_b64_tr_b16 v[148:149], v205 offset:8192\n\t"
    "v_mfma_f32_16x16x32_bf16 v[108:111], v[156:159], v[196:199], v[108:111]\n\t"
    "ds_read_b64_tr_b16 v[150:151], v205 offset:8704\n\t"
    "s_add_u32 m0, s61, 99328\n\t"
    "s_mov_b32 s63, 3670144\n\t"
    "buffer_load_dwordx4 v216, s[40:43], s63 offen lds\n\t"
    "v_mfma_f32_16x16x32_bf16 v[112:115], v[160:163], v[184:187], v[112:115]\n\t"
    "ds_read_b64_tr_b16 v[152:153], v206 offset:0\n\t"
    "v_mfma_f32_16x16x32_bf16 v[120:123], v[160:163], v[188:191], v[120:123]\n\t"
    "ds_read_b64_tr_b16 v[154:155], v206 offset:512\n\t"
    "s_add_u32 m0, s60, 32768\n\t"
    "s_mov_b32 s63, 16384\n\t"
    "buffer_load_dwordx4 v243, s[48:51], s63 offen nt lds\n\t"
    "v_mfma_f32_16x16x32_bf16 v[112:115], v[164:167], v[192:195], v[112:115]\n\t"
    "ds_read_b64_tr_b16 v[156:157], v206 offset:8192\n\t"
    "v_mfma_f32_16x16x32_bf16 v[120:123], v[164:167], v[196:199], v[120:123]\n\t"
    "ds_read_b64_tr_b16 v[158:159], v206 offset:8704\n\t"
    "s_add_u32 m0, s60, 40960\n\t"
    "s_mov_b32 s63, 24576\n\t"
    "buffer_load_dwordx4 v243, s[48:51], s63 offen nt lds\n\t"
    "v_mfma_f32_16x16x32_bf16 v[116:119], v[128:131], v[184:187], v[116:119]\n\t"
    "ds_read_b64_tr_b16 v[160:161], v207 offset:0\n\t"
    "v_mfma_f32_16x16x32_bf16 v[124:127], v[128:131], v[188:191], v[124:127]\n\t"
    "ds_read_b64_tr_b16 v[162:163], v207 offset:512\n\t"
    "s_add_u32 m0, s60, 49152\n\t"
    "s_mov_b32 s63, 32768\n\t"
    "buffer_load_dwordx4 v243, s[48:51], s63 offen nt lds\n\t"
    "v_mfma_f32_16x16x32_bf16 v[116:119], v[132:135], v[192:195], v[116:119]\n\t"
    "ds_read_b64_tr_b16 v[164:165], v207 offset:8192\n\t"
    "v_mfma_f32_16x16x32_bf16 v[124:127], v[132:135], v[196:199], v[124:127]\n\t"
    "ds_read_b64_tr_b16 v[166:167], v207 offset:8704\n\t"
    "s_add_u32 m0, s60, 57344\n\t"
    "s_mov_b32 s63, 40960\n\t"
    "buffer_load_dwordx4 v243, s[48:51], s63 offen nt lds\n\t"
    "s_waitcnt lgkmcnt(12)\n\t"
    "v_mfma_f32_16x16x32_bf16 v[0:3], v[136:139], v[168:171], v[0:3]\n\t"
    "ds_read_b64_tr_b16 v[128:129], v204 offset:1024\n\t"
    "s_setprio 2\n\t"
    "v_mfma_f32_16x16x32_bf16 v[8:11], v[136:139], v[172:175], v[8:11]\n\t"
    "ds_read_b64_tr_b16 v[130:131], v204 offset:1536\n\t"
    "v_mfma_f32_16x16x32_bf16 v[0:3], v[140:143], v[176:179], v[0:3]\n\t"
    "ds_read_b64_tr_b16 v[132:133], v204 offset:9216\n\t"
    "v_mfma_f32_16x16x32_bf16 v[8:11], v[140:143], v[180:183], v[8:11]\n\t"
    "ds_read_b64_tr_b16 v[134:135], v204 offset:9728\n\t"
    "s_waitcnt lgkmcnt(12)\n\t"
    "v_mfma_f32_16x16x32_bf16 v[4:7], v[144:147], v[168:171], v[4:7]\n\t"
    "ds_read_b64_tr_b16 v[136:137], v205 offset:1024\n\t"
    "v_mfma_f32_16x16x32_bf16 v[12:15], v[144:147], v[172:175], v[12:15]\n\t"
    "ds_read_b64_tr_b16 v[138:139], v205 offset:1536\n\t"
    "v_mfma_f32_16x16x32_bf16 v[4:7], v[148:151], v[176:179], v[4:7]\n\t"
    "ds_read_b64_tr_b16 v[140:141], v205 offset:9216\n\t"
    "v_mfma_f32_16x16x32_bf16 v[12:15], v[148:151], v[180:183], v[12:15]\n\t"
    "ds_read_b64_tr_b16 v[142:143], v205 offset:9728\n\t"
    "s_waitcnt lgkmcnt(12)\n\t"
    "v_mfma_f32_16x16x32_bf16 v[16:19], v[152:155], v[168:171], v[16:19]\n\t"
    "ds_read_b64_tr_b16 v[144:145], v206 offset:1024\n\t"
    "s_setprio 1\n\t"
    "v_mfma_f32_16x16x32_bf16 v[24:27], v[152:155], v[172:175], v[24:27]\n\t"
    "ds_read_b64_tr_b16 v[146:147], v206 offset:1536\n\t"
    "v_mfma_f32_16x16x32_bf16 v[16:19], v[156:159], v[176:179], v[16:19]\n\t"
    "ds_read_b64_tr_b16 v[148:149], v206 offset:9216\n\t"
    "v_mfma_f32_16x16x32_bf16 v[24:27], v[156:159], v[180:183], v[24:27]\n\t"
    "ds_read_b64_tr_b16 v[150:151], v206 offset:9728\n\t"
    "s_waitcnt lgkmcnt(12)\n\t"
    "v_mfma_f32_16x16x32_bf16 v[20:23], v[160:163], v[168:171], v[20:23]\n\t"
    "ds_read_b64_tr_b16 v[152:153], v207 offset:1024\n\t"
    "v_mfma_f32_16x16x32_bf16 v[28:31], v[160:163], v[172:175], v[28:31]\n\t"
    "ds_read_b64_tr_b16 v[154:155], v207 offset:1536\n\t"
    "v_mfma_f32_16x16x32_bf16 v[20:23], v[164:167], v[176:179], v[20:23]\n\t"
    "ds_read_b64_tr_b16 v[156:157], v207 offset:9216\n\t"
    "s_setprio 0\n\t"
    "v_mfma_f32_16x16x32_bf16 v[28:31], v[164:167], v[180:183], v[28:31]\n\t"
    "ds_read_b64_tr_b16 v[158:159], v207 offset:9728\n\t"
    "s_waitcnt lgkmcnt(12)\n\t"
    "v_mfma_f32_16x16x32_bf16 v[32:35], v[128:131], v[168:171], v[32:35]\n\t"
    "v_mfma_f32_16x16x32_bf16 v[40:43], v[128:131], v[172:175], v[40:43]\n\t"
    "v_mfma_f32_16x16x32_bf16 v[32:35], v[132:135], v[176:179], v[32:35]\n\t"
    "v_mfma_f32_16x16x32_bf16 v[40:43], v[132:135], v[180:183], v[40:43]\n\t"
    "s_waitcnt vmcnt(13) lgkmcnt(0)\n\t"
    "s_barrier\n\t"
    "s_setprio 3\n\t"
    "ds_read_b64_tr_b16 v[160:161], v204 offset:16384\n\t"
    "ds_read_b64_tr_b16 v[162:163], v204 offset:16896\n\t"
    "ds_read_b64_tr_b16 v[164:165], v204 offset:24576\n\t"
    "ds_read_b64_tr_b16 v[166:167], v204 offset:25088\n\t"
    "v_mfma_f32_16x16x32_bf16 v[36:39], v[136:139], v[168:171], v[36:39]\n\t"
    "ds_read_b64_tr_b16 v[128:129], v205 offset:16384\n\t"
    "v_mfma_f32_16x16x32_bf16 v[44:47], v[136:139], v[172:175], v[44:47]\n\t"
    "ds_read_b64_tr_b16 v[130:131], v205 offset:16896\n\t"
    "v_mfma_f32_16x16x32_bf16 v[36:39], v[140:143], v[176:179], v[36:39]\n\t"
    "ds_read_b64_tr_b16 v[132:133], v205 offset:24576\n\t"
    "v_mfma_f32_16x16x32_bf16 v[44:47], v[140:143], v[180:183], v[44:47]\n\t"
    "ds_read_b64_tr_b16 v[134:135], v205 offset:25088\n\t"
    "v_mfma_f32_16x16x32_bf16 v[48:51], v[144:147], v[168:171], v[48:51]\n\t"
    "ds_read_b64_tr_b16 v[136:137], v206 offset:16384\n\t"
    "s_mov_b32 s62, s71\n\t"
    "v_mfma_f32_16x16x32_bf16 v[56:59], v[144:147], v[172:175], v[56:59]\n\t"
    "ds_read_b64_tr_b16 v[138:139], v206 offset:16896\n\t"
    "v_mfma_f32_16x16x32_bf16 v[48:51], v[148:151], v[176:179], v[48:51]\n\t"
    "ds_read_b64_tr_b16 v[140:141], v206 offset:24576\n\t"
    "v_mfma_f32_16x16x32_bf16 v[56:59], v[148:151], v[180:183], v[56:59]\n\t"
    "ds_read_b64_tr_b16 v[142:143], v206 offset:25088\n\t"
    "v_mfma_f32_16x16x32_bf16 v[52:55], v[152:155], v[168:171], v[52:55]\n\t"
    "ds_read_b64_tr_b16 v[144:145], v207 offset:16384\n\t"
    "v_mfma_f32_16x16x32_bf16 v[60:63], v[152:155], v[172:175], v[60:63]\n\t"
    "ds_read_b64_tr_b16 v[146:147], v207 offset:16896\n\t"
    "s_add_u32 m0, s60, 131072\n\t"
    "s_mov_b32 s63, 16384\n\t"
    "buffer_load_dwordx4 v242, s[44:47], s63 offen nt lds\n\t"
    "v_mfma_f32_16x16x32_bf16 v[52:55], v[156:159], v[176:179], v[52:55]\n\t"
    "ds_read_b64_tr_b16 v[148:149], v207 offset:24576\n\t"
    "v_mfma_f32_16x16x32_bf16 v[60:63], v[156:159], v[180:183], v[60:63]\n\t"
    "ds_read_b64_tr_b16 v[150:151], v207 offset:25088\n\t"
    "s_waitcnt lgkmcnt(12)\n\t"
    "v_mfma_f32_16x16x32_bf16 v[64:67], v[160:163], v[168:171], v[64:67]\n\t"
    "ds_read_b64_tr_b16 v[152:153], v204 offset:17408\n\t"
    "s_setprio 2\n\t"
    "v_mfma_f32_16x16x32_bf16 v[72:75], v[160:163], v[172:175], v[72:75]\n\t"
    "ds_read_b64_tr_b16 v[154:155], v204 offset:17920\n\t"
    "v_mfma_f32_16x16x32_bf16 v[64:67], v[164:167], v[176:179], v[64:67]\n\t"
    "ds_read_b64_tr_b16 v[156:157], v204 offset:25600\n\t"
    "v_mfma_f32_16x16x32_bf16 v[72:75], v[164:167], v[180:183], v[72:75]\n\t"
    "ds_read_b64_tr_b16 v[158:159], v204 offset:26112\n\t"
    "s_waitcnt lgkmcnt(12)\n\t"
    "v_mfma_f32_16x16x32_bf16 v[68:71], v[128:131], v[168:171], v[68:71]\n\t"
    "ds_read_b64_tr_b16 v[160:161], v205 offset:17408\n\t"
    "s_waitcnt vmcnt(10)\n\t"
    "ds_read_b128 v[184:187], v208 offset:0\n\t"
    "v_mfma_f32_16x16x32_bf16 v[76:79], v[128:131], v[172:175], v[76:79]\n\t"
    "ds_read_b64_tr_b16 v[162:163], v205 offset:17920\n\t"
    "ds_read_b128 v[188:191], v208 offset:2048\n\t"
    "v_mfma_f32_16x16x32_bf16 v[68:71], v[132:135], v[176:179], v[68:71]\n\t"
    "ds_read_b64_tr_b16 v[164:165], v205 offset:25600\n\t"
    "v_mfma_f32_16x16x32_bf16 v[76:79], v[132:135], v[180:183], v[76:79]\n\t"
    "ds_read_b64_tr_b16 v[166:167], v205 offset:26112\n\t"
    "s_add_u32 m0, s60, 139264\n\t"
    "s_mov_b32 s63, 24576\n\t"
    "buffer_load_dwordx4 v242, s[44:47], s63 offen nt lds\n\t"
    "s_waitcnt lgkmcnt(14)\n\t"
    "v_mfma_f32_16x16x32_bf16 v[80:83], v[136:139], v[168:171], v[80:83]\n\t"
    "ds_read_b64_tr_b16 v[128:129], v206 offset:17408\n\t"
    "s_setprio 1\n\t"
    "ds_read_b128 v[192:195], v209 offset:0\n\t"
    "v_mfma_f32_16x16x32_bf16 v[88:91], v[136:139], v[172:175], v[88:91]\n\t"
    "ds_read_b64_tr_b16 v[130:131], v206 offset:17920\n\t"
    "ds_read_b128 v[196:199], v209 offset:2048\n\t"
    "v_mfma_f32_16x16x32_bf16 v[80:83], v[140:143], v[176:179], v[80:83]\n\t"
    "ds_read_b64_tr_b16 v[132:133], v206 offset:25600\n\t"
    "v_mfma_f32_16x16x32_bf16 v[88:91], v[140:143], v[180:183], v[88:91]\n\t"
    "ds_read_b64_tr_b16 v[134:135], v206 offset:26112\n\t"
    "s_waitcnt lgkmcnt(15)\n\t"
    "v_mfma_f32_16x16x32_bf16 v[84:87], v[144:147], v[168:171], v[84:87]\n\t"
    "ds_read_b64_tr_b16 v[136:137], v207 offset:17408\n\t"
    "v_mfma_f32_16x16x32_bf16 v[92:95], v[144:147], v[172:175], v[92:95]\n\t"
    "ds_read_b64_tr_b16 v[138:139], v207 offset:17920\n\t"
    "v_mfma_f32_16x16x32_bf16 v[84:87], v[148:151], v[176:179], v[84:87]\n\t"
    "ds_read_b64_tr_b16 v[140:141], v207 offset:25600\n\t"
    "s_setprio 0\n\t"
    "v_mfma_f32_16x16x32_bf16 v[92:95], v[148:151], v[180:183], v[92:95]\n\t"
    "ds_read_b64_tr_b16 v[142:143], v207 offset:26112\n\t"
    "s_waitcnt lgkmcnt(15)\n\t"
    "v_mfma_f32_16x16x32_bf16 v[96:99], v[152:155], v[168:171], v[96:99]\n\t"
    "v_mfma_f32_16x16x32_bf16 v[104:107], v[152:155], v[172:175], v[104:107]\n\t"
    "v_mfma_f32_16x16x32_bf16 v[96:99], v[156:159], v[176:179], v[96:99]\n\t"
    "v_mfma_f32_16x16x32_bf16 v[104:107], v[156:159], v[180:183], v[104:107]\n\t"
    "s_waitcnt vmcnt(9) lgkmcnt(0)\n\t"
    "s_barrier\n\t"
    "s_setprio 3\n\t"
    "ds_read_b64_tr_b16 v[144:145], v200 offset:0\n\t"
    "ds_read_b64_tr_b16 v[146:147], v200 offset:512\n\t"
    "ds_read_b64_tr_b16 v[148:149], v200 offset:8192\n\t"
    "ds_read_b64_tr_b16 v[150:151], v200 offset:8704\n\t"
    "v_mfma_f32_16x16x32_bf16 v[100:103], v[160:163], v[168:171], v[100:103]\n\t"
    "ds_read_b64_tr_b16 v[152:153], v201 offset:0\n\t"
    "v_mfma_f32_16x16x32_bf16 v[108:111], v[160:163], v[172:175], v[108:111]\n\t"
    "ds_read_b64_tr_b16 v[154:155], v201 offset:512\n\t"
    "v_mfma_f32_16x16x32_bf16 v[100:103], v[164:167], v[176:179], v[100:103]\n\t"
    "ds_read_b64_tr_b16 v[156:157], v201 offset:8192\n\t"
    "v_mfma_f32_16x16x32_bf16 v[108:111], v[164:167], v[180:183], v[108:111]\n\t"
    "ds_read_b64_tr_b16 v[158:159], v201 offset:8704\n\t"
    "v_mfma_f32_16x16x32_bf16 v[112:115], v[128:131], v[168:171], v[112:115]\n\t"
    "ds_read_b64_tr_b16 v[160:161], v202 offset:0\n\t"
    "v_mfma_f32_16x16x32_bf16 v[120:123], v[128:131], v[172:175], v[120:123]\n\t"
    "ds_read_b64_tr_b16 v[162:163], v202 offset:512\n\t"
    "v_mfma_f32_16x16x32_bf16 v[112:115], v[132:135], v[176:179], v[112:115]\n\t"
    "ds_read_b64_tr_b16 v[164:165], v202 offset:8192\n\t"
    "v_mfma_f32_16x16x32_bf16 v[120:123], v[132:135], v[180:183], v[120:123]\n\t"
    "ds_read_b64_tr_b16 v[166:167], v202 offset:8704\n\t"
    "v_mfma_f32_16x16x32_bf16 v[116:119], v[136:139], v[168:171], v[116:119]\n\t"
    "ds_read_b64_tr_b16 v[128:129], v203 offset:0\n\t"
    "v_mfma_f32_16x16x32_bf16 v[124:127], v[136:139], v[172:175], v[124:127]\n\t"
    "ds_read_b64_tr_b16 v[130:131], v203 offset:512\n\t"
    "s_add_u32 m0, s60, 147456\n\t"
    "s_mov_b32 s63, 32768\n\t"
    "buffer_load_dwordx4 v242, s[44:47], s63 offen nt lds\n\t"
    "v_mfma_f32_16x16x32_bf16 v[116:119], v[140:143], v[176:179], v[116:119]\n\t"
    "ds_read_b64_tr_b16 v[132:133], v203 offset:8192\n\t"
    "v_mfma_f32_16x16x32_bf16 v[124:127], v[140:143], v[180:183], v[124:127]\n\t"
    "ds_read_b64_tr_b16 v[134:135], v203 offset:8704\n\t"
    "s_waitcnt lgkmcnt(12)\n\t"
    "v_mfma_f32_16x16x32_bf16 v[0:3], v[144:147], v[184:187], v[0:3]\n\t"
    "ds_read_b64_tr_b16 v[136:137], v200 offset:1024\n\t"
    "s_setprio 2\n\t"
    "v_mfma_f32_16x16x32_bf16 v[8:11], v[144:147], v[188:191], v[8:11]\n\t"
    "ds_read_b64_tr_b16 v[138:139], v200 offset:1536\n\t"
    "v_mfma_f32_16x16x32_bf16 v[0:3], v[148:151], v[192:195], v[0:3]\n\t"
    "ds_read_b64_tr_b16 v[140:141], v200 offset:9216\n\t"
    "v_mfma_f32_16x16x32_bf16 v[8:11], v[148:151], v[196:199], v[8:11]\n\t"
    "ds_read_b64_tr_b16 v[142:143], v200 offset:9728\n\t"
    "s_waitcnt lgkmcnt(12)\n\t"
    "v_mfma_f32_16x16x32_bf16 v[4:7], v[152:155], v[184:187], v[4:7]\n\t"
    "ds_read_b64_tr_b16 v[144:145], v201 offset:1024\n\t"
    "v_mfma_f32_16x16x32_bf16 v[12:15], v[152:155], v[188:191], v[12:15]\n\t"
    "ds_read_b64_tr_b16 v[146:147], v201 offset:1536\n\t"
    "v_mfma_f32_16x16x32_bf16 v[4:7], v[156:159], v[192:195], v[4:7]\n\t"
    "ds_read_b64_tr_b16 v[148:149], v201 offset:9216\n\t"
    "v_mfma_f32_16x16x32_bf16 v[12:15], v[156:159], v[196:199], v[12:15]\n\t"
    "ds_read_b64_tr_b16 v[150:151], v201 offset:9728\n\t"
    "s_add_u32 m0, s60, 155648\n\t"
    "s_mov_b32 s63, 40960\n\t"
    "buffer_load_dwordx4 v242, s[44:47], s63 offen nt lds\n\t"
    "s_waitcnt lgkmcnt(12)\n\t"
    "v_mfma_f32_16x16x32_bf16 v[16:19], v[160:163], v[184:187], v[16:19]\n\t"
    "ds_read_b64_tr_b16 v[152:153], v202 offset:1024\n\t"
    "s_setprio 1\n\t"
    "v_mfma_f32_16x16x32_bf16 v[24:27], v[160:163], v[188:191], v[24:27]\n\t"
    "ds_read_b64_tr_b16 v[154:155], v202 offset:1536\n\t"
    "v_mfma_f32_16x16x32_bf16 v[16:19], v[164:167], v[192:195], v[16:19]\n\t"
    "ds_read_b64_tr_b16 v[156:157], v202 offset:9216\n\t"
    "v_mul_f32 v0, 0x3e0293ee, v0\n\t"
    "v_mul_f32 v1, 0x3e0293ee, v1\n\t"
    "v_cvt_pk_bf16_f32 v0, v0, v1\n\t"
    "v_mul_f32 v2, 0x3e0293ee, v2\n\t"
    "v_mfma_f32_16x16x32_bf16 v[24:27], v[164:167], v[196:199], v[24:27]\n\t"
    "ds_read_b64_tr_b16 v[158:159], v202 offset:9728\n\t"
    "v_mul_f32 v3, 0x3e0293ee, v3\n\t"
    "v_cvt_pk_bf16_f32 v1, v2, v3\n\t"
    "v_mul_f32 v4, 0x3e0293ee, v4\n\t"
    "v_mul_f32 v5, 0x3e0293ee, v5\n\t"
    "s_waitcnt lgkmcnt(12)\n\t"
    "v_mfma_f32_16x16x32_bf16 v[20:23], v[128:131], v[184:187], v[20:23]\n\t"
    "ds_read_b64_tr_b16 v[160:161], v203 offset:1024\n\t"
    "v_cvt_pk_bf16_f32 v2, v4, v5\n\t"
    "v_mul_f32 v6, 0x3e0293ee, v6\n\t"
    "v_mul_f32 v7, 0x3e0293ee, v7\n\t"
    "v_cvt_pk_bf16_f32 v3, v6, v7\n\t"
    "v_mfma_f32_16x16x32_bf16 v[28:31], v[128:131], v[188:191], v[28:31]\n\t"
    "ds_read_b64_tr_b16 v[162:163], v203 offset:1536\n\t"
    "v_mul_f32 v8, 0x3e0293ee, v8\n\t"
    "v_mul_f32 v9, 0x3e0293ee, v9\n\t"
    "v_cvt_pk_bf16_f32 v4, v8, v9\n\t"
    "v_mul_f32 v10, 0x3e0293ee, v10\n\t"
    "v_mfma_f32_16x16x32_bf16 v[20:23], v[132:135], v[192:195], v[20:23]\n\t"
    "ds_read_b64_tr_b16 v[164:165], v203 offset:9216\n\t"
    "s_setprio 0\n\t"
    "v_mul_f32 v11, 0x3e0293ee, v11\n\t"
    "v_cvt_pk_bf16_f32 v5, v10, v11\n\t"
    "v_mul_f32 v12, 0x3e0293ee, v12\n\t"
    "v_mul_f32 v13, 0x3e0293ee, v13\n\t"
    "v_mfma_f32_16x16x32_bf16 v[28:31], v[132:135], v[196:199], v[28:31]\n\t"
    "ds_read_b64_tr_b16 v[166:167], v203 offset:9728\n\t"
    "v_cvt_pk_bf16_f32 v6, v12, v13\n\t"
    "PAD2_%=: s_cbranch_execz PAD10_%=\n\t"
    "v_mul_f32 v14, 0x3e0293ee, v14\n\t"
    "v_mul_f32 v15, 0x3e0293ee, v15\n\t"
    "v_cvt_pk_bf16_f32 v7, v14, v15\n\t"
    "s_waitcnt lgkmcnt(12)\n\t"
    "v_mfma_f32_16x16x32_bf16 v[32:35], v[136:139], v[184:187], v[32:35]\n\t"
    "v_permlane32_swap_b32 v0, v2\n\t"
    "v_permlane32_swap_b32 v1, v3\n\t"
    "v_permlane32_swap_b32 v4, v6\n\t"
    "v_permlane32_swap_b32 v5, v7\n\t"
    "v_mfma_f32_16x16x32_bf16 v[40:43], v[136:139], v[188:191], v[40:43]\n\t"
    "v_permlane16_swap_b32 v0, v2\n\t"
    "v_permlane16_swap_b32 v1, v3\n\t"
    "v_permlane16_swap_b32 v4, v6\n\t"
    "v_permlane16_swap_b32 v5, v7\n\t"
    "v_mfma_f32_16x16x32_bf16 v[32:35], v[140:143], v[192:195], v[32:35]\n\t"
    "v_mul_f32 v16, 0x3e0293ee, v16\n\t"
    "v_mul_f32 v17, 0x3e0293ee, v17\n\t"
    "v_cvt_pk_bf16_f32 v16, v16, v17\n\t"
    "v_mul_f32 v18, 0x3e0293ee, v18\n\t"
    "v_mul_f32 v19, 0x3e0293ee, v19\n\t"
    "v_cvt_pk_bf16_f32 v17, v18, v19\n\t"
    "v_mul_f32 v20, 0x3e0293ee, v20\n\t"
    "v_mfma_f32_16x16x32_bf16 v[40:43], v[140:143], v[196:199], v[40:43]\n\t"
    "s_waitcnt vmcnt(8) lgkmcnt(0)\n\t"
    "s_barrier\n\t"
    "s_setprio 3\n\t"
    "ds_read_b64_tr_b16 v[128:129], v200 offset:16384\n\t"
    "ds_read_b64_tr_b16 v[130:131], v200 offset:16896\n\t"
    "ds_read_b64_tr_b16 v[132:133], v200 offset:24576\n\t"
    "ds_read_b64_tr_b16 v[134:135], v200 offset:25088\n\t"
    "v_mul_f32 v21, 0x3e0293ee, v21\n\t"
    "v_mfma_f32_16x16x32_bf16 v[36:39], v[144:147], v[184:187], v[36:39]\n\t"
    "ds_read_b64_tr_b16 v[136:137], v201 offset:16384\n\t"
    "v_cvt_pk_bf16_f32 v18, v20, v21\n\t"
    "v_mul_f32 v22, 0x3e0293ee, v22\n\t"
    "v_mul_f32 v23, 0x3e0293ee, v23\n\t"
    "v_cvt_pk_bf16_f32 v19, v22, v23\n\t"
    "v_mfma_f32_16x16x32_bf16 v[44:47], v[144:147], v[188:191], v[44:47]\n\t"
    "ds_read_b64_tr_b16 v[138:139], v201 offset:16896\n\t"
    "v_mul_f32 v24, 0x3e0293ee, v24\n\t"
    "v_mul_f32 v25, 0x3e0293ee, v25\n\t"
    "v_cvt_pk_bf16_f32 v20, v24, v25\n\t"
    "v_mul_f32 v26, 0x3e0293ee, v26\n\t"
    "v_mfma_f32_16x16x32_bf16 v[36:39], v[148:151], v[192:195], v[36:39]\n\t"
    "ds_read_b64_tr_b16 v[140:141], v201 offset:24576\n\t"
    "v_mul_f32 v27, 0x3e0293ee, v27\n\t"
    "v_cvt_pk_bf16_f32 v21, v26, v27\n\t"
    "v_mul_f32 v28, 0x3e0293ee, v28\n\t"
    "v_mul_f32 v29, 0x3e0293ee, v29\n\t"
    "v_mfma_f32_16x16x32_bf16 v[44:47], v[148:151], v[196:199], v[44:47]\n\t"
    "ds_read_b64_tr_b16 v[142:143], v201 offset:25088\n\t"
    "v_cvt_pk_bf16_f32 v22, v28, v29\n\t"
    "v_mul_f32 v30, 0x3e0293ee, v30\n\t"
    "v_mul_f32 v31, 0x3e0293ee, v31\n\t"
    "v_cvt_pk_bf16_f32 v23, v30, v31\n\t"
    "v_mfma_f32_16x16x32_bf16 v[48:51], v[152:155], v[184:187], v[48:51]\n\t"
    "ds_read_b64_tr_b16 v[144:145], v202 offset:16384\n\t"
    "v_permlane32_swap_b32 v16, v18\n\t"
    "v_permlane32_swap_b32 v17, v19\n\t"
    "v_permlane32_swap_b32 v20, v22\n\t"
    "v_permlane32_swap_b32 v21, v23\n\t"
    "v_mfma_f32_16x16x32_bf16 v[56:59], v[152:155], v[188:191], v[56:59]\n\t"
    "ds_read_b64_tr_b16 v[146:147], v202 offset:16896\n\t"
    "v_permlane16_swap_b32 v16, v18\n\t"
    "v_permlane16_swap_b32 v17, v19\n\t"
    "v_permlane16_swap_b32 v20, v22\n\t"
    "v_permlane16_swap_b32 v21, v23\n\t"
    "v_mfma_f32_16x16x32_bf16 v[48:51], v[156:159], v[192:195], v[48:51]\n\t"
    "ds_read_b64_tr_b16 v[148:149], v202 offset:24576\n\t"
    "v_mul_f32 v32, 0x3e0293ee, v32\n\t"
    "v_mul_f32 v33, 0x3e0293ee, v33\n\t"
    "v_cvt_pk_bf16_f32 v32, v32, v33\n\t"
    "v_mul_f32 v34, 0x3e0293ee, v34\n\t"
    "v_mfma_f32_16x16x32_bf16 v[56:59], v[156:159], v[196:199], v[56:59]\n\t"
    "ds_read_b64_tr_b16 v[150:151], v202 offset:25088\n\t"
    "v_mul_f32 v35, 0x3e0293ee, v35\n\t"
    "v_cvt_pk_bf16_f32 v33, v34, v35\n\t"
    "v_mul_f32 v36, 0x3e0293ee, v36\n\t"
    "v_mul_f32 v37, 0x3e0293ee, v37\n\t"
    "v_mfma_f32_16x16x32_bf16 v[52:55], v[160:163], v[184:187], v[52:55]\n\t"
    "ds_read_b64_tr_b16 v[152:153], v203 offset:16384\n\t"
    "v_cvt_pk_bf16_f32 v34, v36, v37\n\t"
    "v_mul_f32 v38, 0x3e0293ee, v38\n\t"
    "v_mul_f32 v39, 0x3e0293ee, v39\n\t"
    "v_cvt_pk_bf16_f32 v35, v38, v39\n\t"
    "v_mfma_f32_16x16x32_bf16 v[60:63], v[160:163], v[188:191], v[60:63]\n\t"
    "ds_read_b64_tr_b16 v[154:155], v203 offset:16896\n\t"
    "s_add_u32 m0, s60, 81920\n\t"
    "s_mov_b32 s63, 49152\n\t"
    "buffer_load_dwordx4 v243, s[48:51], s63 offen nt lds\n\t"
    "v_mul_f32 v40, 0x3e0293ee, v40\n\t"
    "v_mul_f32 v41, 0x3e0293ee, v41\n\t"
    "v_cvt_pk_bf16_f32 v36, v40, v41\n\t"
    "v_mul_f32 v42, 0x3e0293ee, v42\n\t"
    "v_mfma_f32_16x16x32_bf16 v[52:55], v[164:167], v[192:195], v[52:55]\n\t"
    "ds_read_b64_tr_b16 v[156:157], v203 offset:24576\n\t"
    "v_mul_f32 v43, 0x3e0293ee, v43\n\t"
    "v_cvt_pk_bf16_f32 v37, v42, v43\n\t"
    "v_mul_f32 v44, 0x3e0293ee, v44\n\t"
    "v_mul_f32 v45, 0x3e0293ee, v45\n\t"
    "v_mfma_f32_16x16x32_bf16 v[60:63], v[164:167], v[196:199], v[60:63]\n\t"
    "ds_read_b64_tr_b16 v[158:159], v203 offset:25088\n\t"
    "v_cvt_pk_bf16_f32 v38, v44, v45\n\t"
    "v_mul_f32 v46, 0x3e0293ee, v46\n\t"
    "v_mul_f32 v47, 0x3e0293ee, v47\n\t"
    "v_cvt_pk_bf16_f32 v39, v46, v47\n\t"
    "s_waitcnt lgkmcnt(12)\n\t"
    "v_mfma_f32_16x16x32_bf16 v[64:67], v[128:131], v[184:187], v[64:67]\n\t"
    "ds_read_b64_tr_b16 v[160:161], v200 offset:17408\n\t"
    "s_setprio 2\n\t"
    "v_permlane32_swap_b32 v32, v34\n\t"
    "v_permlane32_swap_b32 v33, v35\n\t"
    "v_permlane32_swap_b32 v36, v38\n\t"
    "v_permlane32_swap_b32 v37, v39\n\t"
    "v_mfma_f32_16x16x32_bf16 v[72:75], v[128:131], v[188:191], v[72:75]\n\t"
    "ds_read_b64_tr_b16 v[162:163], v200 offset:17920\n\t"
    "v_permlane16_swap_b32 v32, v34\n\t"
    "v_permlane16_swap_b32 v33, v35\n\t"
    "v_permlane16_swap_b32 v36, v38\n\t"
    "v_permlane16_swap_b32 v37, v39\n\t"
    "v_mfma_f32_16x16x32_bf16 v[64:67], v[132:135], v[192:195], v[64:67]\n\t"
    "ds_read_b64_tr_b16 v[164:165], v200 offset:25600\n\t"
    "v_mul_f32 v48, 0x3e0293ee, v48\n\t"
    "v_mul_f32 v49, 0x3e0293ee, v49\n\t"
    "v_cvt_pk_bf16_f32 v48, v48, v49\n\t"
    "v_mul_f32 v50, 0x3e0293ee, v50\n\t"
    "v_mfma_f32_16x16x32_bf16 v[72:75], v[132:135], v[196:199], v[72:75]\n\t"
    "ds_read_b64_tr_b16 v[166:167], v200 offset:26112\n\t"
    "v_mul_f32 v51, 0x3e0293ee, v51\n\t"
    "v_cvt_pk_bf16_f32 v49, v50, v51\n\t"
    "v_mul_f32 v52, 0x3e0293ee, v52\n\t"
    "v_mul_f32 v53, 0x3e0293ee, v53\n\t"
    "s_waitcnt lgkmcnt(12)\n\t"
    "v_mfma_f32_16x16x32_bf16 v[68:71], v[136:139], v[184:187], v[68:71]\n\t"
    "ds_read_b64_tr_b16 v[128:129], v201 offset:17408\n\t"
    "v_cvt_pk_bf16_f32 v50, v52, v53\n\t"
    "v_mul_f32 v54, 0x3e0293ee, v54\n\t"
    "v_mul_f32 v55, 0x3e0293ee, v55\n\t"
    "v_cvt_pk_bf16_f32 v51, v54, v55\n\t"
    "v_mfma_f32_16x16x32_bf16 v[76:79], v[136:139], v[188:191], v[76:79]\n\t"
    "ds_read_b64_tr_b16 v[130:131], v201 offset:17920\n\t"
    "v_mul_f32 v56, 0x3e0293ee, v56\n\t"
    "v_mul_f32 v57, 0x3e0293ee, v57\n\t"
    "v_cvt_pk_bf16_f32 v52, v56, v57\n\t"
    "v_mul_f32 v58, 0x3e0293ee, v58\n\t"
    "v_mfma_f32_16x16x32_bf16 v[68:71], v[140:143], v[192:195], v[68:71]\n\t"
    "ds_read_b64_tr_b16 v[132:133], v201 offset:25600\n\t"
    "v_mul_f32 v59, 0x3e0293ee, v59\n\t"
    "v_cvt_pk_bf16_f32 v53, v58, v59\n\t"
    "v_mul_f32 v60, 0x3e0293ee, v60\n\t"
    "v_mul_f32 v61, 0x3e0293ee, v61\n\t"
    "v_mfma_f32_16x16x32_bf16 v[76:79], v[140:143], v[196:199], v[76:79]\n\t"
    "ds_read_b64_tr_b16 v[134:135], v201 offset:26112\n\t"
    "s_add_u32 m0, s60, 90112\n\t"
    "s_mov_b32 s63, 57344\n\t"
    "buffer_load_dwordx4 v243, s[48:51], s63 offen nt lds\n\t"
    "v_cvt_pk_bf16_f32 v54, v60, v61\n\t"
    "v_mul_f32 v62, 0x3e0293ee, v62\n\t"
    "v_mul_f32 v63, 0x3e0293ee, v63\n\t"
    "v_cvt_pk_bf16_f32 v55, v62, v63\n\t"
    "s_waitcnt lgkmcnt(12)\n\t"
    "v_mfma_f32_16x16x32_bf16 v[80:83], v[144:147], v[184:187], v[80:83]\n\t"
    "ds_read_b64_tr_b16 v[136:137], v202 offset:17408\n\t"
    "s_setprio 1\n\t"
    "v_permlane32_swap_b32 v48, v50\n\t"
    "v_permlane32_swap_b32 v49, v51\n\t"
    "v_permlane32_swap_b32 v52, v54\n\t"
    "v_permlane32_swap_b32 v53, v55\n\t"
    "v_mfma_f32_16x16x32_bf16 v[88:91], v[144:147], v[188:191], v[88:91]\n\t"
    "ds_read_b64_tr_b16 v[138:139], v202 offset:17920\n\t"
    "v_permlane16_swap_b32 v48, v50\n\t"
    "v_permlane16_swap_b32 v49, v51\n\t"
    "v_permlane16_swap_b32 v52, v54\n\t"
    "v_permlane16_swap_b32 v53, v55\n\t"
    "v_mfma_f32_16x16x32_bf16 v[80:83], v[148:151], v[192:195], v[80:83]\n\t"
    "ds_read_b64_tr_b16 v[140:141], v202 offset:25600\n\t"
    "v_mul_f32 v64, 0x3e0293ee, v64\n\t"
    "v_mul_f32 v65, 0x3e0293ee, v65\n\t"
    "v_cvt_pk_bf16_f32 v64, v64, v65\n\t"
    "v_mul_f32 v66, 0x3e0293ee, v66\n\t"
    "v_mfma_f32_16x16x32_bf16 v[88:91], v[148:151], v[196:199], v[88:91]\n\t"
    "ds_read_b64_tr_b16 v[142:143], v202 offset:26112\n\t"
    "v_mul_f32 v67, 0x3e0293ee, v67\n\t"
    "v_cvt_pk_bf16_f32 v65, v66, v67\n\t"
    "v_mul_f32 v68, 0x3e0293ee, v68\n\t"
    "v_mul_f32 v69, 0x3e0293ee, v69\n\t"
    "s_waitcnt lgkmcnt(12)\n\t"
    "v_mfma_f32_16x16x32_bf16 v[84:87], v[152:155], v[184:187], v[84:87]\n\t"
    "ds_read_b64_tr_b16 v[144:145], v203 offset:17408\n\t"
    "v_cvt_pk_bf16_f32 v66, v68, v69\n\t"
    "v_mul_f32 v70, 0x3e0293ee, v70\n\t"
    "v_mul_f32 v71, 0x3e0293ee, v71\n\t"
    "v_cvt_pk_bf16_f32 v67, v70, v71\n\t"
    "v_mfma_f32_16x16x32_bf16 v[92:95], v[152:155], v[188:191], v[92:95]\n\t"
    "ds_read_b64_tr_b16 v[146:147], v203 offset:17920\n\t"
    "v_mul_f32 v72, 0x3e0293ee, v72\n\t"
    "v_mul_f32 v73, 0x3e0293ee, v73\n\t"
    "v_cvt_pk_bf16_f32 v68, v72, v73\n\t"
    "v_mul_f32 v74, 0x3e0293ee, v74\n\t"
    "v_mfma_f32_16x16x32_bf16 v[84:87], v[156:159], v[192:195], v[84:87]\n\t"
    "ds_read_b64_tr_b16 v[148:149], v203 offset:25600\n\t"
    "s_setprio 0\n\t"
    "v_mul_f32 v75, 0x3e0293ee, v75\n\t"
    "v_cvt_pk_bf16_f32 v69, v74, v75\n\t"
    "v_mul_f32 v76, 0x3e0293ee, v76\n\t"
    "v_mul_f32 v77, 0x3e0293ee, v77\n\t"
    "v_mfma_f32_16x16x32_bf16 v[92:95], v[156:159], v[196:199], v[92:95]\n\t"
    "ds_read_b64_tr_b16 v[150:151], v203 offset:26112\n\t"
    "v_cvt_pk_bf16_f32 v70, v76, v77\n\t"
    "v_mul_f32 v78, 0x3e0293ee, v78\n\t"
    "v_mul_f32 v79, 0x3e0293ee, v79\n\t"
    "v_cvt_pk_bf16_f32 v71, v78, v79\n\t"
    "s_waitcnt lgkmcnt(12)\n\t"
    "v_mfma_f32_16x16x32_bf16 v[96:99], v[160:163], v[184:187], v[96:99]\n\t"
    "v_permlane32_swap_b32 v64, v66\n\t"
    "v_permlane32_swap_b32 v65, v67\n\t"
    "v_permlane32_swap_b32 v68, v70\n\t"
    "v_permlane32_swap_b32 v69, v71\n\t"
    "v_mfma_f32_16x16x32_bf16 v[104:107], v[160:163], v[188:191], v[104:107]\n\t"
    "v_permlane16_swap_b32 v64, v66\n\t"
    "v_permlane16_swap_b32 v65, v67\n\t"
    "v_permlane16_swap_b32 v68, v70\n\t"
    "v_permlane16_swap_b32 v69, v71\n\t"
    "v_mfma_f32_16x16x32_bf16 v[96:99], v[164:167], v[192:195], v[96:99]\n\t"
    "v_mul_f32 v80, 0x3e0293ee, v80\n\t"
    "v_mul_f32 v81, 0x3e0293ee, v81\n\t"
    "v_cvt_pk_bf16_f32 v80, v80, v81\n\t"
    "v_mul_f32 v82, 0x3e0293ee, v82\n\t"
    "v_mul_f32 v83, 0x3e0293ee, v83\n\t"
    "v_cvt_pk_bf16_f32 v81, v82, v83\n\t"
    "v_mfma_f32_16x16x32_bf16 v[104:107], v[164:167], v[196:199], v[104:107]\n\t"
    "s_waitcnt lgkmcnt(0)\n\t"
    "s_barrier\n\t"
    "s_setprio 3\n\t"
    "ds_read_b128 v[8:11], v240 offset:16384\n\t"
    "ds_read_b128 v[12:15], v240 offset:20480\n\t"
    "v_mul_f32 v84, 0x3e0293ee, v84\n\t"
    "v_mul_f32 v85, 0x3e0293ee, v85\n\t"
    "v_mfma_f32_16x16x32_bf16 v[100:103], v[128:131], v[184:187], v[100:103]\n\t"
    "ds_read_b128 v[24:27], v241 offset:16384\n\t"
    "v_cvt_pk_bf16_f32 v82, v84, v85\n\t"
    "v_mul_f32 v86, 0x3e0293ee, v86\n\t"
    "v_mul_f32 v87, 0x3e0293ee, v87\n\t"
    "v_cvt_pk_bf16_f32 v83, v86, v87\n\t"
    "v_mfma_f32_16x16x32_bf16 v[108:111], v[128:131], v[188:191], v[108:111]\n\t"
    "v_mul_f32 v88, 0x3e0293ee, v88\n\t"
    "v_mul_f32 v89, 0x3e0293ee, v89\n\t"
    "v_cvt_pk_bf16_f32 v84, v88, v89\n\t"
    "v_mul_f32 v90, 0x3e0293ee, v90\n\t"
    "v_mfma_f32_16x16x32_bf16 v[100:103], v[132:135], v[192:195], v[100:103]\n\t"
    "ds_read_b128 v[28:31], v241 offset:20480\n\t"
    "v_mul_f32 v91, 0x3e0293ee, v91\n\t"
    "v_cvt_pk_bf16_f32 v85, v90, v91\n\t"
    "v_mul_f32 v92, 0x3e0293ee, v92\n\t"
    "v_mul_f32 v93, 0x3e0293ee, v93\n\t"
    "v_mfma_f32_16x16x32_bf16 v[108:111], v[132:135], v[196:199], v[108:111]\n\t"
    "s_add_u32 m0, s60, 98304\n\t"
    "s_mov_b32 s63, 49152\n\t"
    "buffer_load_dwordx4 v242, s[44:47], s63 offen nt lds\n\t"
    "v_cvt_pk_bf16_f32 v86, v92, v93\n\t"
    "v_mul_f32 v94, 0x3e0293ee, v94\n\t"
    "v_mul_f32 v95, 0x3e0293ee, v95\n\t"
    "v_cvt_pk_bf16_f32 v87, v94, v95\n\t"
    "v_mfma_f32_16x16x32_bf16 v[112:115], v[136:139], v[184:187], v[112:115]\n\t"
    "ds_read_b128 v[40:43], v240 offset:17408\n\t"
    "v_permlane32_swap_b32 v80, v82\n\t"
    "v_permlane32_swap_b32 v81, v83\n\t"
    "v_permlane32_swap_b32 v84, v86\n\t"
    "v_permlane32_swap_b32 v85, v87\n\t"
    "v_mfma_f32_16x16x32_bf16 v[120:123], v[136:139], v[188:191], v[120:123]\n\t"
    "v_permlane16_swap_b32 v80, v82\n\t"
    "v_permlane16_swap_b32 v81, v83\n\t"
    "v_permlane16_swap_b32 v84, v86\n\t"
    "v_permlane16_swap_b32 v85, v87\n\t"
    "v_mfma_f32_16x16x32_bf16 v[112:115], v[140:143], v[192:195], v[112:115]\n\t"
    "ds_read_b128 v[44:47], v240 offset:21504\n\t"
    "v_mul_f32 v96, 0x3e0293ee, v96\n\t"
    "v_mul_f32 v97, 0x3e0293ee, v97\n\t"
    "v_cvt_pk_bf16_f32 v96, v96, v97\n\t"
    "v_mul_f32 v98, 0x3e0293ee, v98\n\t"
    "v_mfma_f32_16x16x32_bf16 v[120:123], v[140:143], v[196:199], v[120:123]\n\t"
    "s_add_u32 m0, s60, 106496\n\t"
    "s_mov_b32 s63, 57344\n\t"
    "buffer_load_dwordx4 v242, s[44:47], s63 offen nt lds\n\t"
    "v_mul_f32 v99, 0x3e0293ee, v99\n\t"
    "v_cvt_pk_bf16_f32 v97, v98, v99\n\t"
    "v_mul_f32 v100, 0x3e0293ee, v100\n\t"
    "v_mul_f32 v101, 0x3e0293ee, v101\n\t"
    "v_mfma_f32_16x16x32_bf16 v[116:119], v[144:147], v[184:187], v[116:119]\n\t"
    "ds_read_b128 v[56:59], v241 offset:17408\n\t"
    "v_cvt_pk_bf16_f32 v98, v100, v101\n\t"
    "v_mul_f32 v102, 0x3e0293ee, v102\n\t"
    "v_mul_f32 v103, 0x3e0293ee, v103\n\t"
    "v_cvt_pk_bf16_f32 v99, v102, v103\n\t"
    "v_mfma_f32_16x16x32_bf16 v[124:127], v[144:147], v[188:191], v[124:127]\n\t"
    "v_mul_f32 v104, 0x3e0293ee, v104\n\t"
    "v_mul_f32 v105, 0x3e0293ee, v105\n\t"
    "v_cvt_pk_bf16_f32 v100, v104, v105\n\t"
    "v_mul_f32 v106, 0x3e0293ee, v106\n\t"
    "v_mfma_f32_16x16x32_bf16 v[116:119], v[148:151], v[192:195], v[116:119]\n\t"
    "ds_read_b128 v[60:63], v241 offset:21504\n\t"
    "v_mul_f32 v107, 0x3e0293ee, v107\n\t"
    "v_cvt_pk_bf16_f32 v101, v106, v107\n\t"
    "v_mul_f32 v108, 0x3e0293ee, v108\n\t"
    "v_mul_f32 v109, 0x3e0293ee, v109\n\t"
    "v_mfma_f32_16x16x32_bf16 v[124:127], v[148:151], v[196:199], v[124:127]\n\t"
    "s_add_u32 m0, s60, 0\n\t"
    "s_mov_b32 s63, 65536\n\t"
    "buffer_load_dwordx4 v242, s[44:47], s63 offen nt lds\n\t"
    "v_cvt_pk_bf16_f32 v102, v108, v109\n\t"
    "v_mul_f32 v110, 0x3e0293ee, v110\n\t"
    "v_mul_f32 v111, 0x3e0293ee, v111\n\t"
    "v_cvt_pk_bf16_f32 v103, v110, v111\n\t"
    "s_waitcnt lgkmcnt(6)\n\t"
    "v_mfma_f32_16x16x32_bf16 v[208:211], v[8:11], v[64:67], 0\n\t"
    "ds_read_b128 v[72:75], v240 offset:24576\n\t"
    "s_setprio 2\n\t"
    "v_permlane32_swap_b32 v96, v98\n\t"
    "v_permlane32_swap_b32 v97, v99\n\t"
    "v_permlane32_swap_b32 v100, v102\n\t"
    "v_mfma_f32_16x16x32_bf16 v[216:219], v[8:11], v[68:71], 0\n\t"
    "v_permlane32_swap_b32 v101, v103\n\t"
    "v_permlane16_swap_b32 v96, v98\n\t"
    "v_permlane16_swap_b32 v97, v99\n\t"
    "v_permlane16_swap_b32 v100, v102\n\t"
    "v_permlane16_swap_b32 v101, v103\n\t"
    "v_mfma_f32_16x16x32_bf16 v[212:215], v[12:15], v[64:67], 0\n\t"
    "ds_read_b128 v[76:79], v240 offset:28672\n\t"
    "v_mul_f32 v112, 0x3e0293ee, v112\n\t"
    "v_mul_f32 v113, 0x3e0293ee, v113\n\t"
    "v_cvt_pk_bf16_f32 v112, v112, v113\n\t"
    "v_mul_f32 v114, 0x3e0293ee, v114\n\t"
    "v_mfma_f32_16x16x32_bf16 v[220:223], v[12:15], v[68:71], 0\n\t"
    "s_add_u32 m0, s60, 8192\n\t"
    "s_mov_b32 s63, 73728\n\t"
    "buffer_load_dwordx4 v242, s[44:47], s63 offen nt lds\n\t"
    "v_mul_f32 v115, 0x3e0293ee, v115\n\t"
    "v_cvt_pk_bf16_f32 v113, v114, v115\n\t"
    "v_mul_f32 v116, 0x3e0293ee, v116\n\t"
    "v_mul_f32 v117, 0x3e0293ee, v117\n\t"
    "s_waitcnt lgkmcnt(6)\n\t"
    "v_mfma_f32_16x16x32_bf16 v[208:211], v[24:27], v[80:83], v[208:211]\n\t"
    "ds_read_b128 v[88:91], v241 offset:24576\n\t"
    "v_cvt_pk_bf16_f32 v114, v116, v117\n\t"
    "v_mul_f32 v118, 0x3e0293ee, v118\n\t"
    "v_mul_f32 v119, 0x3e0293ee, v119\n\t"
    "v_cvt_pk_bf16_f32 v115, v118, v119\n\t"
    "v_mfma_f32_16x16x32_bf16 v[216:219], v[24:27], v[84:87], v[216:219]\n\t"
    "v_mul_f32 v120, 0x3e0293ee, v120\n\t"
    "v_mul_f32 v121, 0x3e0293ee, v121\n\t"
    "v_cvt_pk_bf16_f32 v116, v120, v121\n\t"
    "v_mul_f32 v122, 0x3e0293ee, v122\n\t"
    "v_mfma_f32_16x16x32_bf16 v[212:215], v[28:31], v[80:83], v[212:215]\n\t"
    "ds_read_b128 v[92:95], v241 offset:28672\n\t"
    "v_mul_f32 v123, 0x3e0293ee, v123\n\t"
    "v_cvt_pk_bf16_f32 v117, v122, v123\n\t"
    "v_mul_f32 v124, 0x3e0293ee, v124\n\t"
    "v_mul_f32 v125, 0x3e0293ee, v125\n\t"
    "v_mfma_f32_16x16x32_bf16 v[220:223], v[28:31], v[84:87], v[220:223]\n\t"
    "v_cvt_pk_bf16_f32 v118, v124, v125\n\t"
    "v_mul_f32 v126, 0x3e0293ee, v126\n\t"
    "v_mul_f32 v127, 0x3e0293ee, v127\n\t"
    "v_cvt_pk_bf16_f32 v119, v126, v127\n\t"
    "s_waitcnt lgkmcnt(6)\n\t"
    "v_mfma_f32_16x16x32_bf16 v[208:211], v[40:43], v[96:99], v[208:211]\n\t"
    "ds_read_b128 v[8:11], v240 offset:25600\n\t"
    "s_setprio 1\n\t"
    "v_permlane32_swap_b32 v112, v114\n\t"
    "v_permlane32_swap_b32 v113, v115\n\t"
    "v_permlane32_swap_b32 v116, v118\n\t"
    "v_mfma_f32_16x16x32_bf16 v[216:219], v[40:43], v[100:103], v[216:219]\n\t"
    "v_permlane32_swap_b32 v117, v119\n\t"
    "v_permlane16_swap_b32 v112, v114\n\t"
    "v_permlane16_swap_b32 v113, v115\n\t"
    "v_permlane16_swap_b32 v116, v118\n\t"
    "v_permlane16_swap_b32 v117, v119\n\t"
    "v_mfma_f32_16x16x32_bf16 v[212:215], v[44:47], v[96:99], v[212:215]\n\t"
    "ds_read_b128 v[12:15], v240 offset:29696\n\t"
    "v_mfma_f32_16x16x32_bf16 v[220:223], v[44:47], v[100:103], v[220:223]\n\t"
    "s_waitcnt lgkmcnt(6)\n\t"
    "v_mfma_f32_16x16x32_bf16 v[208:211], v[56:59], v[112:115], v[208:211]\n\t"
    "ds_read_b128 v[24:27], v241 offset:25600\n\t"
    "v_mfma_f32_16x16x32_bf16 v[216:219], v[56:59], v[116:119], v[216:219]\n\t"
    "v_mfma_f32_16x16x32_bf16 v[212:215], v[60:63], v[112:115], v[212:215]\n\t"
    "ds_read_b128 v[28:31], v241 offset:29696\n\t"
    "v_mfma_f32_16x16x32_bf16 v[220:223], v[60:63], v[116:119], v[220:223]\n\t"
    "s_setprio 0\n\t"
    "s_waitcnt lgkmcnt(6)\n\t"
    "v_mfma_f32_16x16x32_bf16 v[224:227], v[72:75], v[64:67], 0\n\t"
    "v_mfma_f32_16x16x32_bf16 v[232:235], v[72:75], v[68:71], 0\n\t"
    "v_mfma_f32_16x16x32_bf16 v[228:231], v[76:79], v[64:67], 0\n\t"
    "v_max3_f32 v253, v208, v209, v210\n\t"
    "v_max3_f32 v254, v216, v217, v218\n\t"
    "v_max3_f32 v253, v253, v211, v212\n\t"
    "v_max3_f32 v254, v254, v219, v220\n\t"
    "v_max3_f32 v253, v253, v213, v214\n\t"
    "v_max3_f32 v254, v254, v221, v222\n\t"
    "v_max_f32 v253, v253, v215\n\t"
    "v_max_f32 v254, v254, v223\n\t"
    "v_mfma_f32_16x16x32_bf16 v[236:239], v[76:79], v[68:71], 0\n\t"
    "s_waitcnt vmcnt(8) lgkmcnt(0)\n\t"
    "s_barrier\n\t"
    "s_setprio 3\n\t"
    "ds_read_b128 v[40:43], v240 offset:32768\n\t"
    "ds_read_b128 v[44:47], v240 offset:36864\n\t"
    "v_mov_b32 v200, v253\n\t"
    "v_mov_b32 v201, v254\n\t"
    "s_nop 0\n\t"
    "v_permlane32_swap_b32 v253, v200\n\t"
    "v_permlane32_swap_b32 v254, v201\n\t"
    "v_mfma_f32_16x16x32_bf16 v[224:227], v[88:91], v[80:83], v[224:227]\n\t"
    "ds_read_b64_tr_b16 v[56:57], v244 offset:32768\n\t"
    "v_max_f32 v253, v253, v200\n\t"
    "v_max_f32 v254, v254, v201\n\t"
    "v_mov_b32 v200, v253\n\t"
    "v_mov_b32 v201, v254\n\t"
    "s_nop 0\n\t"
    "v_permlane16_swap_b32 v253, v200\n\t"
    "v_permlane16_swap_b32 v254, v201\n\t"
    "v_mfma_f32_16x16x32_bf16 v[232:235], v[88:91], v[84:87], v[232:235]\n\t"
    "ds_read_b64_tr_b16 v[58:59], v244 offset:36864\n\t"
    "v_max_f32 v253, v253, v200\n\t"
    "v_max_f32 v254, v254, v201\n\t"
    "s_cmp_eq_u32 s67, 0\n\t"
    "s_cselect_b32 s65, 0, 0xff800000\n\t"
    "v_sub_f32 v192, s65, v253\n\t"
    "s_cmp_eq_u32 s62, 0\n\t"
    "s_cselect_b32 s65, 0, 0xff800000\n\t"
    "v_sub_f32 v196, s65, v254\n\t"
    "v_add_f32 v208, v192, v208\n\t"
    "v_add_f32 v209, v192, v209\n\t"
    "v_mfma_f32_16x16x32_bf16 v[228:231], v[92:95], v[80:83], v[228:231]\n\t"
    "ds_read_b128 v[60:63], v241 offset:32768\n\t"
    "v_exp_f32 v208, v208\n\t"
    "v_add_f32 v210, v192, v210\n\t"
    "v_exp_f32 v209, v209\n\t"
    "v_add_f32 v248, v248, v208\n\t"
    "v_add_f32 v211, v192, v211\n\t"
    "v_exp_f32 v210, v210\n\t"
    "v_mfma_f32_16x16x32_bf16 v[236:239], v[92:95], v[84:87], v[236:239]\n\t"
    "v_add_f32 v248, v248, v209\n\t"
    "v_cvt_pk_bf16_f32 v104, v208, v209\n\t"
    "v_add_f32 v212, v192, v212\n\t"
    "v_exp_f32 v211, v211\n\t"
    "v_add_f32 v248, v248, v210\n\t"
    "v_add_f32 v213, v192, v213\n\t"
    "v_mfma_f32_16x16x32_bf16 v[224:227], v[8:11], v[96:99], v[224:227]\n\t"
    "ds_read_b64_tr_b16 v[72:73], v245 offset:32768\n\t"
    "v_exp_f32 v212, v212\n\t"
    "v_add_f32 v248, v248, v211\n\t"
    "v_cvt_pk_bf16_f32 v105, v210, v211\n\t"
    "v_add_f32 v214, v192, v214\n\t"
    "v_exp_f32 v213, v213\n\t"
    "v_add_f32 v248, v248, v212\n\t"
    "v_mfma_f32_16x16x32_bf16 v[232:235], v[8:11], v[100:103], v[232:235]\n\t"
    "ds_read_b64_tr_b16 v[74:75], v245 offset:36864\n\t"
    "v_add_f32 v215, v192, v215\n\t"
    "v_exp_f32 v214, v214\n\t"
    "v_add_f32 v248, v248, v213\n\t"
    "v_cvt_pk_bf16_f32 v106, v212, v213\n\t"
    "v_add_f32 v216, v196, v216\n\t"
    "v_exp_f32 v215, v215\n\t"
    "v_mfma_f32_16x16x32_bf16 v[228:231], v[12:15], v[96:99], v[228:231]\n\t"
    "ds_read_b128 v[76:79], v241 offset:36864\n\t"
    "v_add_f32 v248, v248, v214\n\t"
    "v_add_f32 v217, v196, v217\n\t"
    "v_exp_f32 v216, v216\n\t"
    "v_add_f32 v248, v248, v215\n\t"
    "v_cvt_pk_bf16_f32 v107, v214, v215\n\t"
    "v_add_f32 v218, v196, v218\n\t"
    "v_mfma_f32_16x16x32_bf16 v[236:239], v[12:15], v[100:103], v[236:239]\n\t"
    "v_exp_f32 v217, v217\n\t"
    "v_add_f32 v249, v249, v216\n\t"
    "v_add_f32 v219, v196, v219\n\t"
    "v_exp_f32 v218, v218\n\t"
    "v_add_f32 v249, v249, v217\n\t"
    "v_cvt_pk_bf16_f32 v108, v216, v217\n\t"
    "v_mfma_f32_16x16x32_bf16 v[224:227], v[24:27], v[112:115], v[224:227]\n\t"
    "ds_read_b64_tr_b16 v[88:89], v246 offset:32768\n\t"
    "v_add_f32 v220, v196, v220\n\t"
    "v_exp_f32 v219, v219\n\t"
    "v_add_f32 v249, v249, v218\n\t"
    "v_add_f32 v221, v196, v221\n\t"
    "v_exp_f32 v220, v220\n\t"
    "v_add_f32 v249, v249, v219\n\t"
    "v_mfma_f32_16x16x32_bf16 v[232:235], v[24:27], v[116:119], v[232:235]\n\t"
    "ds_read_b64_tr_b16 v[90:91], v246 offset:36864\n\t"
    "v_cvt_pk_bf16_f32 v109, v218, v219\n\t"
    "v_add_f32 v222, v196, v222\n\t"
    "v_exp_f32 v221, v221\n\t"
    "v_add_f32 v249, v249, v220\n\t"
    "v_add_f32 v223, v196, v223\n\t"
    "v_exp_f32 v222, v222\n\t"
    "v_mfma_f32_16x16x32_bf16 v[228:231], v[28:31], v[112:115], v[228:231]\n\t"
    "ds_read_b128 v[92:95], v240 offset:33792\n\t"
    "v_add_f32 v249, v249, v221\n\t"
    "v_cvt_pk_bf16_f32 v110, v220, v221\n\t"
    "v_exp_f32 v223, v223\n\t"
    "v_add_f32 v249, v249, v222\n\t"
    "v_add_f32 v249, v249, v223\n\t"
    "v_cvt_pk_bf16_f32 v111, v222, v223\n\t"
    "v_mfma_f32_16x16x32_bf16 v[236:239], v[28:31], v[116:119], v[236:239]\n\t"
    "s_add_u32 m0, s60, 16384\n\t"
    "s_mov_b32 s63, 81920\n\t"
    "buffer_load_dwordx4 v242, s[44:47], s63 offen nt lds\n\t"
    "v_mov_b32 v193, v192\n\t"
    "v_mov_b32 v194, v192\n\t"
    "v_mov_b32 v195, v192\n\t"
    "v_mov_b32 v197, v196\n\t"
    "v_mov_b32 v198, v196\n\t"
    "v_mov_b32 v199, v196\n\t"
    "s_waitcnt lgkmcnt(9)\n\t"
    "v_mfma_f32_16x16x32_bf16 v[208:211], v[40:43], v[64:67], v[192:195]\n\t"
    "ds_read_b64_tr_b16 v[8:9], v247 offset:32768\n\t"
    "v_add_f32 v224, v192, v224\n\t"
    "v_add_f32 v225, v192, v225\n\t"
    "v_mfma_f32_16x16x32_bf16 v[216:219], v[40:43], v[68:71], v[196:199]\n\t"
    "ds_read_b64_tr_b16 v[10:11], v247 offset:36864\n\t"
    "v_exp_f32 v224, v224\n\t"
    "v_add_f32 v226, v192, v226\n\t"
    "v_mfma_f32_16x16x32_bf16 v[212:215], v[44:47], v[64:67], v[192:195]\n\t"
    "ds_read_b128 v[12:15], v240 offset:37888\n\t"
    "v_exp_f32 v225, v225\n\t"
    "v_mfma_f32_16x16x32_bf16 v[220:223], v[44:47], v[68:71], v[196:199]\n\t"
    "v_add_f32 v248, v248, v224\n\t"
    "v_add_f32 v227, v192, v227\n\t"
    "s_waitcnt lgkmcnt(9)\n\t"
    "v_mfma_f32_16x16x32_bf16 v[128:131], v[56:59], v[104:107], 0\n\t"
    "ds_read_b64_tr_b16 v[24:25], v244 offset:33792\n\t"
    "v_exp_f32 v226, v226\n\t"
    "v_add_f32 v248, v248, v225\n\t"
    "v_mfma_f32_16x16x32_bf16 v[136:139], v[56:59], v[108:111], 0\n\t"
    "ds_read_b64_tr_b16 v[26:27], v244 offset:37888\n\t"
    "v_cvt_pk_bf16_f32 v120, v224, v225\n\t"
    "v_add_f32 v228, v192, v228\n\t"
    "v_mfma_f32_16x16x32_bf16 v[208:211], v[60:63], v[80:83], v[208:211]\n\t"
    "ds_read_b128 v[28:31], v241 offset:33792\n\t"
    "v_exp_f32 v227, v227\n\t"
    "v_mfma_f32_16x16x32_bf16 v[216:219], v[60:63], v[84:87], v[216:219]\n\t"
    "v_add_f32 v248, v248, v226\n\t"
    "v_add_f32 v229, v192, v229\n\t"
    "s_waitcnt lgkmcnt(9)\n\t"
    "v_mfma_f32_16x16x32_bf16 v[132:135], v[72:75], v[104:107], 0\n\t"
    "ds_read_b64_tr_b16 v[40:41], v245 offset:33792\n\t"
    "v_exp_f32 v228, v228\n\t"
    "v_add_f32 v248, v248, v227\n\t"
    "v_mfma_f32_16x16x32_bf16 v[140:143], v[72:75], v[108:111], 0\n\t"
    "ds_read_b64_tr_b16 v[42:43], v245 offset:37888\n\t"
    "v_cvt_pk_bf16_f32 v121, v226, v227\n\t"
    "v_add_f32 v230, v192, v230\n\t"
    "v_mfma_f32_16x16x32_bf16 v[212:215], v[76:79], v[80:83], v[212:215]\n\t"
    "ds_read_b128 v[44:47], v241 offset:37888\n\t"
    "v_exp_f32 v229, v229\n\t"
    "v_mfma_f32_16x16x32_bf16 v[220:223], v[76:79], v[84:87], v[220:223]\n\t"
    "s_add_u32 m0, s60, 24576\n\t"
    "s_mov_b32 s63, 90112\n\t"
    "buffer_load_dwordx4 v242, s[44:47], s63 offen nt lds\n\t"
    "v_add_f32 v248, v248, v228\n\t"
    "v_add_f32 v231, v192, v231\n\t"
    "s_waitcnt lgkmcnt(9)\n\t"
    "v_mfma_f32_16x16x32_bf16 v[144:147], v[88:91], v[104:107], 0\n\t"
    "ds_read_b64_tr_b16 v[56:57], v246 offset:33792\n\t"
    "v_exp_f32 v230, v230\n\t"
    "v_add_f32 v248, v248, v229\n\t"
    "v_mfma_f32_16x16x32_bf16 v[152:155], v[88:91], v[108:111], 0\n\t"
    "ds_read_b64_tr_b16 v[58:59], v246 offset:37888\n\t"
    "v_cvt_pk_bf16_f32 v122, v228, v229\n\t"
    "v_add_f32 v232, v196, v232\n\t"
    "v_mfma_f32_16x16x32_bf16 v[208:211], v[92:95], v[96:99], v[208:211]\n\t"
    "ds_read_b64_tr_b16 v[60:61], v247 offset:33792\n\t"
    "s_setprio 2\n\t"
    "v_exp_f32 v231, v231\n\t"
    "v_mfma_f32_16x16x32_bf16 v[216:219], v[92:95], v[100:103], v[216:219]\n\t"
    "ds_read_b64_tr_b16 v[62:63], v247 offset:37888\n\t"
    "v_add_f32 v248, v248, v230\n\t"
    "v_add_f32 v233, v196, v233\n\t"
    "s_waitcnt lgkmcnt(10)\n\t"
    "v_mfma_f32_16x16x32_bf16 v[148:151], v[8:11], v[104:107], 0\n\t"
    "ds_read_b128 v[72:75], v240 offset:40960\n\t"
    "v_exp_f32 v232, v232\n\t"
    "v_add_f32 v248, v248, v231\n\t"
    "v_mfma_f32_16x16x32_bf16 v[156:159], v[8:11], v[108:111], 0\n\t"
    "v_cvt_pk_bf16_f32 v123, v230, v231\n\t"
    "v_add_f32 v234, v196, v234\n\t"
    "v_mfma_f32_16x16x32_bf16 v[212:215], v[12:15], v[96:99], v[212:215]\n\t"
    "ds_read_b128 v[76:79], v240 offset:45056\n\t"
    "v_exp_f32 v233, v233\n\t"
    "v_mfma_f32_16x16x32_bf16 v[220:223], v[12:15], v[100:103], v[220:223]\n\t"
    "v_add_f32 v249, v249, v232\n\t"
    "v_add_f32 v235, v196, v235\n\t"
    "s_waitcnt lgkmcnt(9)\n\t"
    "v_mfma_f32_16x16x32_bf16 v[160:163], v[24:27], v[104:107], 0\n\t"
    "ds_read_b64_tr_b16 v[88:89], v244 offset:40960\n\t"
    "v_exp_f32 v234, v234\n\t"
    "v_add_f32 v249, v249, v233\n\t"
    "v_mfma_f32_16x16x32_bf16 v[168:171], v[24:27], v[108:111], 0\n\t"
    "ds_read_b64_tr_b16 v[90:91], v244 offset:45056\n\t"
    "v_cvt_pk_bf16_f32 v124, v232, v233\n\t"
    "v_add_f32 v236, v196, v236\n\t"
    "v_mfma_f32_16x16x32_bf16 v[208:211], v[28:31], v[112:115], v[208:211]\n\t"
    "ds_read_b128 v[92:95], v241 offset:40960\n\t"
    "v_exp_f32 v235, v235\n\t"
    "v_mfma_f32_16x16x32_bf16 v[216:219], v[28:31], v[116:119], v[216:219]\n\t"
    "s_add_u32 m0, s60, 114688\n\t"
    "s_mov_b32 s63, 98304\n\t"
    "buffer_load_dwordx4 v242, s[44:47], s63 offen nt lds\n\t"
    "v_add_f32 v249, v249, v234\n\t"
    "v_add_f32 v237, v196, v237\n\t"
    "s_waitcnt lgkmcnt(9)\n\t"
    "v_mfma_f32_16x16x32_bf16 v[164:167], v[40:43], v[104:107], 0\n\t"
    "ds_read_b64_tr_b16 v[8:9], v245 offset:40960\n\t"
    "v_exp_f32 v236, v236\n\t"
    "v_add_f32 v249, v249, v235\n\t"
    "v_mfma_f32_16x16x32_bf16 v[172:175], v[40:43], v[108:111], 0\n\t"
    "ds_read_b64_tr_b16 v[10:11], v245 offset:45056\n\t"
    "v_cvt_pk_bf16_f32 v125, v234, v235\n\t"
    "v_add_f32 v238, v196, v238\n\t"
    "v_mfma_f32_16x16x32_bf16 v[212:215], v[44:47], v[112:115], v[212:215]\n\t"
    "PAD3_%=: s_cbranch_execz PAD11_%=\n\t"
    "ds_read_b128 v[12:15], v241 offset:45056\n\t"
    "v_exp_f32 v237, v237\n\t"
    "v_mfma_f32_16x16x32_bf16 v[220:223], v[44:47], v[116:119], v[220:223]\n\t"
    "v_add_f32 v249, v249, v236\n\t"
    "v_add_f32 v239, v196, v239\n\t"
    "s_waitcnt lgkmcnt(8)\n\t"
    "v_mfma_f32_16x16x32_bf16 v[176:179], v[56:59], v[104:107], 0\n\t"
    "ds_read_b64_tr_b16 v[24:25], v246 offset:40960\n\t"
    "v_exp_f32 v238, v238\n\t"
    "v_add_f32 v249, v249, v237\n\t"
    "v_mfma_f32_16x16x32_bf16 v[184:187], v[56:59], v[108:111], 0\n\t"
    "ds_read_b64_tr_b16 v[26:27], v246 offset:45056\n\t"
    "v_cvt_pk_bf16_f32 v126, v236, v237\n\t"
    "v_exp_f32 v239, v239\n\t"
    "v_mfma_f32_16x16x32_bf16 v[180:183], v[60:63], v[104:107], 0\n\t"
    "ds_read_b128 v[28:31], v240 offset:41984\n\t"
    "v_add_f32 v249, v249, v238\n\t"
    "v_mfma_f32_16x16x32_bf16 v[188:191], v[60:63], v[108:111], 0\n\t"
    "v_add_f32 v249, v249, v239\n\t"
    "v_cvt_pk_bf16_f32 v127, v238, v239\n\t"
    "s_waitcnt lgkmcnt(9)\n\t"
    "v_mfma_f32_16x16x32_bf16 v[224:227], v[72:75], v[64:67], v[192:195]\n\t"
    "ds_read_b64_tr_b16 v[40:41], v247 offset:40960\n\t"
    "s_setprio 1\n\t"
    "v_exp_f32 v208, v208\n\t"
    "v_exp_f32 v209, v209\n\t"
    "v_mfma_f32_16x16x32_bf16 v[232:235], v[72:75], v[68:71], v[196:199]\n\t"
    "ds_read_b64_tr_b16 v[42:43], v247 offset:45056\n\t"
    "v_add_f32 v248, v248, v208\n\t"
    "v_subrev_u32 v202, 98304, v240\n\t"
    "v_subrev_u32 v203, 98304, v241\n\t"
    "v_mfma_f32_16x16x32_bf16 v[228:231], v[76:79], v[64:67], v[192:195]\n\t"
    "ds_read_b128 v[44:47], v240 offset:46080\n\t"
    "v_exp_f32 v210, v210\n\t"
    "v_mfma_f32_16x16x32_bf16 v[236:239], v[76:79], v[68:71], v[196:199]\n\t"
    "s_add_u32 m0, s60, 122880\n\t"
    "s_mov_b32 s63, 106496\n\t"
    "buffer_load_dwordx4 v242, s[44:47], s63 offen nt lds\n\t"
    "v_add_f32 v248, v248, v209\n\t"
    "s_waitcnt lgkmcnt(9)\n\t"
    "v_mfma_f32_16x16x32_bf16 v[128:131], v[88:91], v[120:123], v[128:131]\n\t"
    "ds_read_b64_tr_b16 v[56:57], v244 offset:41984\n\t"
    "v_cvt_pk_bf16_f32 v104, v208, v209\n\t"
    "v_exp_f32 v211, v211\n\t"
    "v_mfma_f32_16x16x32_bf16 v[136:139], v[88:91], v[124:127], v[136:139]\n\t"
    "ds_read_b64_tr_b16 v[58:59], v244 offset:46080\n\t"
    "v_add_f32 v248, v248, v210\n\t"
    "v_mfma_f32_16x16x32_bf16 v[224:227], v[92:95], v[80:83], v[224:227]\n\t"
    "ds_read_b128 v[60:63], v241 offset:41984\n\t"
    "v_exp_f32 v212, v212\n\t"
    "v_mfma_f32_16x16x32_bf16 v[232:235], v[92:95], v[84:87], v[232:235]\n\t"
    "v_add_f32 v248, v248, v211\n\t"
    "s_waitcnt lgkmcnt(9)\n\t"
    "v_mfma_f32_16x16x32_bf16 v[132:135], v[8:11], v[120:123], v[132:135]\n\t"
    "ds_read_b64_tr_b16 v[72:73], v245 offset:41984\n\t"
    "v_cvt_pk_bf16_f32 v105, v210, v211\n\t"
    "v_exp_f32 v213, v213\n\t"
    "v_mfma_f32_16x16x32_bf16 v[140:143], v[8:11], v[124:127], v[140:143]\n\t"
    "ds_read_b64_tr_b16 v[74:75], v245 offset:46080\n\t"
    "v_add_f32 v248, v248, v212\n\t"
    "v_mfma_f32_16x16x32_bf16 v[228:231], v[12:15], v[80:83], v[228:231]\n\t"
    "ds_read_b128 v[76:79], v241 offset:46080\n\t"
    "s_setprio 0\n\t"
    "v_mfma_f32_16x16x32_bf16 v[236:239], v[12:15], v[84:87], v[236:239]\n\t"
    "v_exp_f32 v214, v214\n\t"
    "v_add_f32 v248, v248, v213\n\t"
    "s_waitcnt lgkmcnt(9)\n\t"
    "v_mfma_f32_16x16x32_bf16 v[144:147], v[24:27], v[120:123], v[144:147]\n\t"
    "ds_read_b64_tr_b16 v[88:89], v246 offset:41984\n\t"
    "v_cvt_pk_bf16_f32 v106, v212, v213\n\t"
    "v_exp_f32 v215, v215\n\t"
    "v_mfma_f32_16x16x32_bf16 v[152:155], v[24:27], v[124:127], v[152:155]\n\t"
    "ds_read_b64_tr_b16 v[90:91], v246 offset:46080\n\t"
    "v_add_f32 v248, v248, v214\n\t"
    "v_mfma_f32_16x16x32_bf16 v[224:227], v[28:31], v[96:99], v[224:227]\n\t"
    "ds_read_b64_tr_b16 v[92:93], v247 offset:41984\n\t"
    "v_exp_f32 v216, v216\n\t"
    "v_mfma_f32_16x16x32_bf16 v[232:235], v[28:31], v[100:103], v[232:235]\n\t"
    "ds_read_b64_tr_b16 v[94:95], v247 offset:46080\n\t"
    "v_add_f32 v248, v248, v215\n\t"
    "s_waitcnt lgkmcnt(10)\n\t"
    "v_mfma_f32_16x16x32_bf16 v[148:151], v[40:43], v[120:123], v[148:151]\n\t"
    "v_cvt_pk_bf16_f32 v107, v214, v215\n\t"
    "v_exp_f32 v217, v217\n\t"
    "v_mfma_f32_16x16x32_bf16 v[156:159], v[40:43], v[124:127], v[156:159]\n\t"
    "v_add_f32 v249, v249, v216\n\t"
    "v_mfma_f32_16x16x32_bf16 v[228:231], v[44:47], v[96:99], v[228:231]\n\t"
    "v_exp_f32 v218, v218\n\t"
    "v_add_f32 v249, v249, v217\n\t"
    "v_mfma_f32_16x16x32_bf16 v[236:239], v[44:47], v[100:103], v[236:239]\n\t"
    "s_waitcnt vmcnt(10) lgkmcnt(0)\n\t"
    "s_barrier\n\t"
    "s_setprio 3\n\t"
    "ds_read_b128 v[8:11], v240 offset:49152\n\t"
    "ds_read_b128 v[12:15], v240 offset:53248\n\t"
    "v_mfma_f32_16x16x32_bf16 v[160:163], v[56:59], v[120:123], v[160:163]\n\t"
    "ds_read_b64_tr_b16 v[24:25], v244 offset:0\n\t"
    "v_cvt_pk_bf16_f32 v108, v216, v217\n\t"
    "v_exp_f32 v219, v219\n\t"
    "v_mfma_f32_16x16x32_bf16 v[168:171], v[56:59], v[124:127], v[168:171]\n\t"
    "ds_read_b64_tr_b16 v[26:27], v244 offset:4096\n\t"
    "v_add_f32 v249, v249, v218\n\t"
    "v_mfma_f32_16x16x32_bf16 v[224:227], v[60:63], v[112:115], v[224:227]\n\t"
    "ds_read_b128 v[28:31], v241 offset:49152\n\t"
    "v_exp_f32 v220, v220\n\t"
    "v_mfma_f32_16x16x32_bf16 v[232:235], v[60:63], v[116:119], v[232:235]\n\t"
    "v_add_f32 v249, v249, v219\n\t"
    "v_mfma_f32_16x16x32_bf16 v[164:167], v[72:75], v[120:123], v[164:167]\n\t"
    "ds_read_b64_tr_b16 v[40:41], v245 offset:0\n\t"
    "v_cvt_pk_bf16_f32 v109, v218, v219\n\t"
    "v_exp_f32 v221, v221\n\t"
    "v_mfma_f32_16x16x32_bf16 v[172:175], v[72:75], v[124:127], v[172:175]\n\t"
    "ds_read_b64_tr_b16 v[42:43], v245 offset:4096\n\t"
    "v_add_f32 v249, v249, v220\n\t"
    "v_mfma_f32_16x16x32_bf16 v[228:231], v[76:79], v[112:115], v[228:231]\n\t"
    "ds_read_b128 v[44:47], v241 offset:53248\n\t"
    "v_exp_f32 v222, v222\n\t"
    "v_mfma_f32_16x16x32_bf16 v[236:239], v[76:79], v[116:119], v[236:239]\n\t"
    "v_add_f32 v249, v249, v221\n\t"
    "v_mfma_f32_16x16x32_bf16 v[176:179], v[88:91], v[120:123], v[176:179]\n\t"
    "ds_read_b64_tr_b16 v[56:57], v246 offset:0\n\t"
    "v_cvt_pk_bf16_f32 v110, v220, v221\n\t"
    "v_exp_f32 v223, v223\n\t"
    "v_mfma_f32_16x16x32_bf16 v[184:187], v[88:91], v[124:127], v[184:187]\n\t"
    "ds_read_b64_tr_b16 v[58:59], v246 offset:4096\n\t"
    "v_add_f32 v249, v249, v222\n\t"
    "v_mfma_f32_16x16x32_bf16 v[180:183], v[92:95], v[120:123], v[180:183]\n\t"
    "ds_read_b128 v[60:63], v240 offset:50176\n\t"
    "v_add_f32 v249, v249, v223\n\t"
    "v_mfma_f32_16x16x32_bf16 v[188:191], v[92:95], v[124:127], v[188:191]\n\t"
    "s_add_u32 m0, s60, 131072\n\t"
    "s_mov_b32 s63, 114688\n\t"
    "buffer_load_dwordx4 v242, s[44:47], s63 offen nt lds\n\t"
    "v_cvt_pk_bf16_f32 v111, v222, v223\n\t"
    "s_waitcnt lgkmcnt(9)\n\t"
    "v_mfma_f32_16x16x32_bf16 v[208:211], v[8:11], v[64:67], v[192:195]\n\t"
    "ds_read_b64_tr_b16 v[72:73], v247 offset:0\n\t"
    "v_exp_f32 v224, v224\n\t"
    "v_exp_f32 v225, v225\n\t"
    "v_mfma_f32_16x16x32_bf16 v[216:219], v[8:11], v[68:71], v[196:199]\n\t"
    "ds_read_b64_tr_b16 v[74:75], v247 offset:4096\n\t"
    "v_add_f32 v248, v248, v224\n\t"
    "v_mfma_f32_16x16x32_bf16 v[212:215], v[12:15], v[64:67], v[192:195]\n\t"
    "ds_read_b128 v[76:79], v240 offset:54272\n\t"
    "v_exp_f32 v226, v226\n\t"
    "v_mfma_f32_16x16x32_bf16 v[220:223], v[12:15], v[68:71], v[196:199]\n\t"
    "v_add_f32 v248, v248, v225\n\t"
    "s_waitcnt lgkmcnt(9)\n\t"
    "v_mfma_f32_16x16x32_bf16 v[128:131], v[24:27], v[104:107], v[128:131]\n\t"
    "ds_read_b64_tr_b16 v[88:89], v244 offset:1024\n\t"
    "v_cvt_pk_bf16_f32 v120, v224, v225\n\t"
    "v_exp_f32 v227, v227\n\t"
    "v_mfma_f32_16x16x32_bf16 v[136:139], v[24:27], v[108:111], v[136:139]\n\t"
    "ds_read_b64_tr_b16 v[90:91], v244 offset:5120\n\t"
    "v_add_f32 v248, v248, v226\n\t"
    "v_mfma_f32_16x16x32_bf16 v[208:211], v[28:31], v[80:83], v[208:211]\n\t"
    "ds_read_b128 v[92:95], v241 offset:50176\n\t"
    "v_exp_f32 v228, v228\n\t"
    "v_mfma_f32_16x16x32_bf16 v[216:219], v[28:31], v[84:87], v[216:219]\n\t"
    "v_add_f32 v248, v248, v227\n\t"
    "s_waitcnt lgkmcnt(9)\n\t"
    "v_mfma_f32_16x16x32_bf16 v[132:135], v[40:43], v[104:107], v[132:135]\n\t"
    "ds_read_b64_tr_b16 v[8:9], v245 offset:1024\n\t"
    "v_cvt_pk_bf16_f32 v121, v226, v227\n\t"
    "v_exp_f32 v229, v229\n\t"
    "v_mfma_f32_16x16x32_bf16 v[140:143], v[40:43], v[108:111], v[140:143]\n\t"
    "ds_read_b64_tr_b16 v[10:11], v245 offset:5120\n\t"
    "v_add_f32 v248, v248, v228\n\t"
    "v_mfma_f32_16x16x32_bf16 v[212:215], v[44:47], v[80:83], v[212:215]\n\t"
    "ds_read_b128 v[12:15], v241 offset:54272\n\t"
    "v_exp_f32 v230, v230\n\t"
    "v_mfma_f32_16x16x32_bf16 v[220:223], v[44:47], v[84:87], v[220:223]\n\t"
    "s_add_u32 m0, s60, 139264\n\t"
    "s_mov_b32 s63, 122880\n\t"
    "buffer_load_dwordx4 v242, s[44:47], s63 offen nt lds\n\t"
    "v_add_f32 v248, v248, v229\n\t"
    "s_waitcnt lgkmcnt(9)\n\t"
    "v_mfma_f32_16x16x32_bf16 v[144:147], v[56:59], v[104:107], v[144:147]\n\t"
    "ds_read_b64_tr_b16 v[24:25], v246 offset:1024\n\t"
    "v_cvt_pk_bf16_f32 v122, v228, v229\n\t"
    "v_exp_f32 v231, v231\n\t"
    "v_mfma_f32_16x16x32_bf16 v[152:155], v[56:59], v[108:111], v[152:155]\n\t"
    "ds_read_b64_tr_b16 v[26:27], v246 offset:5120\n\t"
    "v_add_f32 v248, v248, v230\n\t"
    "v_mfma_f32_16x16x32_bf16 v[208:211], v[60:63], v[96:99], v[208:211]\n\t"
    "ds_read_b64_tr_b16 v[28:29], v247 offset:1024\n\t"
    "s_setprio 2\n\t"
    "v_exp_f32 v232, v232\n\t"
    "v_mfma_f32_16x16x32_bf16 v[216:219], v[60:63], v[100:103], v[216:219]\n\t"
    "ds_read_b64_tr_b16 v[30:31], v247 offset:5120\n\t"
    "v_add_f32 v248, v248, v231\n\t"
    "s_waitcnt lgkmcnt(10)\n\t"
    "v_mfma_f32_16x16x32_bf16 v[148:151], v[72:75], v[104:107], v[148:151]\n\t"
    "ds_read_b128 v[40:43], v240 offset:57344\n\t"
    "v_cvt_pk_bf16_f32 v123, v230, v231\n\t"
    "v_mfma_f32_16x16x32_bf16 v[156:159], v[72:75], v[108:111], v[156:159]\n\t"
    "v_exp_f32 v233, v233\n\t"
    "v_add_f32 v249, v249, v232\n\t"
    "v_mfma_f32_16x16x32_bf16 v[212:215], v[76:79], v[96:99], v[212:215]\n\t"
    "ds_read_b128 v[44:47], v240 offset:61440\n\t"
    "v_exp_f32 v234, v234\n\t"
    "v_mfma_f32_16x16x32_bf16 v[220:223], v[76:79], v[100:103], v[220:223]\n\t"
    "v_add_f32 v249, v249, v233\n\t"
    "s_waitcnt lgkmcnt(9)\n\t"
    "v_mfma_f32_16x16x32_bf16 v[160:163], v[88:91], v[104:107], v[160:163]\n\t"
    "ds_read_b64_tr_b16 v[56:57], v244 offset:8192\n\t"
    "v_cvt_pk_bf16_f32 v124, v232, v233\n\t"
    "v_exp_f32 v235, v235\n\t"
    "v_mfma_f32_16x16x32_bf16 v[168:171], v[88:91], v[108:111], v[168:171]\n\t"
    "ds_read_b64_tr_b16 v[58:59], v244 offset:12288\n\t"
    "v_add_f32 v249, v249, v234\n\t"
    "v_mfma_f32_16x16x32_bf16 v[208:211], v[92:95], v[112:115], v[208:211]\n\t"
    "ds_read_b128 v[60:63], v241 offset:57344\n\t"
    "v_exp_f32 v236, v236\n\t"
    "v_mfma_f32_16x16x32_bf16 v[216:219], v[92:95], v[116:119], v[216:219]\n\t"
    "s_add_u32 m0, s60, 65536\n\t"
    "s_mov_b32 s63, 65536\n\t"
    "buffer_load_dwordx4 v243, s[48:51], s63 offen nt lds\n\t"
    "v_add_f32 v249, v249, v235\n\t"
    "s_waitcnt lgkmcnt(9)\n\t"
    "v_mfma_f32_16x16x32_bf16 v[164:167], v[8:11], v[104:107], v[164:167]\n\t"
    "ds_read_b64_tr_b16 v[72:73], v245 offset:8192\n\t"
    "v_cvt_pk_bf16_f32 v125, v234, v235\n\t"
    "v_exp_f32 v237, v237\n\t"
    "v_mfma_f32_16x16x32_bf16 v[172:175], v[8:11], v[108:111], v[172:175]\n\t"
    "ds_read_b64_tr_b16 v[74:75], v245 offset:12288\n\t"
    "v_add_f32 v249, v249, v236\n\t"
    "v_mfma_f32_16x16x32_bf16 v[212:215], v[12:15], v[112:115], v[212:215]\n\t"
    "ds_read_b128 v[76:79], v241 offset:61440\n\t"
    "v_exp_f32 v238, v238\n\t"
    "v_mfma_f32_16x16x32_bf16 v[220:223], v[12:15], v[116:119], v[220:223]\n\t"
    "v_add_f32 v249, v249, v237\n\t"
    "s_waitcnt lgkmcnt(8)\n\t"
    "v_mfma_f32_16x16x32_bf16 v[176:179], v[24:27], v[104:107], v[176:179]\n\t"
    "ds_read_b64_tr_b16 v[88:89], v246 offset:8192\n\t"
    "v_cvt_pk_bf16_f32 v126, v236, v237\n\t"
    "v_exp_f32 v239, v239\n\t"
    "v_mfma_f32_16x16x32_bf16 v[184:187], v[24:27], v[108:111], v[184:187]\n\t"
    "ds_read_b64_tr_b16 v[90:91], v246 offset:12288\n\t"
    "v_add_f32 v249, v249, v238\n\t"
    "v_mfma_f32_16x16x32_bf16 v[180:183], v[28:31], v[104:107], v[180:183]\n\t"
    "ds_read_b128 v[92:95], v240 offset:58368\n\t"
    "v_add_f32 v249, v249, v239\n\t"
    "v_mfma_f32_16x16x32_bf16 v[188:191], v[28:31], v[108:111], v[188:191]\n\t"
    "v_cvt_pk_bf16_f32 v127, v238, v239\n\t"
    "s_waitcnt lgkmcnt(9)\n\t"
    "v_mfma_f32_16x16x32_bf16 v[224:227], v[40:43], v[64:67], v[192:195]\n\t"
    "ds_read_b64_tr_b16 v[8:9], v247 offset:8192\n\t"
    "s_setprio 1\n\t"
    "v_exp_f32 v208, v208\n\t"
    "v_mfma_f32_16x16x32_bf16 v[232:235], v[40:43], v[68:71], v[196:199]\n\t"
    "ds_read_b64_tr_b16 v[10:11], v247 offset:12288\n\t"
    "v_exp_f32 v209, v209\n\t"
    "v_add_f32 v248, v248, v208\n\t"
    "v_mfma_f32_16x16x32_bf16 v[228:231], v[44:47], v[64:67], v[192:195]\n\t"
    "ds_read_b128 v[12:15], v240 offset:62464\n\t"
    "v_exp_f32 v210, v210\n\t"
    "v_mfma_f32_16x16x32_bf16 v[236:239], v[44:47], v[68:71], v[196:199]\n\t"
    "s_add_u32 m0, s60, 73728\n\t"
    "s_mov_b32 s63, 73728\n\t"
    "buffer_load_dwordx4 v243, s[48:51], s63 offen nt lds\n\t"
    "v_add_f32 v248, v248, v209\n\t"
    "s_waitcnt lgkmcnt(9)\n\t"
    "v_mfma_f32_16x16x32_bf16 v[128:131], v[56:59], v[120:123], v[128:131]\n\t"
    "ds_read_b64_tr_b16 v[24:25], v244 offset:9216\n\t"
    "v_cvt_pk_bf16_f32 v104, v208, v209\n\t"
    "v_exp_f32 v211, v211\n\t"
    "v_mfma_f32_16x16x32_bf16 v[136:139], v[56:59], v[124:127], v[136:139]\n\t"
    "ds_read_b64_tr_b16 v[26:27], v244 offset:13312\n\t"
    "v_add_f32 v248, v248, v210\n\t"
    "v_mfma_f32_16x16x32_bf16 v[224:227], v[60:63], v[80:83], v[224:227]\n\t"
    "ds_read_b128 v[28:31], v241 offset:58368\n\t"
    "v_exp_f32 v212, v212\n\t"
    "v_mfma_f32_16x16x32_bf16 v[232:235], v[60:63], v[84:87], v[232:235]\n\t"
    "v_add_f32 v248, v248, v211\n\t"
    "s_waitcnt lgkmcnt(9)\n\t"
    "v_mfma_f32_16x16x32_bf16 v[132:135], v[72:75], v[120:123], v[132:135]\n\t"
    "ds_read_b64_tr_b16 v[40:41], v245 offset:9216\n\t"
    "v_cvt_pk_bf16_f32 v105, v210, v211\n\t"
    "v_exp_f32 v213, v213\n\t"
    "v_mfma_f32_16x16x32_bf16 v[140:143], v[72:75], v[124:127], v[140:143]\n\t"
    "ds_read_b64_tr_b16 v[42:43], v245 offset:13312\n\t"
    "v_add_f32 v248, v248, v212\n\t"
    "v_mfma_f32_16x16x32_bf16 v[228:231], v[76:79], v[80:83], v[228:231]\n\t"
    "ds_read_b128 v[44:47], v241 offset:62464\n\t"
    "s_setprio 0\n\t"
    "v_mfma_f32_16x16x32_bf16 v[236:239], v[76:79], v[84:87], v[236:239]\n\t"
    "v_exp_f32 v214, v214\n\t"
    "v_add_f32 v248, v248, v213\n\t"
    "s_waitcnt lgkmcnt(9)\n\t"
    "v_mfma_f32_16x16x32_bf16 v[144:147], v[88:91], v[120:123], v[144:147]\n\t"
    "ds_read_b64_tr_b16 v[56:57], v246 offset:9216\n\t"
    "v_cvt_pk_bf16_f32 v106, v212, v213\n\t"
    "v_exp_f32 v215, v215\n\t"
    "v_mfma_f32_16x16x32_bf16 v[152:155], v[88:91], v[124:127], v[152:155]\n\t"
    "ds_read_b64_tr_b16 v[58:59], v246 offset:13312\n\t"
    "v_add_f32 v248, v248, v214\n\t"
    "v_mfma_f32_16x16x32_bf16 v[224:227], v[92:95], v[96:99], v[224:227]\n\t"
    "ds_read_b64_tr_b16 v[60:61], v247 offset:9216\n\t"
    "v_exp_f32 v216, v216\n\t"
    "v_mfma_f32_16x16x32_bf16 v[232:235], v[92:95], v[100:103], v[232:235]\n\t"
    "ds_read_b64_tr_b16 v[62:63], v247 offset:13312\n\t"
    "v_add_f32 v248, v248, v215\n\t"
    "s_waitcnt lgkmcnt(10)\n\t"
    "v_mfma_f32_16x16x32_bf16 v[148:151], v[8:11], v[120:123], v[148:151]\n\t"
    "v_cvt_pk_bf16_f32 v107, v214, v215\n\t"
    "v_exp_f32 v217, v217\n\t"
    "v_mfma_f32_16x16x32_bf16 v[156:159], v[8:11], v[124:127], v[156:159]\n\t"
    "v_add_f32 v249, v249, v216\n\t"
    "v_mfma_f32_16x16x32_bf16 v[228:231], v[12:15], v[96:99], v[228:231]\n\t"
    "v_exp_f32 v218, v218\n\t"
    "v_add_f32 v249, v249, v217\n\t"
    "v_mfma_f32_16x16x32_bf16 v[236:239], v[12:15], v[100:103], v[236:239]\n\t"
    "s_waitcnt vmcnt(10) lgkmcnt(0)\n\t"
    "s_barrier\n\t"
    "s_setprio 3\n\t"
    "ds_read_b128 v[72:75], v240 offset:0\n\t"
    "ds_read_b128 v[76:79], v240 offset:4096\n\t"
    "v_mfma_f32_16x16x32_bf16 v[160:163], v[24:27], v[120:123], v[160:163]\n\t"
    "ds_read_b64_tr_b16 v[88:89], v244 offset:16384\n\t"
    "v_cvt_pk_bf16_f32 v108, v216, v217\n\t"
    "v_exp_f32 v219, v219\n\t"
    "v_mfma_f32_16x16x32_bf16 v[168:171], v[24:27], v[124:127], v[168:171]\n\t"
    "ds_read_b64_tr_b16 v[90:91], v244 offset:20480\n\t"
    "v_add_f32 v249, v249, v218\n\t"
    "v_mfma_f32_16x16x32_bf16 v[224:227], v[28:31], v[112:115], v[224:227]\n\t"
    "ds_read_b128 v[92:95], v241 offset:0\n\t"
    "v_exp_f32 v220, v220\n\t"
    "v_mfma_f32_16x16x32_bf16 v[232:235], v[28:31], v[116:119], v[232:235]\n\t"
    "v_add_f32 v249, v249, v219\n\t"
    "v_mfma_f32_16x16x32_bf16 v[164:167], v[40:43], v[120:123], v[164:167]\n\t"
    "ds_read_b64_tr_b16 v[8:9], v245 offset:16384\n\t"
    "v_cvt_pk_bf16_f32 v109, v218, v219\n\t"
    "v_exp_f32 v221, v221\n\t"
    "v_mfma_f32_16x16x32_bf16 v[172:175], v[40:43], v[124:127], v[172:175]\n\t"
    "ds_read_b64_tr_b16 v[10:11], v245 offset:20480\n\t"
    "v_add_f32 v249, v249, v220\n\t"
    "v_mfma_f32_16x16x32_bf16 v[228:231], v[44:47], v[112:115], v[228:231]\n\t"
    "ds_read_b128 v[12:15], v241 offset:4096\n\t"
    "v_exp_f32 v222, v222\n\t"
    "v_mfma_f32_16x16x32_bf16 v[236:239], v[44:47], v[116:119], v[236:239]\n\t"
    "v_add_f32 v249, v249, v221\n\t"
    "v_mfma_f32_16x16x32_bf16 v[176:179], v[56:59], v[120:123], v[176:179]\n\t"
    "ds_read_b64_tr_b16 v[24:25], v246 offset:16384\n\t"
    "v_cvt_pk_bf16_f32 v110, v220, v221\n\t"
    "v_exp_f32 v223, v223\n\t"
    "v_mfma_f32_16x16x32_bf16 v[184:187], v[56:59], v[124:127], v[184:187]\n\t"
    "ds_read_b64_tr_b16 v[26:27], v246 offset:20480\n\t"
    "v_add_f32 v249, v249, v222\n\t"
    "v_mfma_f32_16x16x32_bf16 v[180:183], v[60:63], v[120:123], v[180:183]\n\t"
    "ds_read_b128 v[28:31], v240 offset:1024\n\t"
    "v_add_f32 v249, v249, v223\n\t"
    "v_mfma_f32_16x16x32_bf16 v[188:191], v[60:63], v[124:127], v[188:191]\n\t"
    "s_add_u32 m0, s60, 147456\n\t"
    "s_mov_b32 s63, 131072\n\t"
    "buffer_load_dwordx4 v242, s[44:47], s63 offen nt lds\n\t"
    "v_cvt_pk_bf16_f32 v111, v222, v223\n\t"
    "s_waitcnt lgkmcnt(9)\n\t"
    "v_mfma_f32_16x16x32_bf16 v[208:211], v[72:75], v[64:67], v[192:195]\n\t"
    "ds_read_b64_tr_b16 v[40:41], v247 offset:16384\n\t"
    "v_exp_f32 v224, v224\n\t"
    "v_exp_f32 v225, v225\n\t"
    "v_mfma_f32_16x16x32_bf16 v[216:219], v[72:75], v[68:71], v[196:199]\n\t"
    "ds_read_b64_tr_b16 v[42:43], v247 offset:20480\n\t"
    "v_add_f32 v248, v248, v224\n\t"
    "v_mfma_f32_16x16x32_bf16 v[212:215], v[76:79], v[64:67], v[192:195]\n\t"
    "ds_read_b128 v[44:47], v240 offset:5120\n\t"
    "v_exp_f32 v226, v226\n\t"
    "v_mfma_f32_16x16x32_bf16 v[220:223], v[76:79], v[68:71], v[196:199]\n\t"
    "v_add_f32 v248, v248, v225\n\t"
    "s_waitcnt lgkmcnt(9)\n\t"
    "v_mfma_f32_16x16x32_bf16 v[128:131], v[88:91], v[104:107], v[128:131]\n\t"
    "ds_read_b64_tr_b16 v[56:57], v244 offset:17408\n\t"
    "v_cvt_pk_bf16_f32 v120, v224, v225\n\t"
    "v_exp_f32 v227, v227\n\t"
    "v_mfma_f32_16x16x32_bf16 v[136:139], v[88:91], v[108:111], v[136:139]\n\t"
    "ds_read_b64_tr_b16 v[58:59], v244 offset:21504\n\t"
    "v_add_f32 v248, v248, v226\n\t"
    "v_mfma_f32_16x16x32_bf16 v[208:211], v[92:95], v[80:83], v[208:211]\n\t"
    "ds_read_b128 v[60:63], v241 offset:1024\n\t"
    "v_exp_f32 v228, v228\n\t"
    "v_mfma_f32_16x16x32_bf16 v[216:219], v[92:95], v[84:87], v[216:219]\n\t"
    "v_add_f32 v248, v248, v227\n\t"
    "s_waitcnt lgkmcnt(9)\n\t"
    "v_mfma_f32_16x16x32_bf16 v[132:135], v[8:11], v[104:107], v[132:135]\n\t"
    "ds_read_b64_tr_b16 v[72:73], v245 offset:17408\n\t"
    "v_cvt_pk_bf16_f32 v121, v226, v227\n\t"
    "v_exp_f32 v229, v229\n\t"
    "v_mfma_f32_16x16x32_bf16 v[140:143], v[8:11], v[108:111], v[140:143]\n\t"
    "ds_read_b64_tr_b16 v[74:75], v245 offset:21504\n\t"
    "v_add_f32 v248, v248, v228\n\t"
    "v_mfma_f32_16x16x32_bf16 v[212:215], v[12:15], v[80:83], v[212:215]\n\t"
    "ds_read_b128 v[76:79], v241 offset:5120\n\t"
    "v_exp_f32 v230, v230\n\t"
    "v_mfma_f32_16x16x32_bf16 v[220:223], v[12:15], v[84:87], v[220:223]\n\t"
    "s_add_u32 m0, s60, 155648\n\t"
    "s_mov_b32 s63, 139264\n\t"
    "buffer_load_dwordx4 v242, s[44:47], s63 offen nt lds\n\t"
    "v_add_f32 v248, v248, v229\n\t"
    "s_waitcnt lgkmcnt(9)\n\t"
    "v_mfma_f32_16x16x32_bf16 v[144:147], v[24:27], v[104:107], v[144:147]\n\t"
    "ds_read_b64_tr_b16 v[88:89], v246 offset:17408\n\t"
    "v_cvt_pk_bf16_f32 v122, v228, v229\n\t"
    "v_exp_f32 v231, v231\n\t"
    "v_mfma_f32_16x16x32_bf16 v[152:155], v[24:27], v[108:111], v[152:155]\n\t"
    "ds_read_b64_tr_b16 v[90:91], v246 offset:21504\n\t"
    "v_add_f32 v248, v248, v230\n\t"
    "v_mfma_f32_16x16x32_bf16 v[208:211], v[28:31], v[96:99], v[208:211]\n\t"
    "ds_read_b64_tr_b16 v[92:93], v247 offset:17408\n\t"
    "s_setprio 2\n\t"
    "v_exp_f32 v232, v232\n\t"
    "v_mfma_f32_16x16x32_bf16 v[216:219], v[28:31], v[100:103], v[216:219]\n\t"
    "ds_read_b64_tr_b16 v[94:95], v247 offset:21504\n\t"
    "v_add_f32 v248, v248, v231\n\t"
    "s_waitcnt lgkmcnt(10)\n\t"
    "v_mfma_f32_16x16x32_bf16 v[148:151], v[40:43], v[104:107], v[148:151]\n\t"
    "ds_read_b128 v[8:11], v240 offset:8192\n\t"
    "v_cvt_pk_bf16_f32 v123, v230, v231\n\t"
    "v_mfma_f32_16x16x32_bf16 v[156:159], v[40:43], v[108:111], v[156:159]\n\t"
    "v_exp_f32 v233, v233\n\t"
    "v_add_f32 v249, v249, v232\n\t"
    "v_mfma_f32_16x16x32_bf16 v[212:215], v[44:47], v[96:99], v[212:215]\n\t"
    "ds_read_b128 v[12:15], v240 offset:12288\n\t"
    "v_exp_f32 v234, v234\n\t"
    "v_mfma_f32_16x16x32_bf16 v[220:223], v[44:47], v[100:103], v[220:223]\n\t"
    "v_add_f32 v249, v249, v233\n\t"
    "s_waitcnt lgkmcnt(9)\n\t"
    "v_mfma_f32_16x16x32_bf16 v[160:163], v[56:59], v[104:107], v[160:163]\n\t"
    "ds_read_b64_tr_b16 v[24:25], v244 offset:24576\n\t"
    "v_cvt_pk_bf16_f32 v124, v232, v233\n\t"
    "v_exp_f32 v235, v235\n\t"
    "v_mfma_f32_16x16x32_bf16 v[168:171], v[56:59], v[108:111], v[168:171]\n\t"
    "ds_read_b64_tr_b16 v[26:27], v244 offset:28672\n\t"
    "v_add_f32 v249, v249, v234\n\t"
    "v_mfma_f32_16x16x32_bf16 v[208:211], v[60:63], v[112:115], v[208:211]\n\t"
    "ds_read_b128 v[28:31], v241 offset:8192\n\t"
    "v_exp_f32 v236, v236\n\t"
    "v_mfma_f32_16x16x32_bf16 v[216:219], v[60:63], v[116:119], v[216:219]\n\t"
    "s_add_u32 m0, s60, 32768\n\t"
    "s_mov_b32 s63, 81920\n\t"
    "buffer_load_dwordx4 v243, s[48:51], s63 offen nt lds\n\t"
    "v_add_f32 v249, v249, v235\n\t"
    "s_waitcnt lgkmcnt(9)\n\t"
    "v_mfma_f32_16x16x32_bf16 v[164:167], v[72:75], v[104:107], v[164:167]\n\t"
    "ds_read_b64_tr_b16 v[40:41], v245 offset:24576\n\t"
    "v_cvt_pk_bf16_f32 v125, v234, v235\n\t"
    "v_exp_f32 v237, v237\n\t"
    "v_mfma_f32_16x16x32_bf16 v[172:175], v[72:75], v[108:111], v[172:175]\n\t"
    "ds_read_b64_tr_b16 v[42:43], v245 offset:28672\n\t"
    "v_add_f32 v249, v249, v236\n\t"
    "v_mfma_f32_16x16x32_bf16 v[212:215], v[76:79], v[112:115], v[212:215]\n\t"
    "ds_read_b128 v[44:47], v241 offset:12288\n\t"
    "v_exp_f32 v238, v238\n\t"
    "v_mfma_f32_16x16x32_bf16 v[220:223], v[76:79], v[116:119], v[220:223]\n\t"
    "v_add_f32 v249, v249, v237\n\t"
    "s_waitcnt lgkmcnt(8)\n\t"
    "v_mfma_f32_16x16x32_bf16 v[176:179], v[88:91], v[104:107], v[176:179]\n\t"
    "ds_read_b64_tr_b16 v[56:57], v246 offset:24576\n\t"
    "v_cvt_pk_bf16_f32 v126, v236, v237\n\t"
    "v_exp_f32 v239, v239\n\t"
    "v_mfma_f32_16x16x32_bf16 v[184:187], v[88:91], v[108:111], v[184:187]\n\t"
    "ds_read_b64_tr_b16 v[58:59], v246 offset:28672\n\t"
    "v_add_f32 v249, v249, v238\n\t"
    "v_mfma_f32_16x16x32_bf16 v[180:183], v[92:95], v[104:107], v[180:183]\n\t"
    "ds_read_b128 v[60:63], v240 offset:9216\n\t"
    "v_add_f32 v249, v249, v239\n\t"
    "v_mfma_f32_16x16x32_bf16 v[188:191], v[92:95], v[108:111], v[188:191]\n\t"
    "v_cvt_pk_bf16_f32 v127, v238, v239\n\t"
    "s_waitcnt lgkmcnt(9)\n\t"
    "v_mfma_f32_16x16x32_bf16 v[224:227], v[8:11], v[64:67], v[192:195]\n\t"
    "ds_read_b64_tr_b16 v[72:73], v247 offset:24576\n\t"
    "s_setprio 1\n\t"
    "v_exp_f32 v208, v208\n\t"
    "v_mfma_f32_16x16x32_bf16 v[232:235], v[8:11], v[68:71], v[196:199]\n\t"
    "ds_read_b64_tr_b16 v[74:75], v247 offset:28672\n\t"
    "v_exp_f32 v209, v209\n\t"
    "v_add_f32 v248, v248, v208\n\t"
    "v_mfma_f32_16x16x32_bf16 v[228:231], v[12:15], v[64:67], v[192:195]\n\t"
    "ds_read_b128 v[76:79], v240 offset:13312\n\t"
    "v_exp_f32 v210, v210\n\t"
    "v_mfma_f32_16x16x32_bf16 v[236:239], v[12:15], v[68:71], v[196:199]\n\t"
    "s_add_u32 m0, s60, 40960\n\t"
    "s_mov_b32 s63, 90112\n\t"
    "buffer_load_dwordx4 v243, s[48:51], s63 offen nt lds\n\t"
    "v_add_f32 v248, v248, v209\n\t"
    "s_waitcnt lgkmcnt(9)\n\t"
    "v_mfma_f32_16x16x32_bf16 v[128:131], v[24:27], v[120:123], v[128:131]\n\t"
    "ds_read_b64_tr_b16 v[88:89], v244 offset:25600\n\t"
    "v_cvt_pk_bf16_f32 v104, v208, v209\n\t"
    "v_exp_f32 v211, v211\n\t"
    "v_mfma_f32_16x16x32_bf16 v[136:139], v[24:27], v[124:127], v[136:139]\n\t"
    "ds_read_b64_tr_b16 v[90:91], v244 offset:29696\n\t"
    "v_add_f32 v248, v248, v210\n\t"
    "v_mfma_f32_16x16x32_bf16 v[224:227], v[28:31], v[80:83], v[224:227]\n\t"
    "ds_read_b128 v[92:95], v241 offset:9216\n\t"
    "v_exp_f32 v212, v212\n\t"
    "v_mfma_f32_16x16x32_bf16 v[232:235], v[28:31], v[84:87], v[232:235]\n\t"
    "v_add_f32 v248, v248, v211\n\t"
    "s_waitcnt lgkmcnt(9)\n\t"
    "v_mfma_f32_16x16x32_bf16 v[132:135], v[40:43], v[120:123], v[132:135]\n\t"
    "ds_read_b64_tr_b16 v[8:9], v245 offset:25600\n\t"
    "v_cvt_pk_bf16_f32 v105, v210, v211\n\t"
    "v_exp_f32 v213, v213\n\t"
    "v_mfma_f32_16x16x32_bf16 v[140:143], v[40:43], v[124:127], v[140:143]\n\t"
    "ds_read_b64_tr_b16 v[10:11], v245 offset:29696\n\t"
    "v_add_f32 v248, v248, v212\n\t"
    "v_mfma_f32_16x16x32_bf16 v[228:231], v[44:47], v[80:83], v[228:231]\n\t"
    "ds_read_b128 v[12:15], v241 offset:13312\n\t"
    "s_setprio 0\n\t"
    "v_mfma_f32_16x16x32_bf16 v[236:239], v[44:47], v[84:87], v[236:239]\n\t"
    "v_exp_f32 v214, v214\n\t"
    "v_add_f32 v248, v248, v213\n\t"
    "s_waitcnt lgkmcnt(9)\n\t"
    "v_mfma_f32_16x16x32_bf16 v[144:147], v[56:59], v[120:123], v[144:147]\n\t"
    "ds_read_b64_tr_b16 v[24:25], v246 offset:25600\n\t"
    "v_cvt_pk_bf16_f32 v106, v212, v213\n\t"
    "v_exp_f32 v215, v215\n\t"
    "v_mfma_f32_16x16x32_bf16 v[152:155], v[56:59], v[124:127], v[152:155]\n\t"
    "ds_read_b64_tr_b16 v[26:27], v246 offset:29696\n\t"
    "v_add_f32 v248, v248, v214\n\t"
    "v_mfma_f32_16x16x32_bf16 v[224:227], v[60:63], v[96:99], v[224:227]\n\t"
    "ds_read_b64_tr_b16 v[28:29], v247 offset:25600\n\t"
    "v_exp_f32 v216, v216\n\t"
    "v_mfma_f32_16x16x32_bf16 v[232:235], v[60:63], v[100:103], v[232:235]\n\t"
    "ds_read_b64_tr_b16 v[30:31], v247 offset:29696\n\t"
    "v_add_f32 v248, v248, v215\n\t"
    "s_waitcnt lgkmcnt(10)\n\t"
    "v_mfma_f32_16x16x32_bf16 v[148:151], v[72:75], v[120:123], v[148:151]\n\t"
    "v_cvt_pk_bf16_f32 v107, v214, v215\n\t"
    "v_exp_f32 v217, v217\n\t"
    "v_mfma_f32_16x16x32_bf16 v[156:159], v[72:75], v[124:127], v[156:159]\n\t"
    "v_add_f32 v249, v249, v216\n\t"
    "v_mfma_f32_16x16x32_bf16 v[228:231], v[76:79], v[96:99], v[228:231]\n\t"
    "v_exp_f32 v218, v218\n\t"
    "v_add_f32 v249, v249, v217\n\t"
    "v_mfma_f32_16x16x32_bf16 v[236:239], v[76:79], v[100:103], v[236:239]\n\t"
    "s_waitcnt vmcnt(12) lgkmcnt(0)\n\t"
    "s_barrier\n\t"
    "s_setprio 3\n\t"
    "ds_read_b128 v[40:43], v202 offset:0\n\t"
    "ds_read_b128 v[44:47], v202 offset:4096\n\t"
    "v_mfma_f32_16x16x32_bf16 v[160:163], v[88:91], v[120:123], v[160:163]\n\t"
    "ds_read_b64_tr_b16 v[56:57], v244 offset:49152\n\t"
    "v_cvt_pk_bf16_f32 v108, v216, v217\n\t"
    "v_exp_f32 v219, v219\n\t"
    "v_mfma_f32_16x16x32_bf16 v[168:171], v[88:91], v[124:127], v[168:171]\n\t"
    "ds_read_b64_tr_b16 v[58:59], v244 offset:53248\n\t"
    "v_add_f32 v249, v249, v218\n\t"
    "v_mfma_f32_16x16x32_bf16 v[224:227], v[92:95], v[112:115], v[224:227]\n\t"
    "ds_read_b128 v[60:63], v203 offset:0\n\t"
    "v_exp_f32 v220, v220\n\t"
    "v_mfma_f32_16x16x32_bf16 v[232:235], v[92:95], v[116:119], v[232:235]\n\t"
    "v_add_f32 v249, v249, v219\n\t"
    "v_mfma_f32_16x16x32_bf16 v[164:167], v[8:11], v[120:123], v[164:167]\n\t"
    "ds_read_b64_tr_b16 v[72:73], v245 offset:49152\n\t"
    "v_cvt_pk_bf16_f32 v109, v218, v219\n\t"
    "v_exp_f32 v221, v221\n\t"
    "v_mfma_f32_16x16x32_bf16 v[172:175], v[8:11], v[124:127], v[172:175]\n\t"
    "ds_read_b64_tr_b16 v[74:75], v245 offset:53248\n\t"
    "v_add_f32 v249, v249, v220\n\t"
    "v_mfma_f32_16x16x32_bf16 v[228:231], v[12:15], v[112:115], v[228:231]\n\t"
    "ds_read_b128 v[76:79], v203 offset:4096\n\t"
    "v_exp_f32 v222, v222\n\t"
    "v_mfma_f32_16x16x32_bf16 v[236:239], v[12:15], v[116:119], v[236:239]\n\t"
    "v_add_f32 v249, v249, v221\n\t"
    "v_mfma_f32_16x16x32_bf16 v[176:179], v[24:27], v[120:123], v[176:179]\n\t"
    "ds_read_b64_tr_b16 v[88:89], v246 offset:49152\n\t"
    "v_cvt_pk_bf16_f32 v110, v220, v221\n\t"
    "v_exp_f32 v223, v223\n\t"
    "v_mfma_f32_16x16x32_bf16 v[184:187], v[24:27], v[124:127], v[184:187]\n\t"
    "ds_read_b64_tr_b16 v[90:91], v246 offset:53248\n\t"
    "v_add_f32 v249, v249, v222\n\t"
    "v_mfma_f32_16x16x32_bf16 v[180:183], v[28:31], v[120:123], v[180:183]\n\t"
    "ds_read_b128 v[92:95], v202 offset:1024\n\t"
    "v_add_f32 v249, v249, v223\n\t"
    "v_mfma_f32_16x16x32_bf16 v[188:191], v[28:31], v[124:127], v[188:191]\n\t"
    "s_add_u32 m0, s60, 98304\n\t"
    "s_mov_b32 s63, 147456\n\t"
    "buffer_load_dwordx4 v242, s[44:47], s63 offen nt lds\n\t"
    "v_cvt_pk_bf16_f32 v111, v222, v223\n\t"
    "s_waitcnt lgkmcnt(9)\n\t"
    "v_mfma_f32_16x16x32_bf16 v[208:211], v[40:43], v[64:67], v[192:195]\n\t"
    "ds_read_b64_tr_b16 v[8:9], v247 offset:49152\n\t"
    "v_exp_f32 v224, v224\n\t"
    "v_exp_f32 v225, v225\n\t"
    "v_mfma_f32_16x16x32_bf16 v[216:219], v[40:43], v[68:71], v[196:199]\n\t"
    "ds_read_b64_tr_b16 v[10:11], v247 offset:53248\n\t"
    "v_add_f32 v248, v248, v224\n\t"
    "v_mfma_f32_16x16x32_bf16 v[212:215], v[44:47], v[64:67], v[192:195]\n\t"
    "ds_read_b128 v[12:15], v202 offset:5120\n\t"
    "v_exp_f32 v226, v226\n\t"
    "v_mfma_f32_16x16x32_bf16 v[220:223], v[44:47], v[68:71], v[196:199]\n\t"
    "v_add_f32 v248, v248, v225\n\t"
    "s_waitcnt lgkmcnt(9)\n\t"
    "v_mfma_f32_16x16x32_bf16 v[128:131], v[56:59], v[104:107], v[128:131]\n\t"
    "ds_read_b64_tr_b16 v[24:25], v244 offset:50176\n\t"
    "v_cvt_pk_bf16_f32 v120, v224, v225\n\t"
    "v_exp_f32 v227, v227\n\t"
    "v_mfma_f32_16x16x32_bf16 v[136:139], v[56:59], v[108:111], v[136:139]\n\t"
    "ds_read_b64_tr_b16 v[26:27], v244 offset:54272\n\t"
    "v_add_f32 v248, v248, v226\n\t"
    "v_mfma_f32_16x16x32_bf16 v[208:211], v[60:63], v[80:83], v[208:211]\n\t"
    "ds_read_b128 v[28:31], v203 offset:1024\n\t"
    "v_exp_f32 v228, v228\n\t"
    "v_mfma_f32_16x16x32_bf16 v[216:219], v[60:63], v[84:87], v[216:219]\n\t"
    "v_add_f32 v248, v248, v227\n\t"
    "s_waitcnt lgkmcnt(9)\n\t"
    "v_mfma_f32_16x16x32_bf16 v[132:135], v[72:75], v[104:107], v[132:135]\n\t"
    "ds_read_b64_tr_b16 v[40:41], v245 offset:50176\n\t"
    "v_cvt_pk_bf16_f32 v121, v226, v227\n\t"
    "v_exp_f32 v229, v229\n\t"
    "v_mfma_f32_16x16x32_bf16 v[140:143], v[72:75], v[108:111], v[140:143]\n\t"
    "ds_read_b64_tr_b16 v[42:43], v245 offset:54272\n\t"
    "v_add_f32 v248, v248, v228\n\t"
    "v_mfma_f32_16x16x32_bf16 v[212:215], v[76:79], v[80:83], v[212:215]\n\t"
    "ds_read_b128 v[44:47], v203 offset:5120\n\t"
    "v_exp_f32 v230, v230\n\t"
    "v_mfma_f32_16x16x32_bf16 v[220:223], v[76:79], v[84:87], v[220:223]\n\t"
    "s_add_u32 m0, s60, 106496\n\t"
    "s_mov_b32 s63, 155648\n\t"
    "buffer_load_dwordx4 v242, s[44:47], s63 offen nt lds\n\t"
    "v_add_f32 v248, v248, v229\n\t"
    "s_waitcnt lgkmcnt(9)\n\t"
    "v_mfma_f32_16x16x32_bf16 v[144:147], v[88:91], v[104:107], v[144:147]\n\t"
    "ds_read_b64_tr_b16 v[56:57], v246 offset:50176\n\t"
    "v_cvt_pk_bf16_f32 v122, v228, v229\n\t"
    "v_exp_f32 v231, v231\n\t"
    "v_mfma_f32_16x16x32_bf16 v[152:155], v[88:91], v[108:111], v[152:155]\n\t"
    "ds_read_b64_tr_b16 v[58:59], v246 offset:54272\n\t"
    "PAD4_%=: s_cbranch_execz PAD12_%=\n\t"
    "v_add_f32 v248, v248, v230\n\t"
    "v_mfma_f32_16x16x32_bf16 v[208:211], v[92:95], v[96:99], v[208:211]\n\t"
    "ds_read_b64_tr_b16 v[60:61], v247 offset:50176\n\t"
    "s_setprio 2\n\t"
    "v_exp_f32 v232, v232\n\t"
    "v_mfma_f32_16x16x32_bf16 v[216:219], v[92:95], v[100:103], v[216:219]\n\t"
    "ds_read_b64_tr_b16 v[62:63], v247 offset:54272\n\t"
    "v_add_f32 v248, v248, v231\n\t"
    "s_waitcnt lgkmcnt(10)\n\t"
    "v_mfma_f32_16x16x32_bf16 v[148:151], v[8:11], v[104:107], v[148:151]\n\t"
    "ds_read_b128 v[72:75], v202 offset:8192\n\t"
    "v_cvt_pk_bf16_f32 v123, v230, v231\n\t"
    "v_mfma_f32_16x16x32_bf16 v[156:159], v[8:11], v[108:111], v[156:159]\n\t"
    "v_exp_f32 v233, v233\n\t"
    "v_add_f32 v249, v249, v232\n\t"
    "v_mfma_f32_16x16x32_bf16 v[212:215], v[12:15], v[96:99], v[212:215]\n\t"
    "ds_read_b128 v[76:79], v202 offset:12288\n\t"
    "v_exp_f32 v234, v234\n\t"
    "v_mfma_f32_16x16x32_bf16 v[220:223], v[12:15], v[100:103], v[220:223]\n\t"
    "v_add_f32 v249, v249, v233\n\t"
    "s_waitcnt lgkmcnt(9)\n\t"
    "v_mfma_f32_16x16x32_bf16 v[160:163], v[24:27], v[104:107], v[160:163]\n\t"
    "ds_read_b64_tr_b16 v[88:89], v244 offset:57344\n\t"
    "v_cvt_pk_bf16_f32 v124, v232, v233\n\t"
    "v_exp_f32 v235, v235\n\t"
    "v_mfma_f32_16x16x32_bf16 v[168:171], v[24:27], v[108:111], v[168:171]\n\t"
    "ds_read_b64_tr_b16 v[90:91], v244 offset:61440\n\t"
    "v_add_f32 v249, v249, v234\n\t"
    "v_mfma_f32_16x16x32_bf16 v[208:211], v[28:31], v[112:115], v[208:211]\n\t"
    "ds_read_b128 v[92:95], v203 offset:8192\n\t"
    "v_exp_f32 v236, v236\n\t"
    "v_mfma_f32_16x16x32_bf16 v[216:219], v[28:31], v[116:119], v[216:219]\n\t"
    "s_add_u32 m0, s60, 49152\n\t"
    "s_mov_b32 s63, 98304\n\t"
    "buffer_load_dwordx4 v243, s[48:51], s63 offen nt lds\n\t"
    "v_add_f32 v249, v249, v235\n\t"
    "s_waitcnt lgkmcnt(9)\n\t"
    "v_mfma_f32_16x16x32_bf16 v[164:167], v[40:43], v[104:107], v[164:167]\n\t"
    "ds_read_b64_tr_b16 v[8:9], v245 offset:57344\n\t"
    "v_cvt_pk_bf16_f32 v125, v234, v235\n\t"
    "v_exp_f32 v237, v237\n\t"
    "v_mfma_f32_16x16x32_bf16 v[172:175], v[40:43], v[108:111], v[172:175]\n\t"
    "ds_read_b64_tr_b16 v[10:11], v245 offset:61440\n\t"
    "v_add_f32 v249, v249, v236\n\t"
    "v_mfma_f32_16x16x32_bf16 v[212:215], v[44:47], v[112:115], v[212:215]\n\t"
    "ds_read_b128 v[12:15], v203 offset:12288\n\t"
    "v_exp_f32 v238, v238\n\t"
    "v_mfma_f32_16x16x32_bf16 v[220:223], v[44:47], v[116:119], v[220:223]\n\t"
    "v_add_f32 v249, v249, v237\n\t"
    "s_waitcnt lgkmcnt(8)\n\t"
    "v_mfma_f32_16x16x32_bf16 v[176:179], v[56:59], v[104:107], v[176:179]\n\t"
    "ds_read_b64_tr_b16 v[24:25], v246 offset:57344\n\t"
    "v_cvt_pk_bf16_f32 v126, v236, v237\n\t"
    "v_exp_f32 v239, v239\n\t"
    "v_mfma_f32_16x16x32_bf16 v[184:187], v[56:59], v[108:111], v[184:187]\n\t"
    "ds_read_b64_tr_b16 v[26:27], v246 offset:61440\n\t"
    "v_add_f32 v249, v249, v238\n\t"
    "v_mfma_f32_16x16x32_bf16 v[180:183], v[60:63], v[104:107], v[180:183]\n\t"
    "ds_read_b128 v[28:31], v202 offset:9216\n\t"
    "v_add_f32 v249, v249, v239\n\t"
    "v_mfma_f32_16x16x32_bf16 v[188:191], v[60:63], v[108:111], v[188:191]\n\t"
    "v_cvt_pk_bf16_f32 v127, v238, v239\n\t"
    "s_waitcnt lgkmcnt(9)\n\t"
    "v_mfma_f32_16x16x32_bf16 v[224:227], v[72:75], v[64:67], v[192:195]\n\t"
    "ds_read_b64_tr_b16 v[40:41], v247 offset:57344\n\t"
    "s_setprio 1\n\t"
    "v_exp_f32 v208, v208\n\t"
    "v_mfma_f32_16x16x32_bf16 v[232:235], v[72:75], v[68:71], v[196:199]\n\t"
    "ds_read_b64_tr_b16 v[42:43], v247 offset:61440\n\t"
    "v_exp_f32 v209, v209\n\t"
    "v_add_f32 v248, v248, v208\n\t"
    "v_mfma_f32_16x16x32_bf16 v[228:231], v[76:79], v[64:67], v[192:195]\n\t"
    "ds_read_b128 v[44:47], v202 offset:13312\n\t"
    "v_exp_f32 v210, v210\n\t"
    "v_mfma_f32_16x16x32_bf16 v[236:239], v[76:79], v[68:71], v[196:199]\n\t"
    "s_add_u32 m0, s60, 57344\n\t"
    "s_mov_b32 s63, 106496\n\t"
    "buffer_load_dwordx4 v243, s[48:51], s63 offen nt lds\n\t"
    "v_add_f32 v248, v248, v209\n\t"
    "s_waitcnt lgkmcnt(9)\n\t"
    "v_mfma_f32_16x16x32_bf16 v[128:131], v[88:91], v[120:123], v[128:131]\n\t"
    "ds_read_b64_tr_b16 v[56:57], v244 offset:58368\n\t"
    "v_cvt_pk_bf16_f32 v104, v208, v209\n\t"
    "v_exp_f32 v211, v211\n\t"
    "v_mfma_f32_16x16x32_bf16 v[136:139], v[88:91], v[124:127], v[136:139]\n\t"
    "ds_read_b64_tr_b16 v[58:59], v244 offset:62464\n\t"
    "v_add_f32 v248, v248, v210\n\t"
    "v_mfma_f32_16x16x32_bf16 v[224:227], v[92:95], v[80:83], v[224:227]\n\t"
    "ds_read_b128 v[60:63], v203 offset:9216\n\t"
    "v_exp_f32 v212, v212\n\t"
    "v_mfma_f32_16x16x32_bf16 v[232:235], v[92:95], v[84:87], v[232:235]\n\t"
    "v_add_f32 v248, v248, v211\n\t"
    "s_waitcnt lgkmcnt(9)\n\t"
    "v_mfma_f32_16x16x32_bf16 v[132:135], v[8:11], v[120:123], v[132:135]\n\t"
    "ds_read_b64_tr_b16 v[72:73], v245 offset:58368\n\t"
    "v_cvt_pk_bf16_f32 v105, v210, v211\n\t"
    "v_exp_f32 v213, v213\n\t"
    "v_mfma_f32_16x16x32_bf16 v[140:143], v[8:11], v[124:127], v[140:143]\n\t"
    "ds_read_b64_tr_b16 v[74:75], v245 offset:62464\n\t"
    "v_add_f32 v248, v248, v212\n\t"
    "v_mfma_f32_16x16x32_bf16 v[228:231], v[12:15], v[80:83], v[228:231]\n\t"
    "ds_read_b128 v[76:79], v203 offset:13312\n\t"
    "s_setprio 0\n\t"
    "v_mfma_f32_16x16x32_bf16 v[236:239], v[12:15], v[84:87], v[236:239]\n\t"
    "v_exp_f32 v214, v214\n\t"
    "v_add_f32 v248, v248, v213\n\t"
    "s_waitcnt lgkmcnt(9)\n\t"
    "v_mfma_f32_16x16x32_bf16 v[144:147], v[24:27], v[120:123], v[144:147]\n\t"
    "ds_read_b64_tr_b16 v[88:89], v246 offset:58368\n\t"
    "v_cvt_pk_bf16_f32 v106, v212, v213\n\t"
    "v_exp_f32 v215, v215\n\t"
    "v_mfma_f32_16x16x32_bf16 v[152:155], v[24:27], v[124:127], v[152:155]\n\t"
    "ds_read_b64_tr_b16 v[90:91], v246 offset:62464\n\t"
    "v_add_f32 v248, v248, v214\n\t"
    "v_mfma_f32_16x16x32_bf16 v[224:227], v[28:31], v[96:99], v[224:227]\n\t"
    "ds_read_b64_tr_b16 v[92:93], v247 offset:58368\n\t"
    "v_exp_f32 v216, v216\n\t"
    "v_mfma_f32_16x16x32_bf16 v[232:235], v[28:31], v[100:103], v[232:235]\n\t"
    "ds_read_b64_tr_b16 v[94:95], v247 offset:62464\n\t"
    "v_add_f32 v248, v248, v215\n\t"
    "s_waitcnt lgkmcnt(10)\n\t"
    "v_mfma_f32_16x16x32_bf16 v[148:151], v[40:43], v[120:123], v[148:151]\n\t"
    "v_cvt_pk_bf16_f32 v107, v214, v215\n\t"
    "v_exp_f32 v217, v217\n\t"
    "v_mfma_f32_16x16x32_bf16 v[156:159], v[40:43], v[124:127], v[156:159]\n\t"
    "v_add_f32 v249, v249, v216\n\t"
    "v_mfma_f32_16x16x32_bf16 v[228:231], v[44:47], v[96:99], v[228:231]\n\t"
    "v_exp_f32 v218, v218\n\t"
    "v_add_f32 v249, v249, v217\n\t"
    "v_mfma_f32_16x16x32_bf16 v[236:239], v[44:47], v[100:103], v[236:239]\n\t"
    "s_waitcnt vmcnt(8) lgkmcnt(0)\n\t"
    "s_barrier\n\t"
    "s_setprio 3\n\t"
    "ds_read_b128 v[8:11], v202 offset:16384\n\t"
    "ds_read_b128 v[12:15], v202 offset:20480\n\t"
    "v_mfma_f32_16x16x32_bf16 v[160:163], v[56:59], v[120:123], v[160:163]\n\t"
    "ds_read_b64_tr_b16 v[24:25], v244 offset:32768\n\t"
    "v_cvt_pk_bf16_f32 v108, v216, v217\n\t"
    "v_exp_f32 v219, v219\n\t"
    "v_mfma_f32_16x16x32_bf16 v[168:171], v[56:59], v[124:127], v[168:171]\n\t"
    "ds_read_b64_tr_b16 v[26:27], v244 offset:36864\n\t"
    "v_add_f32 v249, v249, v218\n\t"
    "v_mfma_f32_16x16x32_bf16 v[224:227], v[60:63], v[112:115], v[224:227]\n\t"
    "ds_read_b128 v[28:31], v203 offset:16384\n\t"
    "v_exp_f32 v220, v220\n\t"
    "v_mfma_f32_16x16x32_bf16 v[232:235], v[60:63], v[116:119], v[232:235]\n\t"
    "v_add_f32 v249, v249, v219\n\t"
    "v_mfma_f32_16x16x32_bf16 v[164:167], v[72:75], v[120:123], v[164:167]\n\t"
    "ds_read_b64_tr_b16 v[40:41], v245 offset:32768\n\t"
    "v_cvt_pk_bf16_f32 v109, v218, v219\n\t"
    "v_exp_f32 v221, v221\n\t"
    "v_mfma_f32_16x16x32_bf16 v[172:175], v[72:75], v[124:127], v[172:175]\n\t"
    "ds_read_b64_tr_b16 v[42:43], v245 offset:36864\n\t"
    "v_add_f32 v249, v249, v220\n\t"
    "v_mfma_f32_16x16x32_bf16 v[228:231], v[76:79], v[112:115], v[228:231]\n\t"
    "ds_read_b128 v[44:47], v203 offset:20480\n\t"
    "v_exp_f32 v222, v222\n\t"
    "v_mfma_f32_16x16x32_bf16 v[236:239], v[76:79], v[116:119], v[236:239]\n\t"
    "v_add_f32 v249, v249, v221\n\t"
    "v_mfma_f32_16x16x32_bf16 v[176:179], v[88:91], v[120:123], v[176:179]\n\t"
    "ds_read_b64_tr_b16 v[56:57], v246 offset:32768\n\t"
    "v_cvt_pk_bf16_f32 v110, v220, v221\n\t"
    "v_exp_f32 v223, v223\n\t"
    "v_mfma_f32_16x16x32_bf16 v[184:187], v[88:91], v[124:127], v[184:187]\n\t"
    "ds_read_b64_tr_b16 v[58:59], v246 offset:36864\n\t"
    "v_add_f32 v249, v249, v222\n\t"
    "v_mfma_f32_16x16x32_bf16 v[180:183], v[92:95], v[120:123], v[180:183]\n\t"
    "ds_read_b128 v[60:63], v202 offset:17408\n\t"
    "v_add_f32 v249, v249, v223\n\t"
    "v_mfma_f32_16x16x32_bf16 v[188:191], v[92:95], v[124:127], v[188:191]\n\t"
    "s_add_u32 m0, s60, 0\n\t"
    "s_mov_b32 s63, 163840\n\t"
    "buffer_load_dwordx4 v242, s[44:47], s63 offen nt lds\n\t"
    "v_cvt_pk_bf16_f32 v111, v222, v223\n\t"
    "s_waitcnt lgkmcnt(9)\n\t"
    "v_mfma_f32_16x16x32_bf16 v[208:211], v[8:11], v[64:67], v[192:195]\n\t"
    "ds_read_b64_tr_b16 v[72:73], v247 offset:32768\n\t"
    "v_exp_f32 v224, v224\n\t"
    "v_exp_f32 v225, v225\n\t"
    "v_mfma_f32_16x16x32_bf16 v[216:219], v[8:11], v[68:71], v[196:199]\n\t"
    "ds_read_b64_tr_b16 v[74:75], v247 offset:36864\n\t"
    "v_add_f32 v248, v248, v224\n\t"
    "v_mfma_f32_16x16x32_bf16 v[212:215], v[12:15], v[64:67], v[192:195]\n\t"
    "ds_read_b128 v[76:79], v202 offset:21504\n\t"
    "v_exp_f32 v226, v226\n\t"
    "v_mfma_f32_16x16x32_bf16 v[220:223], v[12:15], v[68:71], v[196:199]\n\t"
    "v_add_f32 v248, v248, v225\n\t"
    "s_waitcnt lgkmcnt(9)\n\t"
    "v_mfma_f32_16x16x32_bf16 v[128:131], v[24:27], v[104:107], v[128:131]\n\t"
    "ds_read_b64_tr_b16 v[88:89], v244 offset:33792\n\t"
    "v_cvt_pk_bf16_f32 v120, v224, v225\n\t"
    "v_exp_f32 v227, v227\n\t"
    "v_mfma_f32_16x16x32_bf16 v[136:139], v[24:27], v[108:111], v[136:139]\n\t"
    "ds_read_b64_tr_b16 v[90:91], v244 offset:37888\n\t"
    "v_add_f32 v248, v248, v226\n\t"
    "v_mfma_f32_16x16x32_bf16 v[208:211], v[28:31], v[80:83], v[208:211]\n\t"
    "ds_read_b128 v[92:95], v203 offset:17408\n\t"
    "v_exp_f32 v228, v228\n\t"
    "v_mfma_f32_16x16x32_bf16 v[216:219], v[28:31], v[84:87], v[216:219]\n\t"
    "v_add_f32 v248, v248, v227\n\t"
    "s_waitcnt lgkmcnt(9)\n\t"
    "v_mfma_f32_16x16x32_bf16 v[132:135], v[40:43], v[104:107], v[132:135]\n\t"
    "ds_read_b64_tr_b16 v[8:9], v245 offset:33792\n\t"
    "v_cvt_pk_bf16_f32 v121, v226, v227\n\t"
    "v_exp_f32 v229, v229\n\t"
    "v_mfma_f32_16x16x32_bf16 v[140:143], v[40:43], v[108:111], v[140:143]\n\t"
    "ds_read_b64_tr_b16 v[10:11], v245 offset:37888\n\t"
    "v_add_f32 v248, v248, v228\n\t"
    "v_mfma_f32_16x16x32_bf16 v[212:215], v[44:47], v[80:83], v[212:215]\n\t"
    "ds_read_b128 v[12:15], v203 offset:21504\n\t"
    "v_exp_f32 v230, v230\n\t"
    "v_mfma_f32_16x16x32_bf16 v[220:223], v[44:47], v[84:87], v[220:223]\n\t"
    "s_add_u32 m0, s60, 8192\n\t"
    "s_mov_b32 s63, 172032\n\t"
    "buffer_load_dwordx4 v242, s[44:47], s63 offen nt lds\n\t"
    "v_add_f32 v248, v248, v229\n\t"
    "s_waitcnt lgkmcnt(9)\n\t"
    "v_mfma_f32_16x16x32_bf16 v[144:147], v[56:59], v[104:107], v[144:147]\n\t"
    "ds_read_b64_tr_b16 v[24:25], v246 offset:33792\n\t"
    "v_cvt_pk_bf16_f32 v122, v228, v229\n\t"
    "v_exp_f32 v231, v231\n\t"
    "v_mfma_f32_16x16x32_bf16 v[152:155], v[56:59], v[108:111], v[152:155]\n\t"
    "ds_read_b64_tr_b16 v[26:27], v246 offset:37888\n\t"
    "v_add_f32 v248, v248, v230\n\t"
    "v_mfma_f32_16x16x32_bf16 v[208:211], v[60:63], v[96:99], v[208:211]\n\t"
    "ds_read_b64_tr_b16 v[28:29], v247 offset:33792\n\t"
    "s_setprio 2\n\t"
    "v_exp_f32 v232, v232\n\t"
    "v_mfma_f32_16x16x32_bf16 v[216:219], v[60:63], v[100:103], v[216:219]\n\t"
    "ds_read_b64_tr_b16 v[30:31], v247 offset:37888\n\t"
    "v_add_f32 v248, v248, v231\n\t"
    "s_waitcnt lgkmcnt(10)\n\t"
    "v_mfma_f32_16x16x32_bf16 v[148:151], v[72:75], v[104:107], v[148:151]\n\t"
    "ds_read_b128 v[40:43], v202 offset:24576\n\t"
    "v_cvt_pk_bf16_f32 v123, v230, v231\n\t"
    "v_mfma_f32_16x16x32_bf16 v[156:159], v[72:75], v[108:111], v[156:159]\n\t"
    "v_exp_f32 v233, v233\n\t"
    "v_add_f32 v249, v249, v232\n\t"
    "v_mfma_f32_16x16x32_bf16 v[212:215], v[76:79], v[96:99], v[212:215]\n\t"
    "ds_read_b128 v[44:47], v202 offset:28672\n\t"
    "v_exp_f32 v234, v234\n\t"
    "v_mfma_f32_16x16x32_bf16 v[220:223], v[76:79], v[100:103], v[220:223]\n\t"
    "v_add_f32 v249, v249, v233\n\t"
    "s_waitcnt lgkmcnt(9)\n\t"
    "v_mfma_f32_16x16x32_bf16 v[160:163], v[88:91], v[104:107], v[160:163]\n\t"
    "ds_read_b64_tr_b16 v[56:57], v244 offset:40960\n\t"
    "v_cvt_pk_bf16_f32 v124, v232, v233\n\t"
    "v_exp_f32 v235, v235\n\t"
    "v_mfma_f32_16x16x32_bf16 v[168:171], v[88:91], v[108:111], v[168:171]\n\t"
    "ds_read_b64_tr_b16 v[58:59], v244 offset:45056\n\t"
    "v_add_f32 v249, v249, v234\n\t"
    "v_mfma_f32_16x16x32_bf16 v[208:211], v[92:95], v[112:115], v[208:211]\n\t"
    "ds_read_b128 v[60:63], v203 offset:24576\n\t"
    "v_exp_f32 v236, v236\n\t"
    "v_mfma_f32_16x16x32_bf16 v[216:219], v[92:95], v[116:119], v[216:219]\n\t"
    "s_add_u32 m0, s60, 81920\n\t"
    "s_mov_b32 s63, 114688\n\t"
    "buffer_load_dwordx4 v243, s[48:51], s63 offen nt lds\n\t"
    "v_add_f32 v249, v249, v235\n\t"
    "s_waitcnt lgkmcnt(9)\n\t"
    "v_mfma_f32_16x16x32_bf16 v[164:167], v[8:11], v[104:107], v[164:167]\n\t"
    "ds_read_b64_tr_b16 v[72:73], v245 offset:40960\n\t"
    "v_cvt_pk_bf16_f32 v125, v234, v235\n\t"
    "v_exp_f32 v237, v237\n\t"
    "v_mfma_f32_16x16x32_bf16 v[172:175], v[8:11], v[108:111], v[172:175]\n\t"
    "ds_read_b64_tr_b16 v[74:75], v245 offset:45056\n\t"
    "v_add_f32 v249, v249, v236\n\t"
    "v_mfma_f32_16x16x32_bf16 v[212:215], v[12:15], v[112:115], v[212:215]\n\t"
    "ds_read_b128 v[76:79], v203 offset:28672\n\t"
    "v_exp_f32 v238, v238\n\t"
    "v_mfma_f32_16x16x32_bf16 v[220:223], v[12:15], v[116:119], v[220:223]\n\t"
    "v_add_f32 v249, v249, v237\n\t"
    "s_waitcnt lgkmcnt(8)\n\t"
    "v_mfma_f32_16x16x32_bf16 v[176:179], v[24:27], v[104:107], v[176:179]\n\t"
    "ds_read_b64_tr_b16 v[88:89], v246 offset:40960\n\t"
    "v_cvt_pk_bf16_f32 v126, v236, v237\n\t"
    "v_exp_f32 v239, v239\n\t"
    "v_mfma_f32_16x16x32_bf16 v[184:187], v[24:27], v[108:111], v[184:187]\n\t"
    "ds_read_b64_tr_b16 v[90:91], v246 offset:45056\n\t"
    "v_add_f32 v249, v249, v238\n\t"
    "v_mfma_f32_16x16x32_bf16 v[180:183], v[28:31], v[104:107], v[180:183]\n\t"
    "ds_read_b128 v[92:95], v202 offset:25600\n\t"
    "v_add_f32 v249, v249, v239\n\t"
    "v_mfma_f32_16x16x32_bf16 v[188:191], v[28:31], v[108:111], v[188:191]\n\t"
    "v_cvt_pk_bf16_f32 v127, v238, v239\n\t"
    "s_waitcnt lgkmcnt(9)\n\t"
    "v_mfma_f32_16x16x32_bf16 v[224:227], v[40:43], v[64:67], v[192:195]\n\t"
    "ds_read_b64_tr_b16 v[8:9], v247 offset:40960\n\t"
    "s_setprio 1\n\t"
    "v_exp_f32 v208, v208\n\t"
    "v_exp_f32 v209, v209\n\t"
    "v_mfma_f32_16x16x32_bf16 v[232:235], v[40:43], v[68:71], v[196:199]\n\t"
    "ds_read_b64_tr_b16 v[10:11], v247 offset:45056\n\t"
    "v_add_f32 v248, v248, v208\n\t"
    "s_and_b32 s79, s66, 3\n\t"
    "v_mfma_f32_16x16x32_bf16 v[228:231], v[44:47], v[64:67], v[192:195]\n\t"
    "ds_read_b128 v[12:15], v202 offset:29696\n\t"
    "v_exp_f32 v210, v210\n\t"
    "v_mfma_f32_16x16x32_bf16 v[236:239], v[44:47], v[68:71], v[196:199]\n\t"
    "s_add_u32 m0, s60, 90112\n\t"
    "s_mov_b32 s63, 122880\n\t"
    "buffer_load_dwordx4 v243, s[48:51], s63 offen nt lds\n\t"
    "v_add_f32 v248, v248, v209\n\t"
    "s_waitcnt lgkmcnt(9)\n\t"
    "v_mfma_f32_16x16x32_bf16 v[128:131], v[56:59], v[120:123], v[128:131]\n\t"
    "ds_read_b64_tr_b16 v[24:25], v244 offset:41984\n\t"
    "v_cvt_pk_bf16_f32 v104, v208, v209\n\t"
    "v_exp_f32 v211, v211\n\t"
    "v_mfma_f32_16x16x32_bf16 v[136:139], v[56:59], v[124:127], v[136:139]\n\t"
    "ds_read_b64_tr_b16 v[26:27], v244 offset:46080\n\t"
    "v_add_f32 v248, v248, v210\n\t"
    "v_mfma_f32_16x16x32_bf16 v[224:227], v[60:63], v[80:83], v[224:227]\n\t"
    "ds_read_b128 v[28:31], v203 offset:25600\n\t"
    "v_exp_f32 v212, v212\n\t"
    "v_mfma_f32_16x16x32_bf16 v[232:235], v[60:63], v[84:87], v[232:235]\n\t"
    "v_add_f32 v248, v248, v211\n\t"
    "s_waitcnt lgkmcnt(9)\n\t"
    "v_mfma_f32_16x16x32_bf16 v[132:135], v[72:75], v[120:123], v[132:135]\n\t"
    "ds_read_b64_tr_b16 v[40:41], v245 offset:41984\n\t"
    "v_cvt_pk_bf16_f32 v105, v210, v211\n\t"
    "v_exp_f32 v213, v213\n\t"
    "v_mfma_f32_16x16x32_bf16 v[140:143], v[72:75], v[124:127], v[140:143]\n\t"
    "ds_read_b64_tr_b16 v[42:43], v245 offset:46080\n\t"
    "v_add_f32 v248, v248, v212\n\t"
    "v_mfma_f32_16x16x32_bf16 v[228:231], v[76:79], v[80:83], v[228:231]\n\t"
    "ds_read_b128 v[44:47], v203 offset:29696\n\t"
    "s_setprio 0\n\t"
    "v_mfma_f32_16x16x32_bf16 v[236:239], v[76:79], v[84:87], v[236:239]\n\t"
    "v_exp_f32 v214, v214\n\t"
    "v_add_f32 v248, v248, v213\n\t"
    "s_waitcnt lgkmcnt(9)\n\t"
    "v_mfma_f32_16x16x32_bf16 v[144:147], v[88:91], v[120:123], v[144:147]\n\t"
    "ds_read_b64_tr_b16 v[56:57], v246 offset:41984\n\t"
    "v_cvt_pk_bf16_f32 v106, v212, v213\n\t"
    "v_exp_f32 v215, v215\n\t"
    "v_mfma_f32_16x16x32_bf16 v[152:155], v[88:91], v[124:127], v[152:155]\n\t"
    "ds_read_b64_tr_b16 v[58:59], v246 offset:46080\n\t"
    "v_add_f32 v248, v248, v214\n\t"
    "v_mfma_f32_16x16x32_bf16 v[224:227], v[92:95], v[96:99], v[224:227]\n\t"
    "ds_read_b64_tr_b16 v[60:61], v247 offset:41984\n\t"
    "v_exp_f32 v216, v216\n\t"
    "v_mfma_f32_16x16x32_bf16 v[232:235], v[92:95], v[100:103], v[232:235]\n\t"
    "ds_read_b64_tr_b16 v[62:63], v247 offset:46080\n\t"
    "v_add_f32 v248, v248, v215\n\t"
    "s_waitcnt lgkmcnt(10)\n\t"
    "v_mfma_f32_16x16x32_bf16 v[148:151], v[8:11], v[120:123], v[148:151]\n\t"
    "v_cvt_pk_bf16_f32 v107, v214, v215\n\t"
    "v_exp_f32 v217, v217\n\t"
    "v_mfma_f32_16x16x32_bf16 v[156:159], v[8:11], v[124:127], v[156:159]\n\t"
    "v_add_f32 v249, v249, v216\n\t"
    "v_mfma_f32_16x16x32_bf16 v[228:231], v[12:15], v[96:99], v[228:231]\n\t"
    "v_exp_f32 v218, v218\n\t"
    "v_add_f32 v249, v249, v217\n\t"
    "v_mfma_f32_16x16x32_bf16 v[236:239], v[12:15], v[100:103], v[236:239]\n\t"
    "s_waitcnt vmcnt(8) lgkmcnt(0)\n\t"
    "s_barrier\n\t"
    "s_setprio 3\n\t"
    "ds_read_b128 v[72:75], v240 offset:16384\n\t"
    "ds_read_b128 v[76:79], v240 offset:20480\n\t"
    "v_mfma_f32_16x16x32_bf16 v[160:163], v[24:27], v[120:123], v[160:163]\n\t"
    "ds_read_b64_tr_b16 v[88:89], v244 offset:0\n\t"
    "v_cvt_pk_bf16_f32 v108, v216, v217\n\t"
    "v_exp_f32 v219, v219\n\t"
    "v_mfma_f32_16x16x32_bf16 v[168:171], v[24:27], v[124:127], v[168:171]\n\t"
    "ds_read_b64_tr_b16 v[90:91], v244 offset:4096\n\t"
    "v_add_f32 v249, v249, v218\n\t"
    "v_mfma_f32_16x16x32_bf16 v[224:227], v[28:31], v[112:115], v[224:227]\n\t"
    "ds_read_b128 v[92:95], v241 offset:16384\n\t"
    "v_exp_f32 v220, v220\n\t"
    "v_mfma_f32_16x16x32_bf16 v[232:235], v[28:31], v[116:119], v[232:235]\n\t"
    "v_add_f32 v249, v249, v219\n\t"
    "v_mfma_f32_16x16x32_bf16 v[164:167], v[40:43], v[120:123], v[164:167]\n\t"
    "ds_read_b64_tr_b16 v[8:9], v245 offset:0\n\t"
    "v_cvt_pk_bf16_f32 v109, v218, v219\n\t"
    "v_exp_f32 v221, v221\n\t"
    "v_mfma_f32_16x16x32_bf16 v[172:175], v[40:43], v[124:127], v[172:175]\n\t"
    "ds_read_b64_tr_b16 v[10:11], v245 offset:4096\n\t"
    "v_add_f32 v249, v249, v220\n\t"
    "v_mfma_f32_16x16x32_bf16 v[228:231], v[44:47], v[112:115], v[228:231]\n\t"
    "ds_read_b128 v[12:15], v241 offset:20480\n\t"
    "v_exp_f32 v222, v222\n\t"
    "v_mfma_f32_16x16x32_bf16 v[236:239], v[44:47], v[116:119], v[236:239]\n\t"
    "v_add_f32 v249, v249, v221\n\t"
    "v_mfma_f32_16x16x32_bf16 v[176:179], v[56:59], v[120:123], v[176:179]\n\t"
    "ds_read_b64_tr_b16 v[24:25], v246 offset:0\n\t"
    "v_cvt_pk_bf16_f32 v110, v220, v221\n\t"
    "v_exp_f32 v223, v223\n\t"
    "v_mfma_f32_16x16x32_bf16 v[184:187], v[56:59], v[124:127], v[184:187]\n\t"
    "ds_read_b64_tr_b16 v[26:27], v246 offset:4096\n\t"
    "v_add_f32 v249, v249, v222\n\t"
    "v_mfma_f32_16x16x32_bf16 v[180:183], v[60:63], v[120:123], v[180:183]\n\t"
    "ds_read_b128 v[28:31], v240 offset:17408\n\t"
    "v_add_f32 v249, v249, v223\n\t"
    "v_mfma_f32_16x16x32_bf16 v[188:191], v[60:63], v[124:127], v[188:191]\n\t"
    "s_add_u32 m0, s60, 16384\n\t"
    "s_mov_b32 s63, 180224\n\t"
    "buffer_load_dwordx4 v242, s[44:47], s63 offen nt lds\n\t"
    "v_cvt_pk_bf16_f32 v111, v222, v223\n\t"
    "s_waitcnt lgkmcnt(9)\n\t"
    "v_mfma_f32_16x16x32_bf16 v[208:211], v[72:75], v[64:67], v[192:195]\n\t"
    "ds_read_b64_tr_b16 v[40:41], v247 offset:0\n\t"
    "v_exp_f32 v224, v224\n\t"
    "v_exp_f32 v225, v225\n\t"
    "v_mfma_f32_16x16x32_bf16 v[216:219], v[72:75], v[68:71], v[196:199]\n\t"
    "ds_read_b64_tr_b16 v[42:43], v247 offset:4096\n\t"
    "v_add_f32 v248, v248, v224\n\t"
    "v_mfma_f32_16x16x32_bf16 v[212:215], v[76:79], v[64:67], v[192:195]\n\t"
    "ds_read_b128 v[44:47], v240 offset:21504\n\t"
    "v_exp_f32 v226, v226\n\t"
    "v_mfma_f32_16x16x32_bf16 v[220:223], v[76:79], v[68:71], v[196:199]\n\t"
    "v_add_f32 v248, v248, v225\n\t"
    "s_waitcnt lgkmcnt(9)\n\t"
    "v_mfma_f32_16x16x32_bf16 v[128:131], v[88:91], v[104:107], v[128:131]\n\t"
    "ds_read_b64_tr_b16 v[56:57], v244 offset:1024\n\t"
    "v_cvt_pk_bf16_f32 v120, v224, v225\n\t"
    "v_exp_f32 v227, v227\n\t"
    "v_mfma_f32_16x16x32_bf16 v[136:139], v[88:91], v[108:111], v[136:139]\n\t"
    "ds_read_b64_tr_b16 v[58:59], v244 offset:5120\n\t"
    "v_add_f32 v248, v248, v226\n\t"
    "v_mfma_f32_16x16x32_bf16 v[208:211], v[92:95], v[80:83], v[208:211]\n\t"
    "ds_read_b128 v[60:63], v241 offset:17408\n\t"
    "v_exp_f32 v228, v228\n\t"
    "v_mfma_f32_16x16x32_bf16 v[216:219], v[92:95], v[84:87], v[216:219]\n\t"
    "v_add_f32 v248, v248, v227\n\t"
    "s_waitcnt lgkmcnt(9)\n\t"
    "v_mfma_f32_16x16x32_bf16 v[132:135], v[8:11], v[104:107], v[132:135]\n\t"
    "ds_read_b64_tr_b16 v[72:73], v245 offset:1024\n\t"
    "v_cvt_pk_bf16_f32 v121, v226, v227\n\t"
    "v_exp_f32 v229, v229\n\t"
    "v_mfma_f32_16x16x32_bf16 v[140:143], v[8:11], v[108:111], v[140:143]\n\t"
    "ds_read_b64_tr_b16 v[74:75], v245 offset:5120\n\t"
    "v_add_f32 v248, v248, v228\n\t"
    "v_mfma_f32_16x16x32_bf16 v[212:215], v[12:15], v[80:83], v[212:215]\n\t"
    "ds_read_b128 v[76:79], v241 offset:21504\n\t"
    "v_exp_f32 v230, v230\n\t"
    "v_mfma_f32_16x16x32_bf16 v[220:223], v[12:15], v[84:87], v[220:223]\n\t"
    "s_add_u32 m0, s60, 24576\n\t"
    "s_mov_b32 s63, 188416\n\t"
    "buffer_load_dwordx4 v242, s[44:47], s63 offen nt lds\n\t"
    "v_add_f32 v248, v248, v229\n\t"
    "s_waitcnt lgkmcnt(9)\n\t"
    "v_mfma_f32_16x16x32_bf16 v[144:147], v[24:27], v[104:107], v[144:147]\n\t"
    "ds_read_b64_tr_b16 v[88:89], v246 offset:1024\n\t"
    "v_cvt_pk_bf16_f32 v122, v228, v229\n\t"
    "v_exp_f32 v231, v231\n\t"
    "v_mfma_f32_16x16x32_bf16 v[152:155], v[24:27], v[108:111], v[152:155]\n\t"
    "ds_read_b64_tr_b16 v[90:91], v246 offset:5120\n\t"
    "v_add_f32 v248, v248, v230\n\t"
    "v_mfma_f32_16x16x32_bf16 v[208:211], v[28:31], v[96:99], v[208:211]\n\t"
    "ds_read_b64_tr_b16 v[92:93], v247 offset:1024\n\t"
    "s_setprio 2\n\t"
    "v_exp_f32 v232, v232\n\t"
    "v_mfma_f32_16x16x32_bf16 v[216:219], v[28:31], v[100:103], v[216:219]\n\t"
    "ds_read_b64_tr_b16 v[94:95], v247 offset:5120\n\t"
    "v_add_f32 v248, v248, v231\n\t"
    "s_waitcnt lgkmcnt(10)\n\t"
    "v_mfma_f32_16x16x32_bf16 v[148:151], v[40:43], v[104:107], v[148:151]\n\t"
    "ds_read_b128 v[8:11], v240 offset:24576\n\t"
    "v_cvt_pk_bf16_f32 v123, v230, v231\n\t"
    "v_mfma_f32_16x16x32_bf16 v[156:159], v[40:43], v[108:111], v[156:159]\n\t"
    "v_exp_f32 v233, v233\n\t"
    "v_add_f32 v249, v249, v232\n\t"
    "v_mfma_f32_16x16x32_bf16 v[212:215], v[44:47], v[96:99], v[212:215]\n\t"
    "ds_read_b128 v[12:15], v240 offset:28672\n\t"
    "v_exp_f32 v234, v234\n\t"
    "v_mfma_f32_16x16x32_bf16 v[220:223], v[44:47], v[100:103], v[220:223]\n\t"
    "v_add_f32 v249, v249, v233\n\t"
    "s_waitcnt lgkmcnt(9)\n\t"
    "v_mfma_f32_16x16x32_bf16 v[160:163], v[56:59], v[104:107], v[160:163]\n\t"
    "ds_read_b64_tr_b16 v[24:25], v244 offset:8192\n\t"
    "v_cvt_pk_bf16_f32 v124, v232, v233\n\t"
    "v_exp_f32 v235, v235\n\t"
    "v_mfma_f32_16x16x32_bf16 v[168:171], v[56:59], v[108:111], v[168:171]\n\t"
    "ds_read_b64_tr_b16 v[26:27], v244 offset:12288\n\t"
    "v_add_f32 v249, v249, v234\n\t"
    "v_mfma_f32_16x16x32_bf16 v[208:211], v[60:63], v[112:115], v[208:211]\n\t"
    "ds_read_b128 v[28:31], v241 offset:24576\n\t"
    "v_exp_f32 v236, v236\n\t"
    "v_mfma_f32_16x16x32_bf16 v[216:219], v[60:63], v[116:119], v[216:219]\n\t"
    "s_add_u32 m0, s60, 65536\n\t"
    "s_mov_b32 s63, 131072\n\t"
    "buffer_load_dwordx4 v243, s[48:51], s63 offen nt lds\n\t"
    "v_add_f32 v249, v249, v235\n\t"
    "s_waitcnt lgkmcnt(9)\n\t"
    "v_mfma_f32_16x16x32_bf16 v[164:167], v[72:75], v[104:107], v[164:167]\n\t"
    "ds_read_b64_tr_b16 v[40:41], v245 offset:8192\n\t"
    "v_cvt_pk_bf16_f32 v125, v234, v235\n\t"
    "v_exp_f32 v237, v237\n\t"
    "v_mfma_f32_16x16x32_bf16 v[172:175], v[72:75], v[108:111], v[172:175]\n\t"
    "ds_read_b64_tr_b16 v[42:43], v245 offset:12288\n\t"
    "v_add_f32 v249, v249, v236\n\t"
    "v_mfma_f32_16x16x32_bf16 v[212:215], v[76:79], v[112:115], v[212:215]\n\t"
    "ds_read_b128 v[44:47], v241 offset:28672\n\t"
    "v_exp_f32 v238, v238\n\t"
    "v_mfma_f32_16x16x32_bf16 v[220:223], v[76:79], v[116:119], v[220:223]\n\t"
    "v_add_f32 v249, v249, v237\n\t"
    "s_waitcnt lgkmcnt(8)\n\t"
    "v_mfma_f32_16x16x32_bf16 v[176:179], v[88:91], v[104:107], v[176:179]\n\t"
    "ds_read_b64_tr_b16 v[56:57], v246 offset:8192\n\t"
    "v_cvt_pk_bf16_f32 v126, v236, v237\n\t"
    "v_exp_f32 v239, v239\n\t"
    "v_mfma_f32_16x16x32_bf16 v[184:187], v[88:91], v[108:111], v[184:187]\n\t"
    "ds_read_b64_tr_b16 v[58:59], v246 offset:12288\n\t"
    "v_add_f32 v249, v249, v238\n\t"
    "v_mfma_f32_16x16x32_bf16 v[180:183], v[92:95], v[104:107], v[180:183]\n\t"
    "ds_read_b128 v[60:63], v240 offset:25600\n\t"
    "v_add_f32 v249, v249, v239\n\t"
    "v_mfma_f32_16x16x32_bf16 v[188:191], v[92:95], v[108:111], v[188:191]\n\t"
    "v_cvt_pk_bf16_f32 v127, v238, v239\n\t"
    "s_waitcnt lgkmcnt(9)\n\t"
    "v_mfma_f32_16x16x32_bf16 v[224:227], v[8:11], v[64:67], v[192:195]\n\t"
    "ds_read_b64_tr_b16 v[72:73], v247 offset:8192\n\t"
    "s_setprio 1\n\t"
    "v_exp_f32 v208, v208\n\t"
    "v_mfma_f32_16x16x32_bf16 v[232:235], v[8:11], v[68:71], v[196:199]\n\t"
    "ds_read_b64_tr_b16 v[74:75], v247 offset:12288\n\t"
    "v_exp_f32 v209, v209\n\t"
    "v_add_f32 v248, v248, v208\n\t"
    "v_mfma_f32_16x16x32_bf16 v[228:231], v[12:15], v[64:67], v[192:195]\n\t"
    "ds_read_b128 v[76:79], v240 offset:29696\n\t"
    "v_exp_f32 v210, v210\n\t"
    "v_mfma_f32_16x16x32_bf16 v[236:239], v[12:15], v[68:71], v[196:199]\n\t"
    "s_add_u32 m0, s60, 73728\n\t"
    "s_mov_b32 s63, 139264\n\t"
    "buffer_load_dwordx4 v243, s[48:51], s63 offen nt lds\n\t"
    "v_add_f32 v248, v248, v209\n\t"
    "s_waitcnt lgkmcnt(9)\n\t"
    "v_mfma_f32_16x16x32_bf16 v[128:131], v[24:27], v[120:123], v[128:131]\n\t"
    "ds_read_b64_tr_b16 v[88:89], v244 offset:9216\n\t"
    "v_cvt_pk_bf16_f32 v104, v208, v209\n\t"
    "v_exp_f32 v211, v211\n\t"
    "v_mfma_f32_16x16x32_bf16 v[136:139], v[24:27], v[124:127], v[136:139]\n\t"
    "ds_read_b64_tr_b16 v[90:91], v244 offset:13312\n\t"
    "v_add_f32 v248, v248, v210\n\t"
    "v_mfma_f32_16x16x32_bf16 v[224:227], v[28:31], v[80:83], v[224:227]\n\t"
    "ds_read_b128 v[92:95], v241 offset:25600\n\t"
    "v_exp_f32 v212, v212\n\t"
    "v_mfma_f32_16x16x32_bf16 v[232:235], v[28:31], v[84:87], v[232:235]\n\t"
    "v_add_f32 v248, v248, v211\n\t"
    "s_waitcnt lgkmcnt(9)\n\t"
    "v_mfma_f32_16x16x32_bf16 v[132:135], v[40:43], v[120:123], v[132:135]\n\t"
    "ds_read_b64_tr_b16 v[8:9], v245 offset:9216\n\t"
    "v_cvt_pk_bf16_f32 v105, v210, v211\n\t"
    "v_exp_f32 v213, v213\n\t"
    "v_mfma_f32_16x16x32_bf16 v[140:143], v[40:43], v[124:127], v[140:143]\n\t"
    "ds_read_b64_tr_b16 v[10:11], v245 offset:13312\n\t"
    "v_add_f32 v248, v248, v212\n\t"
    "v_mfma_f32_16x16x32_bf16 v[228:231], v[44:47], v[80:83], v[228:231]\n\t"
    "ds_read_b128 v[12:15], v241 offset:29696\n\t"
    "s_setprio 0\n\t"
    "v_mfma_f32_16x16x32_bf16 v[236:239], v[44:47], v[84:87], v[236:239]\n\t"
    "v_exp_f32 v214, v214\n\t"
    "v_add_f32 v248, v248, v213\n\t"
    "s_waitcnt lgkmcnt(9)\n\t"
    "v_mfma_f32_16x16x32_bf16 v[144:147], v[56:59], v[120:123], v[144:147]\n\t"
    "ds_read_b64_tr_b16 v[24:25], v246 offset:9216\n\t"
    "v_cvt_pk_bf16_f32 v106, v212, v213\n\t"
    "v_exp_f32 v215, v215\n\t"
    "v_mfma_f32_16x16x32_bf16 v[152:155], v[56:59], v[124:127], v[152:155]\n\t"
    "ds_read_b64_tr_b16 v[26:27], v246 offset:13312\n\t"
    "v_add_f32 v248, v248, v214\n\t"
    "v_mfma_f32_16x16x32_bf16 v[224:227], v[60:63], v[96:99], v[224:227]\n\t"
    "ds_read_b64_tr_b16 v[28:29], v247 offset:9216\n\t"
    "v_exp_f32 v216, v216\n\t"
    "v_mfma_f32_16x16x32_bf16 v[232:235], v[60:63], v[100:103], v[232:235]\n\t"
    "ds_read_b64_tr_b16 v[30:31], v247 offset:13312\n\t"
    "v_add_f32 v248, v248, v215\n\t"
    "s_waitcnt lgkmcnt(10)\n\t"
    "v_mfma_f32_16x16x32_bf16 v[148:151], v[72:75], v[120:123], v[148:151]\n\t"
    "v_cvt_pk_bf16_f32 v107, v214, v215\n\t"
    "v_exp_f32 v217, v217\n\t"
    "v_mfma_f32_16x16x32_bf16 v[156:159], v[72:75], v[124:127], v[156:159]\n\t"
    "v_add_f32 v249, v249, v216\n\t"
    "v_mfma_f32_16x16x32_bf16 v[228:231], v[76:79], v[96:99], v[228:231]\n\t"
    "v_exp_f32 v218, v218\n\t"
    "v_add_f32 v249, v249, v217\n\t"
    "v_mfma_f32_16x16x32_bf16 v[236:239], v[76:79], v[100:103], v[236:239]\n\t"
    "s_waitcnt vmcnt(8) lgkmcnt(0)\n\t"
    "s_barrier\n\t"
    "s_setprio 3\n\t"
    "ds_read_b128 v[40:43], v240 offset:32768\n\t"
    "ds_read_b128 v[44:47], v240 offset:36864\n\t"
    "v_mfma_f32_16x16x32_bf16 v[160:163], v[88:91], v[120:123], v[160:163]\n\t"
    "ds_read_b64_tr_b16 v[56:57], v244 offset:16384\n\t"
    "v_cvt_pk_bf16_f32 v108, v216, v217\n\t"
    "v_exp_f32 v219, v219\n\t"
    "v_mfma_f32_16x16x32_bf16 v[168:171], v[88:91], v[124:127], v[168:171]\n\t"
    "ds_read_b64_tr_b16 v[58:59], v244 offset:20480\n\t"
    "v_add_f32 v249, v249, v218\n\t"
    "v_mfma_f32_16x16x32_bf16 v[224:227], v[92:95], v[112:115], v[224:227]\n\t"
    "ds_read_b128 v[60:63], v241 offset:32768\n\t"
    "v_exp_f32 v220, v220\n\t"
    "v_mfma_f32_16x16x32_bf16 v[232:235], v[92:95], v[116:119], v[232:235]\n\t"
    "v_add_f32 v249, v249, v219\n\t"
    "v_mfma_f32_16x16x32_bf16 v[164:167], v[8:11], v[120:123], v[164:167]\n\t"
    "ds_read_b64_tr_b16 v[72:73], v245 offset:16384\n\t"
    "v_cvt_pk_bf16_f32 v109, v218, v219\n\t"
    "v_exp_f32 v221, v221\n\t"
    "v_mfma_f32_16x16x32_bf16 v[172:175], v[8:11], v[124:127], v[172:175]\n\t"
    "ds_read_b64_tr_b16 v[74:75], v245 offset:20480\n\t"
    "v_add_f32 v249, v249, v220\n\t"
    "v_mfma_f32_16x16x32_bf16 v[228:231], v[12:15], v[112:115], v[228:231]\n\t"
    "ds_read_b128 v[76:79], v241 offset:36864\n\t"
    "v_exp_f32 v222, v222\n\t"
    "v_mfma_f32_16x16x32_bf16 v[236:239], v[12:15], v[116:119], v[236:239]\n\t"
    "v_add_f32 v249, v249, v221\n\t"
    "v_mfma_f32_16x16x32_bf16 v[176:179], v[24:27], v[120:123], v[176:179]\n\t"
    "ds_read_b64_tr_b16 v[88:89], v246 offset:16384\n\t"
    "v_cvt_pk_bf16_f32 v110, v220, v221\n\t"
    "v_exp_f32 v223, v223\n\t"
    "v_mfma_f32_16x16x32_bf16 v[184:187], v[24:27], v[124:127], v[184:187]\n\t"
    "ds_read_b64_tr_b16 v[90:91], v246 offset:20480\n\t"
    "v_add_f32 v249, v249, v222\n\t"
    "v_mfma_f32_16x16x32_bf16 v[180:183], v[28:31], v[120:123], v[180:183]\n\t"
    "ds_read_b128 v[92:95], v240 offset:33792\n\t"
    "v_add_f32 v249, v249, v223\n\t"
    "v_mfma_f32_16x16x32_bf16 v[188:191], v[28:31], v[124:127], v[188:191]\n\t"
    "s_add_u32 m0, s60, 114688\n\t"
    "s_mov_b32 s63, 196608\n\t"
    "buffer_load_dwordx4 v242, s[44:47], s63 offen nt lds\n\t"
    "v_cvt_pk_bf16_f32 v111, v222, v223\n\t"
    "s_waitcnt lgkmcnt(9)\n\t"
    "v_mfma_f32_16x16x32_bf16 v[208:211], v[40:43], v[64:67], v[192:195]\n\t"
    "ds_read_b64_tr_b16 v[8:9], v247 offset:16384\n\t"
    "v_exp_f32 v224, v224\n\t"
    "v_exp_f32 v225, v225\n\t"
    "v_mfma_f32_16x16x32_bf16 v[216:219], v[40:43], v[68:71], v[196:199]\n\t"
    "ds_read_b64_tr_b16 v[10:11], v247 offset:20480\n\t"
    "v_add_f32 v248, v248, v224\n\t"
    "v_mfma_f32_16x16x32_bf16 v[212:215], v[44:47], v[64:67], v[192:195]\n\t"
    "PAD5_%=: s_cbranch_execz PADRET_%=\n\t"
    "ds_read_b128 v[12:15], v240 offset:37888\n\t"
    "v_exp_f32 v226, v226\n\t"
    "v_mfma_f32_16x16x32_bf16 v[220:223], v[44:47], v[68:71], v[196:199]\n\t"
    "v_add_f32 v248, v248, v225\n\t"
    "s_waitcnt lgkmcnt(9)\n\t"
    "v_mfma_f32_16x16x32_bf16 v[128:131], v[56:59], v[104:107], v[128:131]\n\t"
    "ds_read_b64_tr_b16 v[24:25], v244 offset:17408\n\t"
    "v_cvt_pk_bf16_f32 v120, v224, v225\n\t"
    "v_exp_f32 v227, v227\n\t"
    "v_mfma_f32_16x16x32_bf16 v[136:139], v[56:59], v[108:111], v[136:139]\n\t"
    "ds_read_b64_tr_b16 v[26:27], v244 offset:21504\n\t"
    "v_add_f32 v248, v248, v226\n\t"
    "v_mfma_f32_16x16x32_bf16 v[208:211], v[60:63], v[80:83], v[208:211]\n\t"
    "ds_read_b128 v[28:31], v241 offset:33792\n\t"
    "v_exp_f32 v228, v228\n\t"
    "v_mfma_f32_16x16x32_bf16 v[216:219], v[60:63], v[84:87], v[216:219]\n\t"
    "v_add_f32 v248, v248, v227\n\t"
    "s_waitcnt lgkmcnt(9)\n\t"
    "v_mfma_f32_16x16x32_bf16 v[132:135], v[72:75], v[104:107], v[132:135]\n\t"
    "ds_read_b64_tr_b16 v[40:41], v245 offset:17408\n\t"
    "v_cvt_pk_bf16_f32 v121, v226, v227\n\t"
    "v_exp_f32 v229, v229\n\t"
    "v_mfma_f32_16x16x32_bf16 v[140:143], v[72:75], v[108:111], v[140:143]\n\t"
    "ds_read_b64_tr_b16 v[42:43], v245 offset:21504\n\t"
    "v_add_f32 v248, v248, v228\n\t"
    "v_mfma_f32_16x16x32_bf16 v[212:215], v[76:79], v[80:83], v[212:215]\n\t"
    "ds_read_b128 v[44:47], v241 offset:37888\n\t"
    "v_exp_f32 v230, v230\n\t"
    "v_mfma_f32_16x16x32_bf16 v[220:223], v[76:79], v[84:87], v[220:223]\n\t"
    "s_add_u32 m0, s60, 122880\n\t"
    "s_mov_b32 s63, 204800\n\t"
    "buffer_load_dwordx4 v242, s[44:47], s63 offen nt lds\n\t"
    "v_add_f32 v248, v248, v229\n\t"
    "s_waitcnt lgkmcnt(9)\n\t"
    "v_mfma_f32_16x16x32_bf16 v[144:147], v[88:91], v[104:107], v[144:147]\n\t"
    "ds_read_b64_tr_b16 v[56:57], v246 offset:17408\n\t"
    "v_cvt_pk_bf16_f32 v122, v228, v229\n\t"
    "v_exp_f32 v231, v231\n\t"
    "v_mfma_f32_16x16x32_bf16 v[152:155], v[88:91], v[108:111], v[152:155]\n\t"
    "ds_read_b64_tr_b16 v[58:59], v246 offset:21504\n\t"
    "v_add_f32 v248, v248, v230\n\t"
    "v_mfma_f32_16x16x32_bf16 v[208:211], v[92:95], v[96:99], v[208:211]\n\t"
    "ds_read_b64_tr_b16 v[60:61], v247 offset:17408\n\t"
    "s_setprio 2\n\t"
    "v_exp_f32 v232, v232\n\t"
    "v_mfma_f32_16x16x32_bf16 v[216:219], v[92:95], v[100:103], v[216:219]\n\t"
    "ds_read_b64_tr_b16 v[62:63], v247 offset:21504\n\t"
    "v_add_f32 v248, v248, v231\n\t"
    "s_waitcnt lgkmcnt(10)\n\t"
    "v_mfma_f32_16x16x32_bf16 v[148:151], v[8:11], v[104:107], v[148:151]\n\t"
    "ds_read_b128 v[72:75], v240 offset:40960\n\t"
    "v_cvt_pk_bf16_f32 v123, v230, v231\n\t"
    "v_mfma_f32_16x16x32_bf16 v[156:159], v[8:11], v[108:111], v[156:159]\n\t"
    "v_exp_f32 v233, v233\n\t"
    "v_add_f32 v249, v249, v232\n\t"
    "v_mfma_f32_16x16x32_bf16 v[212:215], v[12:15], v[96:99], v[212:215]\n\t"
    "ds_read_b128 v[76:79], v240 offset:45056\n\t"
    "v_exp_f32 v234, v234\n\t"
    "v_mfma_f32_16x16x32_bf16 v[220:223], v[12:15], v[100:103], v[220:223]\n\t"
    "v_add_f32 v249, v249, v233\n\t"
    "s_waitcnt lgkmcnt(9)\n\t"
    "v_mfma_f32_16x16x32_bf16 v[160:163], v[24:27], v[104:107], v[160:163]\n\t"
    "ds_read_b64_tr_b16 v[88:89], v244 offset:24576\n\t"
    "v_cvt_pk_bf16_f32 v124, v232, v233\n\t"
    "v_exp_f32 v235, v235\n\t"
    "v_mfma_f32_16x16x32_bf16 v[168:171], v[24:27], v[108:111], v[168:171]\n\t"
    "ds_read_b64_tr_b16 v[90:91], v244 offset:28672\n\t"
    "v_add_f32 v249, v249, v234\n\t"
    "v_mfma_f32_16x16x32_bf16 v[208:211], v[28:31], v[112:115], v[208:211]\n\t"
    "ds_read_b128 v[92:95], v241 offset:40960\n\t"
    "v_exp_f32 v236, v236\n\t"
    "v_mfma_f32_16x16x32_bf16 v[216:219], v[28:31], v[116:119], v[216:219]\n\t"
    "s_add_u32 m0, s60, 32768\n\t"
    "s_mov_b32 s63, 147456\n\t"
    "buffer_load_dwordx4 v243, s[48:51], s63 offen nt lds\n\t"
    "v_add_f32 v249, v249, v235\n\t"
    "s_waitcnt lgkmcnt(9)\n\t"
    "v_mfma_f32_16x16x32_bf16 v[164:167], v[40:43], v[104:107], v[164:167]\n\t"
    "ds_read_b64_tr_b16 v[8:9], v245 offset:24576\n\t"
    "v_cvt_pk_bf16_f32 v125, v234, v235\n\t"
    "v_exp_f32 v237, v237\n\t"
    "v_mfma_f32_16x16x32_bf16 v[172:175], v[40:43], v[108:111], v[172:175]\n\t"
    "ds_read_b64_tr_b16 v[10:11], v245 offset:28672\n\t"
    "v_add_f32 v249, v249, v236\n\t"
    "v_mfma_f32_16x16x32_bf16 v[212:215], v[44:47], v[112:115], v[212:215]\n\t"
    "ds_read_b128 v[12:15], v241 offset:45056\n\t"
    "v_exp_f32 v238, v238\n\t"
    "v_mfma_f32_16x16x32_bf16 v[220:223], v[44:47], v[116:119], v[220:223]\n\t"
    "v_add_f32 v249, v249, v237\n\t"
    "s_waitcnt lgkmcnt(8)\n\t"
    "v_mfma_f32_16x16x32_bf16 v[176:179], v[56:59], v[104:107], v[176:179]\n\t"
    "ds_read_b64_tr_b16 v[24:25], v246 offset:24576\n\t"
    "v_cvt_pk_bf16_f32 v126, v236, v237\n\t"
    "v_exp_f32 v239, v239\n\t"
    "v_mfma_f32_16x16x32_bf16 v[184:187], v[56:59], v[108:111], v[184:187]\n\t"
    "ds_read_b64_tr_b16 v[26:27], v246 offset:28672\n\t"
    "v_add_f32 v249, v249, v238\n\t"
    "v_mfma_f32_16x16x32_bf16 v[180:183], v[60:63], v[104:107], v[180:183]\n\t"
    "ds_read_b128 v[28:31], v240 offset:41984\n\t"
    "v_add_f32 v249, v249, v239\n\t"
    "v_mfma_f32_16x16x32_bf16 v[188:191], v[60:63], v[108:111], v[188:191]\n\t"
    "v_cvt_pk_bf16_f32 v127, v238, v239\n\t"
    "s_waitcnt lgkmcnt(9)\n\t"
    "v_mfma_f32_16x16x32_bf16 v[224:227], v[72:75], v[64:67], v[192:195]\n\t"
    "ds_read_b64_tr_b16 v[40:41], v247 offset:24576\n\t"
    "s_setprio 1\n\t"
    "v_exp_f32 v208, v208\n\t"
    "v_mfma_f32_16x16x32_bf16 v[232:235], v[72:75], v[68:71], v[196:199]\n\t"
    "ds_read_b64_tr_b16 v[42:43], v247 offset:28672\n\t"
    "v_exp_f32 v209, v209\n\t"
    "v_add_f32 v248, v248, v208\n\t"
    "v_mfma_f32_16x16x32_bf16 v[228:231], v[76:79], v[64:67], v[192:195]\n\t"
    "ds_read_b128 v[44:47], v240 offset:46080\n\t"
    "v_exp_f32 v210, v210\n\t"
    "v_mfma_f32_16x16x32_bf16 v[236:239], v[76:79], v[68:71], v[196:199]\n\t"
    "s_add_u32 m0, s60, 40960\n\t"
    "s_mov_b32 s63, 155648\n\t"
    "buffer_load_dwordx4 v243, s[48:51], s63 offen nt lds\n\t"
    "v_add_f32 v248, v248, v209\n\t"
    "s_waitcnt lgkmcnt(9)\n\t"
    "v_mfma_f32_16x16x32_bf16 v[128:131], v[88:91], v[120:123], v[128:131]\n\t"
    "ds_read_b64_tr_b16 v[56:57], v244 offset:25600\n\t"
    "v_cvt_pk_bf16_f32 v104, v208, v209\n\t"
    "v_exp_f32 v211, v211\n\t"
    "v_mfma_f32_16x16x32_bf16 v[136:139], v[88:91], v[124:127], v[136:139]\n\t"
    "ds_read_b64_tr_b16 v[58:59], v244 offset:29696\n\t"
    "v_add_f32 v248, v248, v210\n\t"
    "v_mfma_f32_16x16x32_bf16 v[224:227], v[92:95], v[80:83], v[224:227]\n\t"
    "ds_read_b128 v[60:63], v241 offset:41984\n\t"
    "v_exp_f32 v212, v212\n\t"
    "v_mfma_f32_16x16x32_bf16 v[232:235], v[92:95], v[84:87], v[232:235]\n\t"
    "v_add_f32 v248, v248, v211\n\t"
    "s_waitcnt lgkmcnt(9)\n\t"
    "v_mfma_f32_16x16x32_bf16 v[132:135], v[8:11], v[120:123], v[132:135]\n\t"
    "ds_read_b64_tr_b16 v[72:73], v245 offset:25600\n\t"
    "v_cvt_pk_bf16_f32 v105, v210, v211\n\t"
    "v_exp_f32 v213, v213\n\t"
    "v_mfma_f32_16x16x32_bf16 v[140:143], v[8:11], v[124:127], v[140:143]\n\t"
    "ds_read_b64_tr_b16 v[74:75], v245 offset:29696\n\t"
    "v_add_f32 v248, v248, v212\n\t"
    "v_mfma_f32_16x16x32_bf16 v[228:231], v[12:15], v[80:83], v[228:231]\n\t"
    "ds_read_b128 v[76:79], v241 offset:46080\n\t"
    "s_setprio 0\n\t"
    "v_mfma_f32_16x16x32_bf16 v[236:239], v[12:15], v[84:87], v[236:239]\n\t"
    "v_exp_f32 v214, v214\n\t"
    "v_add_f32 v248, v248, v213\n\t"
    "s_waitcnt lgkmcnt(9)\n\t"
    "v_mfma_f32_16x16x32_bf16 v[144:147], v[24:27], v[120:123], v[144:147]\n\t"
    "ds_read_b64_tr_b16 v[88:89], v246 offset:25600\n\t"
    "v_cvt_pk_bf16_f32 v106, v212, v213\n\t"
    "v_exp_f32 v215, v215\n\t"
    "v_mfma_f32_16x16x32_bf16 v[152:155], v[24:27], v[124:127], v[152:155]\n\t"
    "ds_read_b64_tr_b16 v[90:91], v246 offset:29696\n\t"
    "v_add_f32 v248, v248, v214\n\t"
    "v_mfma_f32_16x16x32_bf16 v[224:227], v[28:31], v[96:99], v[224:227]\n\t"
    "ds_read_b64_tr_b16 v[92:93], v247 offset:25600\n\t"
    "v_exp_f32 v216, v216\n\t"
    "v_mfma_f32_16x16x32_bf16 v[232:235], v[28:31], v[100:103], v[232:235]\n\t"
    "ds_read_b64_tr_b16 v[94:95], v247 offset:29696\n\t"
    "v_add_f32 v248, v248, v215\n\t"
    "s_waitcnt lgkmcnt(10)\n\t"
    "v_mfma_f32_16x16x32_bf16 v[148:151], v[40:43], v[120:123], v[148:151]\n\t"
    "v_cvt_pk_bf16_f32 v107, v214, v215\n\t"
    "v_exp_f32 v217, v217\n\t"
    "v_mfma_f32_16x16x32_bf16 v[156:159], v[40:43], v[124:127], v[156:159]\n\t"
    "v_add_f32 v249, v249, v216\n\t"
    "v_mfma_f32_16x16x32_bf16 v[228:231], v[44:47], v[96:99], v[228:231]\n\t"
    "v_exp_f32 v218, v218\n\t"
    "v_add_f32 v249, v249, v217\n\t"
    "v_mfma_f32_16x16x32_bf16 v[236:239], v[44:47], v[100:103], v[236:239]\n\t"
    "s_waitcnt vmcnt(8) lgkmcnt(0)\n\t"
    "s_barrier\n\t"
    "s_setprio 3\n\t"
    "ds_read_b128 v[8:11], v240 offset:49152\n\t"
    "ds_read_b128 v[12:15], v240 offset:53248\n\t"
    "v_mfma_f32_16x16x32_bf16 v[160:163], v[56:59], v[120:123], v[160:163]\n\t"
    "ds_read_b64_tr_b16 v[24:25], v244 offset:49152\n\t"
    "v_cvt_pk_bf16_f32 v108, v216, v217\n\t"
    "v_exp_f32 v219, v219\n\t"
    "v_mfma_f32_16x16x32_bf16 v[168:171], v[56:59], v[124:127], v[168:171]\n\t"
    "ds_read_b64_tr_b16 v[26:27], v244 offset:53248\n\t"
    "v_add_f32 v249, v249, v218\n\t"
    "v_mfma_f32_16x16x32_bf16 v[224:227], v[60:63], v[112:115], v[224:227]\n\t"
    "ds_read_b128 v[28:31], v241 offset:49152\n\t"
    "v_exp_f32 v220, v220\n\t"
    "v_mfma_f32_16x16x32_bf16 v[232:235], v[60:63], v[116:119], v[232:235]\n\t"
    "v_add_f32 v249, v249, v219\n\t"
    "v_mfma_f32_16x16x32_bf16 v[164:167], v[72:75], v[120:123], v[164:167]\n\t"
    "ds_read_b64_tr_b16 v[40:41], v245 offset:49152\n\t"
    "v_cvt_pk_bf16_f32 v109, v218, v219\n\t"
    "v_exp_f32 v221, v221\n\t"
    "v_mfma_f32_16x16x32_bf16 v[172:175], v[72:75], v[124:127], v[172:175]\n\t"
    "ds_read_b64_tr_b16 v[42:43], v245 offset:53248\n\t"
    "v_add_f32 v249, v249, v220\n\t"
    "v_mfma_f32_16x16x32_bf16 v[228:231], v[76:79], v[112:115], v[228:231]\n\t"
    "ds_read_b128 v[44:47], v241 offset:53248\n\t"
    "v_exp_f32 v222, v222\n\t"
    "v_mfma_f32_16x16x32_bf16 v[236:239], v[76:79], v[116:119], v[236:239]\n\t"
    "v_add_f32 v249, v249, v221\n\t"
    "v_mfma_f32_16x16x32_bf16 v[176:179], v[88:91], v[120:123], v[176:179]\n\t"
    "ds_read_b64_tr_b16 v[56:57], v246 offset:49152\n\t"
    "v_cvt_pk_bf16_f32 v110, v220, v221\n\t"
    "v_exp_f32 v223, v223\n\t"
    "v_mfma_f32_16x16x32_bf16 v[184:187], v[88:91], v[124:127], v[184:187]\n\t"
    "ds_read_b64_tr_b16 v[58:59], v246 offset:53248\n\t"
    "v_add_f32 v249, v249, v222\n\t"
    "v_mfma_f32_16x16x32_bf16 v[180:183], v[92:95], v[120:123], v[180:183]\n\t"
    "ds_read_b128 v[60:63], v240 offset:50176\n\t"
    "v_add_f32 v249, v249, v223\n\t"
    "v_mfma_f32_16x16x32_bf16 v[188:191], v[92:95], v[124:127], v[188:191]\n\t"
    "s_add_u32 m0, s60, 131072\n\t"
    "s_mov_b32 s63, 212992\n\t"
    "buffer_load_dwordx4 v242, s[44:47], s63 offen nt lds\n\t"
    "v_cvt_pk_bf16_f32 v111, v222, v223\n\t"
    "s_waitcnt lgkmcnt(9)\n\t"
    "v_mfma_f32_16x16x32_bf16 v[208:211], v[8:11], v[0:3], 0\n\t"
    "ds_read_b64_tr_b16 v[72:73], v247 offset:49152\n\t"
    "v_exp_f32 v224, v224\n\t"
    "v_exp_f32 v225, v225\n\t"
    "v_mfma_f32_16x16x32_bf16 v[216:219], v[8:11], v[4:7], 0\n\t"
    "ds_read_b64_tr_b16 v[74:75], v247 offset:53248\n\t"
    "v_add_f32 v248, v248, v224\n\t"
    "v_mfma_f32_16x16x32_bf16 v[212:215], v[12:15], v[0:3], 0\n\t"
    "ds_read_b128 v[76:79], v240 offset:54272\n\t"
    "v_exp_f32 v226, v226\n\t"
    "v_mfma_f32_16x16x32_bf16 v[220:223], v[12:15], v[4:7], 0\n\t"
    "v_add_f32 v248, v248, v225\n\t"
    "s_waitcnt lgkmcnt(9)\n\t"
    "v_mfma_f32_16x16x32_bf16 v[128:131], v[24:27], v[104:107], v[128:131]\n\t"
    "ds_read_b64_tr_b16 v[88:89], v244 offset:50176\n\t"
    "v_cvt_pk_bf16_f32 v120, v224, v225\n\t"
    "v_exp_f32 v227, v227\n\t"
    "v_mfma_f32_16x16x32_bf16 v[136:139], v[24:27], v[108:111], v[136:139]\n\t"
    "ds_read_b64_tr_b16 v[90:91], v244 offset:54272\n\t"
    "v_add_f32 v248, v248, v226\n\t"
    "v_mfma_f32_16x16x32_bf16 v[208:211], v[28:31], v[16:19], v[208:211]\n\t"
    "ds_read_b128 v[92:95], v241 offset:50176\n\t"
    "v_exp_f32 v228, v228\n\t"
    "v_mfma_f32_16x16x32_bf16 v[216:219], v[28:31], v[20:23], v[216:219]\n\t"
    "v_add_f32 v248, v248, v227\n\t"
    "s_waitcnt lgkmcnt(9)\n\t"
    "v_mfma_f32_16x16x32_bf16 v[132:135], v[40:43], v[104:107], v[132:135]\n\t"
    "ds_read_b64_tr_b16 v[8:9], v245 offset:50176\n\t"
    "v_cvt_pk_bf16_f32 v121, v226, v227\n\t"
    "v_exp_f32 v229, v229\n\t"
    "v_mfma_f32_16x16x32_bf16 v[140:143], v[40:43], v[108:111], v[140:143]\n\t"
    "ds_read_b64_tr_b16 v[10:11], v245 offset:54272\n\t"
    "v_add_f32 v248, v248, v228\n\t"
    "v_mfma_f32_16x16x32_bf16 v[212:215], v[44:47], v[16:19], v[212:215]\n\t"
    "ds_read_b128 v[12:15], v241 offset:54272\n\t"
    "v_exp_f32 v230, v230\n\t"
    "v_mfma_f32_16x16x32_bf16 v[220:223], v[44:47], v[20:23], v[220:223]\n\t"
    "s_add_u32 m0, s60, 139264\n\t"
    "s_mov_b32 s63, 221184\n\t"
    "buffer_load_dwordx4 v242, s[44:47], s63 offen nt lds\n\t"
    "v_add_f32 v248, v248, v229\n\t"
    "s_waitcnt lgkmcnt(9)\n\t"
    "v_mfma_f32_16x16x32_bf16 v[144:147], v[56:59], v[104:107], v[144:147]\n\t"
    "ds_read_b64_tr_b16 v[24:25], v246 offset:50176\n\t"
    "v_cvt_pk_bf16_f32 v122, v228, v229\n\t"
    "v_exp_f32 v231, v231\n\t"
    "v_mfma_f32_16x16x32_bf16 v[152:155], v[56:59], v[108:111], v[152:155]\n\t"
    "ds_read_b64_tr_b16 v[26:27], v246 offset:54272\n\t"
    "v_add_f32 v248, v248, v230\n\t"
    "v_mfma_f32_16x16x32_bf16 v[208:211], v[60:63], v[32:35], v[208:211]\n\t"
    "ds_read_b64_tr_b16 v[28:29], v247 offset:50176\n\t"
    "s_setprio 2\n\t"
    "v_exp_f32 v232, v232\n\t"
    "v_mfma_f32_16x16x32_bf16 v[216:219], v[60:63], v[36:39], v[216:219]\n\t"
    "ds_read_b64_tr_b16 v[30:31], v247 offset:54272\n\t"
    "v_add_f32 v248, v248, v231\n\t"
    "s_waitcnt lgkmcnt(10)\n\t"
    "v_mfma_f32_16x16x32_bf16 v[148:151], v[72:75], v[104:107], v[148:151]\n\t"
    "ds_read_b128 v[40:43], v240 offset:57344\n\t"
    "v_cvt_pk_bf16_f32 v123, v230, v231\n\t"
    "v_mfma_f32_16x16x32_bf16 v[156:159], v[72:75], v[108:111], v[156:159]\n\t"
    "v_exp_f32 v233, v233\n\t"
    "v_add_f32 v249, v249, v232\n\t"
    "v_mfma_f32_16x16x32_bf16 v[212:215], v[76:79], v[32:35], v[212:215]\n\t"
    "ds_read_b128 v[44:47], v240 offset:61440\n\t"
    "v_exp_f32 v234, v234\n\t"
    "v_mfma_f32_16x16x32_bf16 v[220:223], v[76:79], v[36:39], v[220:223]\n\t"
    "v_add_f32 v249, v249, v233\n\t"
    "s_waitcnt lgkmcnt(9)\n\t"
    "v_mfma_f32_16x16x32_bf16 v[160:163], v[88:91], v[104:107], v[160:163]\n\t"
    "ds_read_b64_tr_b16 v[56:57], v244 offset:57344\n\t"
    "v_cvt_pk_bf16_f32 v124, v232, v233\n\t"
    "v_exp_f32 v235, v235\n\t"
    "v_mfma_f32_16x16x32_bf16 v[168:171], v[88:91], v[108:111], v[168:171]\n\t"
    "ds_read_b64_tr_b16 v[58:59], v244 offset:61440\n\t"
    "v_add_f32 v249, v249, v234\n\t"
    "v_mfma_f32_16x16x32_bf16 v[208:211], v[92:95], v[48:51], v[208:211]\n\t"
    "ds_read_b128 v[60:63], v241 offset:57344\n\t"
    "v_exp_f32 v236, v236\n\t"
    "v_mfma_f32_16x16x32_bf16 v[216:219], v[92:95], v[52:55], v[216:219]\n\t"
    "s_add_u32 m0, s60, 49152\n\t"
    "s_mov_b32 s63, 163840\n\t"
    "buffer_load_dwordx4 v243, s[48:51], s63 offen nt lds\n\t"
    "v_add_f32 v249, v249, v235\n\t"
    "s_waitcnt lgkmcnt(9)\n\t"
    "v_mfma_f32_16x16x32_bf16 v[164:167], v[8:11], v[104:107], v[164:167]\n\t"
    "ds_read_b64_tr_b16 v[72:73], v245 offset:57344\n\t"
    "v_cvt_pk_bf16_f32 v125, v234, v235\n\t"
    "v_exp_f32 v237, v237\n\t"
    "v_mfma_f32_16x16x32_bf16 v[172:175], v[8:11], v[108:111], v[172:175]\n\t"
    "ds_read_b64_tr_b16 v[74:75], v245 offset:61440\n\t"
    "v_add_f32 v249, v249, v236\n\t"
    "v_mfma_f32_16x16x32_bf16 v[212:215], v[12:15], v[48:51], v[212:215]\n\t"
    "ds_read_b128 v[76:79], v241 offset:61440\n\t"
    "v_exp_f32 v238, v238\n\t"
    "v_mfma_f32_16x16x32_bf16 v[220:223], v[12:15], v[52:55], v[220:223]\n\t"
    "v_add_f32 v249, v249, v237\n\t"
    "s_waitcnt lgkmcnt(8)\n\t"
    "v_mfma_f32_16x16x32_bf16 v[176:179], v[24:27], v[104:107], v[176:179]\n\t"
    "ds_read_b64_tr_b16 v[88:89], v246 offset:57344\n\t"
    "v_cvt_pk_bf16_f32 v126, v236, v237\n\t"
    "v_exp_f32 v239, v239\n\t"
    "v_mfma_f32_16x16x32_bf16 v[184:187], v[24:27], v[108:111], v[184:187]\n\t"
    "ds_read_b64_tr_b16 v[90:91], v246 offset:61440\n\t"
    "v_add_f32 v249, v249, v238\n\t"
    "v_mfma_f32_16x16x32_bf16 v[180:183], v[28:31], v[104:107], v[180:183]\n\t"
    "ds_read_b128 v[92:95], v240 offset:58368\n\t"
    "v_add_f32 v249, v249, v239\n\t"
    "v_mfma_f32_16x16x32_bf16 v[188:191], v[28:31], v[108:111], v[188:191]\n\t"
    "v_cvt_pk_bf16_f32 v127, v238, v239\n\t"
    "s_waitcnt lgkmcnt(9)\n\t"
    "v_mfma_f32_16x16x32_bf16 v[224:227], v[40:43], v[0:3], 0\n\t"
    "ds_read_b64_tr_b16 v[8:9], v247 offset:57344\n\t"
    "s_setprio 1\n\t"
    "v_max3_f32 v253, v208, v209, v210\n\t"
    "v_max3_f32 v254, v216, v217, v218\n\t"
    "v_max3_f32 v253, v253, v211, v212\n\t"
    "v_mfma_f32_16x16x32_bf16 v[232:235], v[40:43], v[4:7], 0\n\t"
    "ds_read_b64_tr_b16 v[10:11], v247 offset:61440\n\t"
    "v_max3_f32 v254, v254, v219, v220\n\t"
    "v_max3_f32 v253, v253, v213, v214\n\t"
    "v_max3_f32 v254, v254, v221, v222\n\t"
    "v_mfma_f32_16x16x32_bf16 v[228:231], v[44:47], v[0:3], 0\n\t"
    "ds_read_b128 v[12:15], v240 offset:62464\n\t"
    "v_max_f32 v253, v253, v215\n\t"
    "v_max_f32 v254, v254, v223\n\t"
    "v_mfma_f32_16x16x32_bf16 v[236:239], v[44:47], v[4:7], 0\n\t"
    "s_add_u32 m0, s60, 57344\n\t"
    "s_mov_b32 s63, 172032\n\t"
    "buffer_load_dwordx4 v243, s[48:51], s63 offen nt lds\n\t"
    "v_mov_b32 v200, v253\n\t"
    "v_mov_b32 v201, v254\n\t"
    "s_nop 0\n\t"
    "v_permlane32_swap_b32 v253, v200\n\t"
    "s_waitcnt lgkmcnt(9)\n\t"
    "v_mfma_f32_16x16x32_bf16 v[128:131], v[56:59], v[120:123], v[128:131]\n\t"
    "ds_read_b64_tr_b16 v[24:25], v244 offset:58368\n\t"
    "v_permlane32_swap_b32 v254, v201\n\t"
    "v_max_f32 v253, v253, v200\n\t"
    "v_max_f32 v254, v254, v201\n\t"
    "v_mfma_f32_16x16x32_bf16 v[136:139], v[56:59], v[124:127], v[136:139]\n\t"
    "ds_read_b64_tr_b16 v[26:27], v244 offset:62464\n\t"
    "v_mov_b32 v200, v253\n\t"
    "v_mov_b32 v201, v254\n\t"
    "v_mfma_f32_16x16x32_bf16 v[224:227], v[60:63], v[16:19], v[224:227]\n\t"
    "ds_read_b128 v[28:31], v241 offset:58368\n\t"
    "v_permlane16_swap_b32 v253, v200\n\t"
    "v_permlane16_swap_b32 v254, v201\n\t"
    "v_mfma_f32_16x16x32_bf16 v[232:235], v[60:63], v[20:23], v[232:235]\n\t"
    "v_max_f32 v253, v253, v200\n\t"
    "v_max_f32 v254, v254, v201\n\t"
    "s_cmp_eq_u32 s67, 0\n\t"
    "s_cselect_b32 s65, 0, 0xff800000\n\t"
    "v_sub_f32 v192, s65, v253\n\t"
    "s_waitcnt lgkmcnt(9)\n\t"
    "v_mfma_f32_16x16x32_bf16 v[132:135], v[72:75], v[120:123], v[132:135]\n\t"
    "ds_read_b64_tr_b16 v[40:41], v245 offset:58368\n\t"
    "s_cmp_eq_u32 s62, 0\n\t"
    "s_cselect_b32 s65, 0, 0xff800000\n\t"
    "v_sub_f32 v196, s65, v254\n\t"
    "v_add_f32 v208, v192, v208\n\t"
    "v_add_f32 v209, v192, v209\n\t"
    "v_mfma_f32_16x16x32_bf16 v[140:143], v[72:75], v[124:127], v[140:143]\n\t"
    "ds_read_b64_tr_b16 v[42:43], v245 offset:62464\n\t"
    "v_exp_f32 v208, v208\n\t"
    "v_add_f32 v210, v192, v210\n\t"
    "v_exp_f32 v209, v209\n\t"
    "v_mfma_f32_16x16x32_bf16 v[228:231], v[76:79], v[16:19], v[228:231]\n\t"
    "ds_read_b128 v[44:47], v241 offset:62464\n\t"
    "s_setprio 0\n\t"
    "v_add_f32 v250, v250, v208\n\t"
    "v_add_f32 v211, v192, v211\n\t"
    "v_mfma_f32_16x16x32_bf16 v[236:239], v[76:79], v[20:23], v[236:239]\n\t"
    "v_exp_f32 v210, v210\n\t"
    "v_add_f32 v250, v250, v209\n\t"
    "v_cvt_pk_bf16_f32 v104, v208, v209\n\t"
    "s_waitcnt lgkmcnt(9)\n\t"
    "v_mfma_f32_16x16x32_bf16 v[144:147], v[88:91], v[120:123], v[144:147]\n\t"
    "ds_read_b64_tr_b16 v[56:57], v246 offset:58368\n\t"
    "v_add_f32 v212, v192, v212\n\t"
    "v_exp_f32 v211, v211\n\t"
    "v_add_f32 v250, v250, v210\n\t"
    "v_mfma_f32_16x16x32_bf16 v[152:155], v[88:91], v[124:127], v[152:155]\n\t"
    "ds_read_b64_tr_b16 v[58:59], v246 offset:62464\n\t"
    "v_add_f32 v213, v192, v213\n\t"
    "v_exp_f32 v212, v212\n\t"
    "v_mfma_f32_16x16x32_bf16 v[224:227], v[92:95], v[32:35], v[224:227]\n\t"
    "ds_read_b64_tr_b16 v[60:61], v247 offset:58368\n\t"
    "v_add_f32 v250, v250, v211\n\t"
    "v_cvt_pk_bf16_f32 v105, v210, v211\n\t"
    "v_add_f32 v214, v192, v214\n\t"
    "v_mfma_f32_16x16x32_bf16 v[232:235], v[92:95], v[36:39], v[232:235]\n\t"
    "ds_read_b64_tr_b16 v[62:63], v247 offset:62464\n\t"
    "v_exp_f32 v213, v213\n\t"
    "v_add_f32 v250, v250, v212\n\t"
    "s_waitcnt lgkmcnt(10)\n\t"
    "v_mfma_f32_16x16x32_bf16 v[148:151], v[8:11], v[120:123], v[148:151]\n\t"
    "v_add_f32 v215, v192, v215\n\t"
    "v_exp_f32 v214, v214\n\t"
    "v_add_f32 v250, v250, v213\n\t"
    "v_cvt_pk_bf16_f32 v106, v212, v213\n\t"
    "v_mfma_f32_16x16x32_bf16 v[156:159], v[8:11], v[124:127], v[156:159]\n\t"
    "v_add_f32 v216, v196, v216\n\t"
    "v_exp_f32 v215, v215\n\t"
    "v_mov_b32 v253, v248\n\t"
    "v_mov_b32 v254, v249\n\t"
    "v_mfma_f32_16x16x32_bf16 v[228:231], v[12:15], v[32:35], v[228:231]\n\t"
    "v_add_f32 v250, v250, v214\n\t"
    "v_add_f32 v217, v196, v217\n\t"
    "v_exp_f32 v216, v216\n\t"
    "v_add_f32 v250, v250, v215\n\t"
    "v_cvt_pk_bf16_f32 v107, v214, v215\n\t"
    "v_mfma_f32_16x16x32_bf16 v[236:239], v[12:15], v[36:39], v[236:239]\n\t"
    "s_waitcnt vmcnt(8) lgkmcnt(0)\n\t"
    "s_barrier\n\t"
    "s_setprio 3\n\t"
    "ds_read_b128 v[72:75], v240 offset:0\n\t"
    "ds_read_b128 v[76:79], v240 offset:4096\n\t"
    "v_permlane32_swap_b32 v248, v253\n\t"
    "v_permlane32_swap_b32 v249, v254\n\t"
    "v_mfma_f32_16x16x32_bf16 v[160:163], v[24:27], v[120:123], v[160:163]\n\t"
    "ds_read_b128 v[88:91], v241 offset:0\n\t"
    "v_add_f32 v218, v196, v218\n\t"
    "v_exp_f32 v217, v217\n\t"
    "v_add_f32 v251, v251, v216\n\t"
    "v_mfma_f32_16x16x32_bf16 v[168:171], v[24:27], v[124:127], v[168:171]\n\t"
    "v_add_f32 v219, v196, v219\n\t"
    "v_exp_f32 v218, v218\n\t"
    "v_add_f32 v248, v248, v253\n\t"
    "v_add_f32 v249, v249, v254\n\t"
    "v_mfma_f32_16x16x32_bf16 v[224:227], v[28:31], v[48:51], v[224:227]\n\t"
    "ds_read_b128 v[92:95], v241 offset:4096\n\t"
    "v_add_f32 v251, v251, v217\n\t"
    "v_cvt_pk_bf16_f32 v108, v216, v217\n\t"
    "v_add_f32 v220, v196, v220\n\t"
    "v_mfma_f32_16x16x32_bf16 v[232:235], v[28:31], v[52:55], v[232:235]\n\t"
    "v_exp_f32 v219, v219\n\t"
    "v_add_f32 v251, v251, v218\n\t"
    "v_mov_b32 v253, v248\n\t"
    "v_mov_b32 v254, v249\n\t"
    "v_mfma_f32_16x16x32_bf16 v[164:167], v[40:43], v[120:123], v[164:167]\n\t"
    "ds_read_b128 v[8:11], v240 offset:1024\n\t"
    "v_add_f32 v221, v196, v221\n\t"
    "v_exp_f32 v220, v220\n\t"
    "v_add_f32 v251, v251, v219\n\t"
    "v_cvt_pk_bf16_f32 v109, v218, v219\n\t"
    "v_mfma_f32_16x16x32_bf16 v[172:175], v[40:43], v[124:127], v[172:175]\n\t"
    "v_add_f32 v222, v196, v222\n\t"
    "v_exp_f32 v221, v221\n\t"
    "v_permlane16_swap_b32 v248, v253\n\t"
    "v_permlane16_swap_b32 v249, v254\n\t"
    "v_mfma_f32_16x16x32_bf16 v[228:231], v[44:47], v[48:51], v[228:231]\n\t"
    "ds_read_b128 v[12:15], v240 offset:5120\n\t"
    "v_add_f32 v251, v251, v220\n\t"
    "v_add_f32 v223, v196, v223\n\t"
    "v_exp_f32 v222, v222\n\t"
    "v_mfma_f32_16x16x32_bf16 v[236:239], v[44:47], v[52:55], v[236:239]\n\t"
    "v_add_f32 v251, v251, v221\n\t"
    "v_cvt_pk_bf16_f32 v110, v220, v221\n\t"
    "v_add_f32 v248, v248, v253\n\t"
    "v_add_f32 v249, v249, v254\n\t"
    "v_mfma_f32_16x16x32_bf16 v[176:179], v[56:59], v[120:123], v[176:179]\n\t"
    "ds_read_b128 v[24:27], v241 offset:1024\n\t"
    "v_exp_f32 v223, v223\n\t"
    "v_add_f32 v251, v251, v222\n\t"
    "v_add_f32 v251, v251, v223\n\t"
    "v_mfma_f32_16x16x32_bf16 v[184:187], v[56:59], v[124:127], v[184:187]\n\t"
    "v_cvt_pk_bf16_f32 v111, v222, v223\n\t"
    "v_mov_b32 v193, v192\n\t"
    "v_add_f32 v254, v248, v249\n\t"
    "v_rcp_f32 v248, v248\n\t"
    "v_mfma_f32_16x16x32_bf16 v[180:183], v[60:63], v[120:123], v[180:183]\n\t"
    "ds_read_b128 v[28:31], v241 offset:5120\n\t"
    "v_mov_b32 v194, v192\n\t"
    "v_mov_b32 v195, v192\n\t"
    "v_mov_b32 v197, v196\n\t"
    "v_mfma_f32_16x16x32_bf16 v[188:191], v[60:63], v[124:127], v[188:191]\n\t"
    "s_add_u32 m0, s60, 81920\n\t"
    "s_mov_b32 s63, 180224\n\t"
    "buffer_load_dwordx4 v243, s[48:51], s63 offen nt lds\n\t"
    "v_mov_b32 v198, v196\n\t"
    "v_mov_b32 v199, v196\n\t"
    "v_rcp_f32 v249, v249\n\t"
    "v_cmp_nge_f32 vcc, 0x57800000, v254\n\t"
    "s_nop 1\n\t"
    "s_cmp_lg_u64 vcc, 0\n\t"
    "s_cselect_b32 s65, 1, 0\n\t"
    "s_or_b32 s64, s64, s65\n\t"
    "s_waitcnt lgkmcnt(6)\n\t"
    "v_mfma_f32_16x16x32_bf16 v[208:211], v[72:75], v[0:3], v[192:195]\n\t"
    "ds_read_b64_tr_b16 v[40:41], v244 offset:32768\n\t"
    "v_mfma_f32_16x16x32_bf16 v[216:219], v[72:75], v[4:7], v[196:199]\n\t"
    "ds_read_b64_tr_b16 v[42:43], v244 offset:36864\n\t"
    "v_mfma_f32_16x16x32_bf16 v[212:215], v[76:79], v[0:3], v[192:195]\n\t"
    "ds_read_b64_tr_b16 v[44:45], v245 offset:32768\n\t"
    "v_mul_f32 v128, v248, v128\n\t"
    "v_mul_f32 v129, v248, v129\n\t"
    "v_cvt_pk_bf16_f32 v64, v128, v129\n\t"
    "v_mul_f32 v130, v248, v130\n\t"
    "v_mul_f32 v131, v248, v131\n\t"
    "v_cvt_pk_bf16_f32 v65, v130, v131\n\t"
    "v_mul_f32 v132, v248, v132\n\t"
    "v_mfma_f32_16x16x32_bf16 v[220:223], v[76:79], v[4:7], v[196:199]\n\t"
    "ds_read_b64_tr_b16 v[46:47], v245 offset:36864\n\t"
    "v_mul_f32 v133, v248, v133\n\t"
    "v_cvt_pk_bf16_f32 v66, v132, v133\n\t"
    "v_mul_f32 v134, v248, v134\n\t"
    "v_mul_f32 v135, v248, v135\n\t"
    "v_cvt_pk_bf16_f32 v67, v134, v135\n\t"
    "v_mul_f32 v136, v249, v136\n\t"
    "v_mul_f32 v137, v249, v137\n\t"
    "s_waitcnt lgkmcnt(8)\n\t"
    "v_mfma_f32_16x16x32_bf16 v[208:211], v[88:91], v[16:19], v[208:211]\n\t"
    "ds_read_b64_tr_b16 v[56:57], v246 offset:32768\n\t"
    "v_cvt_pk_bf16_f32 v68, v136, v137\n\t"
    "v_mul_f32 v138, v249, v138\n\t"
    "v_mul_f32 v139, v249, v139\n\t"
    "v_cvt_pk_bf16_f32 v69, v138, v139\n\t"
    "v_mul_f32 v140, v249, v140\n\t"
    "v_mul_f32 v141, v249, v141\n\t"
    "v_cvt_pk_bf16_f32 v70, v140, v141\n\t"
    "v_mfma_f32_16x16x32_bf16 v[216:219], v[88:91], v[20:23], v[216:219]\n\t"
    "ds_read_b64_tr_b16 v[58:59], v246 offset:36864\n\t"
    "v_mul_f32 v142, v249, v142\n\t"
    "v_mul_f32 v143, v249, v143\n\t"
    "v_cvt_pk_bf16_f32 v71, v142, v143\n\t"
    "v_mul_f32 v144, v248, v144\n\t"
    "v_mul_f32 v145, v248, v145\n\t"
    "v_cvt_pk_bf16_f32 v80, v144, v145\n\t"
    "v_mul_f32 v146, v248, v146\n\t"
    "v_mfma_f32_16x16x32_bf16 v[212:215], v[92:95], v[16:19], v[212:215]\n\t"
    "ds_read_b64_tr_b16 v[60:61], v247 offset:32768\n\t"
    "v_mul_f32 v147, v248, v147\n\t"
    "v_cvt_pk_bf16_f32 v81, v146, v147\n\t"
    "v_mul_f32 v148, v248, v148\n\t"
    "v_mul_f32 v149, v248, v149\n\t"
    "v_cvt_pk_bf16_f32 v82, v148, v149\n\t"
    "v_mul_f32 v150, v248, v150\n\t"
    "v_mul_f32 v151, v248, v151\n\t"
    "v_mfma_f32_16x16x32_bf16 v[220:223], v[92:95], v[20:23], v[220:223]\n\t"
    "ds_read_b64_tr_b16 v[62:63], v247 offset:36864\n\t"
    "v_cvt_pk_bf16_f32 v83, v150, v151\n\t"
    "v_mul_f32 v152, v249, v152\n\t"
    "v_mul_f32 v153, v249, v153\n\t"
    "v_cvt_pk_bf16_f32 v84, v152, v153\n\t"
    "v_mul_f32 v154, v249, v154\n\t"
    "v_mul_f32 v155, v249, v155\n\t"
    "v_cvt_pk_bf16_f32 v85, v154, v155\n\t"
    "s_waitcnt lgkmcnt(10)\n\t"
    "v_mfma_f32_16x16x32_bf16 v[208:211], v[8:11], v[32:35], v[208:211]\n\t"
    "ds_read_b64_tr_b16 v[72:73], v244 offset:33792\n\t"
    "v_mul_f32 v156, v249, v156\n\t"
    "v_mul_f32 v157, v249, v157\n\t"
    "v_cvt_pk_bf16_f32 v86, v156, v157\n\t"
    "v_mul_f32 v158, v249, v158\n\t"
    "v_mul_f32 v159, v249, v159\n\t"
    "v_cvt_pk_bf16_f32 v87, v158, v159\n\t"
    "v_mul_f32 v160, v248, v160\n\t"
    "v_mfma_f32_16x16x32_bf16 v[216:219], v[8:11], v[36:39], v[216:219]\n\t"
    "ds_read_b64_tr_b16 v[74:75], v244 offset:37888\n\t"
    "v_mul_f32 v161, v248, v161\n\t"
    "v_cvt_pk_bf16_f32 v96, v160, v161\n\t"
    "v_mul_f32 v162, v248, v162\n\t"
    "v_mul_f32 v163, v248, v163\n\t"
    "v_cvt_pk_bf16_f32 v97, v162, v163\n\t"
    "v_mul_f32 v164, v248, v164\n\t"
    "v_mfma_f32_16x16x32_bf16 v[212:215], v[12:15], v[32:35], v[212:215]\n\t"
    "ds_read_b64_tr_b16 v[76:77], v245 offset:33792\n\t"
    "v_mul_f32 v165, v248, v165\n\t"
    "v_cvt_pk_bf16_f32 v98, v164, v165\n\t"
    "v_mul_f32 v166, v248, v166\n\t"
    "v_mul_f32 v167, v248, v167\n\t"
    "v_cvt_pk_bf16_f32 v99, v166, v167\n\t"
    "v_mul_f32 v168, v249, v168\n\t"
    "v_mul_f32 v169, v249, v169\n\t"
    "v_mfma_f32_16x16x32_bf16 v[220:223], v[12:15], v[36:39], v[220:223]\n\t"
    "ds_read_b64_tr_b16 v[78:79], v245 offset:37888\n\t"
    "s_add_u32 m0, s60, 90112\n\t"
    "s_mov_b32 s63, 188416\n\t"
    "buffer_load_dwordx4 v243, s[48:51], s63 offen nt lds\n\t"
    "v_cvt_pk_bf16_f32 v100, v168, v169\n\t"
    "v_mul_f32 v170, v249, v170\n\t"
    "v_mul_f32 v171, v249, v171\n\t"
    "v_cvt_pk_bf16_f32 v101, v170, v171\n\t"
    "v_mul_f32 v172, v249, v172\n\t"
    "v_mul_f32 v173, v249, v173\n\t"
    "v_cvt_pk_bf16_f32 v102, v172, v173\n\t"
    "s_waitcnt lgkmcnt(12)\n\t"
    "v_mfma_f32_16x16x32_bf16 v[208:211], v[24:27], v[48:51], v[208:211]\n\t"
    "ds_read_b64_tr_b16 v[88:89], v246 offset:33792\n\t"
    "v_mul_f32 v174, v249, v174\n\t"
    "v_mul_f32 v175, v249, v175\n\t"
    "v_cvt_pk_bf16_f32 v103, v174, v175\n\t"
    "v_mul_f32 v176, v248, v176\n\t"
    "v_mul_f32 v177, v248, v177\n\t"
    "v_cvt_pk_bf16_f32 v112, v176, v177\n\t"
    "v_mul_f32 v178, v248, v178\n\t"
    "v_mfma_f32_16x16x32_bf16 v[216:219], v[24:27], v[52:55], v[216:219]\n\t"
    "ds_read_b64_tr_b16 v[90:91], v246 offset:37888\n\t"
    "v_mul_f32 v179, v248, v179\n\t"
    "v_cvt_pk_bf16_f32 v113, v178, v179\n\t"
    "v_mul_f32 v180, v248, v180\n\t"
    "v_mul_f32 v181, v248, v181\n\t"
    "v_cvt_pk_bf16_f32 v114, v180, v181\n\t"
    "v_mul_f32 v182, v248, v182\n\t"
    "v_mul_f32 v183, v248, v183\n\t"
    "v_mfma_f32_16x16x32_bf16 v[212:215], v[28:31], v[48:51], v[212:215]\n\t"
    "ds_read_b64_tr_b16 v[92:93], v247 offset:33792\n\t"
    "s_setprio 2\n\t"
    "v_cvt_pk_bf16_f32 v115, v182, v183\n\t"
    "v_mul_f32 v184, v249, v184\n\t"
    "v_mul_f32 v185, v249, v185\n\t"
    "v_cvt_pk_bf16_f32 v116, v184, v185\n\t"
    "v_mul_f32 v186, v249, v186\n\t"
    "v_mul_f32 v187, v249, v187\n\t"
    "v_mfma_f32_16x16x32_bf16 v[220:223], v[28:31], v[52:55], v[220:223]\n\t"
    "ds_read_b64_tr_b16 v[94:95], v247 offset:37888\n\t"
    "v_cvt_pk_bf16_f32 v117, v186, v187\n\t"
    "v_mul_f32 v188, v249, v188\n\t"
    "v_mul_f32 v189, v249, v189\n\t"
    "v_cvt_pk_bf16_f32 v118, v188, v189\n\t"
    "v_mul_f32 v190, v249, v190\n\t"
    "v_mul_f32 v191, v249, v191\n\t"
    "v_cvt_pk_bf16_f32 v119, v190, v191\n\t"
    "s_waitcnt lgkmcnt(12)\n\t"
    "v_mfma_f32_16x16x32_bf16 v[128:131], v[40:43], v[104:107], 0\n\t"
    "ds_read_b128 v[8:11], v240 offset:8192\n\t"
    "v_add_f32 v224, v192, v224\n\t"
    "v_add_f32 v225, v192, v225\n\t"
    "v_exp_f32 v224, v224\n\t"
    "v_mfma_f32_16x16x32_bf16 v[136:139], v[40:43], v[108:111], 0\n\t"
    "v_add_f32 v226, v192, v226\n\t"
    "v_exp_f32 v225, v225\n\t"
    "v_add_f32 v250, v250, v224\n\t"
    "v_add_f32 v227, v192, v227\n\t"
    "v_mfma_f32_16x16x32_bf16 v[132:135], v[44:47], v[104:107], 0\n\t"
    "ds_read_b128 v[12:15], v240 offset:12288\n\t"
    "v_exp_f32 v226, v226\n\t"
    "v_add_f32 v250, v250, v225\n\t"
    "v_cvt_pk_bf16_f32 v120, v224, v225\n\t"
    "v_mfma_f32_16x16x32_bf16 v[140:143], v[44:47], v[108:111], 0\n\t"
    "v_add_f32 v228, v192, v228\n\t"
    "v_exp_f32 v227, v227\n\t"
    "v_add_f32 v250, v250, v226\n\t"
    "v_add_f32 v229, v192, v229\n\t"
    "s_waitcnt lgkmcnt(10)\n\t"
    "v_mfma_f32_16x16x32_bf16 v[144:147], v[56:59], v[104:107], 0\n\t"
    "ds_read_b64_tr_b16 v[24:25], v244 offset:40960\n\t"
    "v_exp_f32 v228, v228\n\t"
    "v_add_f32 v250, v250, v227\n\t"
    "PAD6_%=: s_cbranch_execz PADRET_%=\n\t"
    "v_cvt_pk_bf16_f32 v121, v226, v227\n\t"
    "v_add_f32 v230, v192, v230\n\t"
    "v_mfma_f32_16x16x32_bf16 v[152:155], v[56:59], v[108:111], 0\n\t"
    "ds_read_b64_tr_b16 v[26:27], v244 offset:45056\n\t"
    "v_exp_f32 v229, v229\n\t"
    "v_add_f32 v250, v250, v228\n\t"
    "v_add_f32 v231, v192, v231\n\t"
    "v_mfma_f32_16x16x32_bf16 v[148:151], v[60:63], v[104:107], 0\n\t"
    "ds_read_b128 v[28:31], v241 offset:8192\n\t"
    "v_exp_f32 v230, v230\n\t"
    "v_add_f32 v250, v250, v229\n\t"
    "v_cvt_pk_bf16_f32 v122, v228, v229\n\t"
    "v_mfma_f32_16x16x32_bf16 v[156:159], v[60:63], v[108:111], 0\n\t"
    "v_add_f32 v232, v196, v232\n\t"
    "v_exp_f32 v231, v231\n\t"
    "v_add_f32 v250, v250, v230\n\t"
    "v_add_f32 v233, v196, v233\n\t"
    "s_waitcnt lgkmcnt(9)\n\t"
    "v_mfma_f32_16x16x32_bf16 v[160:163], v[72:75], v[104:107], 0\n\t"
    "ds_read_b64_tr_b16 v[40:41], v245 offset:40960\n\t"
    "v_exp_f32 v232, v232\n\t"
    "v_add_f32 v250, v250, v231\n\t"
    "v_cvt_pk_bf16_f32 v123, v230, v231\n\t"
    "v_add_f32 v234, v196, v234\n\t"
    "v_mfma_f32_16x16x32_bf16 v[168:171], v[72:75], v[108:111], 0\n\t"
    "ds_read_b64_tr_b16 v[42:43], v245 offset:45056\n\t"
    "v_exp_f32 v233, v233\n\t"
    "v_add_f32 v251, v251, v232\n\t"
    "v_add_f32 v235, v196, v235\n\t"
    "v_mfma_f32_16x16x32_bf16 v[164:167], v[76:79], v[104:107], 0\n\t"
    "ds_read_b128 v[44:47], v241 offset:12288\n\t"
    "v_exp_f32 v234, v234\n\t"
    "v_add_f32 v251, v251, v233\n\t"
    "v_cvt_pk_bf16_f32 v124, v232, v233\n\t"
    "v_mfma_f32_16x16x32_bf16 v[172:175], v[76:79], v[108:111], 0\n\t"
    "v_add_f32 v236, v196, v236\n\t"
    "v_exp_f32 v235, v235\n\t"
    "v_add_f32 v251, v251, v234\n\t"
    "v_add_f32 v237, v196, v237\n\t"
    "s_waitcnt lgkmcnt(8)\n\t"
    "v_mfma_f32_16x16x32_bf16 v[176:179], v[88:91], v[104:107], 0\n\t"
    "ds_read_b64_tr_b16 v[56:57], v246 offset:40960\n\t"
    "v_exp_f32 v236, v236\n\t"
    "v_add_f32 v251, v251, v235\n\t"
    "v_cvt_pk_bf16_f32 v125, v234, v235\n\t"
    "v_add_f32 v238, v196, v238\n\t"
    "v_mfma_f32_16x16x32_bf16 v[184:187], v[88:91], v[108:111], 0\n\t"
    "ds_read_b64_tr_b16 v[58:59], v246 offset:45056\n\t"
    "v_exp_f32 v237, v237\n\t"
    "v_add_f32 v251, v251, v236\n\t"
    "v_add_f32 v239, v196, v239\n\t"
    "v_mfma_f32_16x16x32_bf16 v[180:183], v[92:95], v[104:107], 0\n\t"
    "ds_read_b128 v[60:63], v240 offset:9216\n\t"
    "v_exp_f32 v238, v238\n\t"
    "v_add_f32 v251, v251, v237\n\t"
    "v_cvt_pk_bf16_f32 v126, v236, v237\n\t"
    "v_mfma_f32_16x16x32_bf16 v[188:191], v[92:95], v[108:111], 0\n\t"
    "v_exp_f32 v239, v239\n\t"
    "v_add_f32 v251, v251, v238\n\t"
    "v_add_f32 v251, v251, v239\n\t"
    "v_cvt_pk_bf16_f32 v127, v238, v239\n\t"
    "s_waitcnt lgkmcnt(9)\n\t"
    "v_mfma_f32_16x16x32_bf16 v[224:227], v[8:11], v[0:3], v[192:195]\n\t"
    "ds_read_b64_tr_b16 v[72:73], v247 offset:40960\n\t"
    "s_setprio 1\n\t"
    "s_add_u32 s63, s67, 0\n\t"
    "buffer_store_dwordx4 v[64:67], v252, s[52:55], s63 offen sc1\n\t"
    "v_exp_f32 v208, v208\n\t"
    "v_mfma_f32_16x16x32_bf16 v[232:235], v[8:11], v[4:7], v[196:199]\n\t"
    "ds_read_b64_tr_b16 v[74:75], v247 offset:45056\n\t"
    "v_exp_f32 v209, v209\n\t"
    "v_add_f32 v250, v250, v208\n\t"
    "v_mfma_f32_16x16x32_bf16 v[228:231], v[12:15], v[0:3], v[192:195]\n\t"
    "ds_read_b128 v[76:79], v240 offset:13312\n\t"
    "v_exp_f32 v210, v210\n\t"
    "v_mfma_f32_16x16x32_bf16 v[236:239], v[12:15], v[4:7], v[196:199]\n\t"
    "v_add_f32 v250, v250, v209\n\t"
    "s_waitcnt lgkmcnt(9)\n\t"
    "v_mfma_f32_16x16x32_bf16 v[128:131], v[24:27], v[120:123], v[128:131]\n\t"
    "ds_read_b64_tr_b16 v[88:89], v244 offset:41984\n\t"
    "v_cvt_pk_bf16_f32 v104, v208, v209\n\t"
    "v_exp_f32 v211, v211\n\t"
    "v_mfma_f32_16x16x32_bf16 v[136:139], v[24:27], v[124:127], v[136:139]\n\t"
    "ds_read_b64_tr_b16 v[90:91], v244 offset:46080\n\t"
    "v_add_f32 v250, v250, v210\n\t"
    "v_mfma_f32_16x16x32_bf16 v[224:227], v[28:31], v[16:19], v[224:227]\n\t"
    "ds_read_b128 v[92:95], v241 offset:9216\n\t"
    "v_exp_f32 v212, v212\n\t"
    "v_mfma_f32_16x16x32_bf16 v[232:235], v[28:31], v[20:23], v[232:235]\n\t"
    "v_add_f32 v250, v250, v211\n\t"
    "s_waitcnt lgkmcnt(9)\n\t"
    "v_mfma_f32_16x16x32_bf16 v[132:135], v[40:43], v[120:123], v[132:135]\n\t"
    "ds_read_b64_tr_b16 v[8:9], v245 offset:41984\n\t"
    "v_cvt_pk_bf16_f32 v105, v210, v211\n\t"
    "v_exp_f32 v213, v213\n\t"
    "v_mfma_f32_16x16x32_bf16 v[140:143], v[40:43], v[124:127], v[140:143]\n\t"
    "ds_read_b64_tr_b16 v[10:11], v245 offset:46080\n\t"
    "v_add_f32 v250, v250, v212\n\t"
    "v_mfma_f32_16x16x32_bf16 v[228:231], v[44:47], v[16:19], v[228:231]\n\t"
    "ds_read_b128 v[12:15], v241 offset:13312\n\t"
    "s_setprio 0\n\t"
    "v_mfma_f32_16x16x32_bf16 v[236:239], v[44:47], v[20:23], v[236:239]\n\t"
    "v_exp_f32 v214, v214\n\t"
    "v_add_f32 v250, v250, v213\n\t"
    "s_waitcnt lgkmcnt(9)\n\t"
    "v_mfma_f32_16x16x32_bf16 v[144:147], v[56:59], v[120:123], v[144:147]\n\t"
    "ds_read_b64_tr_b16 v[24:25], v246 offset:41984\n\t"
    "v_cvt_pk_bf16_f32 v106, v212, v213\n\t"
    "v_exp_f32 v215, v215\n\t"
    "v_mfma_f32_16x16x32_bf16 v[152:155], v[56:59], v[124:127], v[152:155]\n\t"
    "ds_read_b64_tr_b16 v[26:27], v246 offset:46080\n\t"
    "v_add_f32 v250, v250, v214\n\t"
    "v_mfma_f32_16x16x32_bf16 v[224:227], v[60:63], v[32:35], v[224:227]\n\t"
    "ds_read_b64_tr_b16 v[28:29], v247 offset:41984\n\t"
    "v_exp_f32 v216, v216\n\t"
    "v_mfma_f32_16x16x32_bf16 v[232:235], v[60:63], v[36:39], v[232:235]\n\t"
    "ds_read_b64_tr_b16 v[30:31], v247 offset:46080\n\t"
    "v_add_f32 v250, v250, v215\n\t"
    "s_waitcnt lgkmcnt(10)\n\t"
    "v_mfma_f32_16x16x32_bf16 v[148:151], v[72:75], v[120:123], v[148:151]\n\t"
    "v_cvt_pk_bf16_f32 v107, v214, v215\n\t"
    "v_exp_f32 v217, v217\n\t"
    "v_mfma_f32_16x16x32_bf16 v[156:159], v[72:75], v[124:127], v[156:159]\n\t"
    "v_add_f32 v251, v251, v216\n\t"
    "v_mfma_f32_16x16x32_bf16 v[228:231], v[76:79], v[32:35], v[228:231]\n\t"
    "v_exp_f32 v218, v218\n\t"
    "v_add_f32 v251, v251, v217\n\t"
    "v_mfma_f32_16x16x32_bf16 v[236:239], v[76:79], v[36:39], v[236:239]\n\t"
    "s_waitcnt vmcnt(7) lgkmcnt(0)\n\t"
    "s_barrier\n\t"
    "s_setprio 3\n\t"
    "ds_read_b128 v[40:43], v202 offset:0\n\t"
    "ds_read_b128 v[44:47], v202 offset:4096\n\t"
    "v_mfma_f32_16x16x32_bf16 v[160:163], v[88:91], v[120:123], v[160:163]\n\t"
    "ds_read_b64_tr_b16 v[56:57], v244 offset:0\n\t"
    "s_add_u32 s63, s62, 0\n\t"
    "buffer_store_dwordx4 v[68:71], v252, s[52:55], s63 offen offset:1024 sc1\n\t"
    "v_cvt_pk_bf16_f32 v108, v216, v217\n\t"
    "v_mfma_f32_16x16x32_bf16 v[168:171], v[88:91], v[124:127], v[168:171]\n\t"
    "ds_read_b64_tr_b16 v[58:59], v244 offset:4096\n\t"
    "v_exp_f32 v219, v219\n\t"
    "v_add_f32 v251, v251, v218\n\t"
    "v_mfma_f32_16x16x32_bf16 v[224:227], v[92:95], v[48:51], v[224:227]\n\t"
    "ds_read_b128 v[60:63], v203 offset:0\n\t"
    "v_exp_f32 v220, v220\n\t"
    "v_mfma_f32_16x16x32_bf16 v[232:235], v[92:95], v[52:55], v[232:235]\n\t"
    "v_add_f32 v251, v251, v219\n\t"
    "v_mfma_f32_16x16x32_bf16 v[164:167], v[8:11], v[120:123], v[164:167]\n\t"
    "ds_read_b64_tr_b16 v[72:73], v245 offset:0\n\t"
    "v_cvt_pk_bf16_f32 v109, v218, v219\n\t"
    "v_exp_f32 v221, v221\n\t"
    "v_mfma_f32_16x16x32_bf16 v[172:175], v[8:11], v[124:127], v[172:175]\n\t"
    "ds_read_b64_tr_b16 v[74:75], v245 offset:4096\n\t"
    "v_add_f32 v251, v251, v220\n\t"
    "v_mfma_f32_16x16x32_bf16 v[228:231], v[12:15], v[48:51], v[228:231]\n\t"
    "ds_read_b128 v[76:79], v203 offset:4096\n\t"
    "v_exp_f32 v222, v222\n\t"
    "v_mfma_f32_16x16x32_bf16 v[236:239], v[12:15], v[52:55], v[236:239]\n\t"
    "v_add_f32 v251, v251, v221\n\t"
    "v_mfma_f32_16x16x32_bf16 v[176:179], v[24:27], v[120:123], v[176:179]\n\t"
    "ds_read_b64_tr_b16 v[88:89], v246 offset:0\n\t"
    "v_cvt_pk_bf16_f32 v110, v220, v221\n\t"
    "v_exp_f32 v223, v223\n\t"
    "v_mfma_f32_16x16x32_bf16 v[184:187], v[24:27], v[124:127], v[184:187]\n\t"
    "ds_read_b64_tr_b16 v[90:91], v246 offset:4096\n\t"
    "v_add_f32 v251, v251, v222\n\t"
    "v_mfma_f32_16x16x32_bf16 v[180:183], v[28:31], v[120:123], v[180:183]\n\t"
    "ds_read_b128 v[92:95], v202 offset:1024\n\t"
    "v_add_f32 v251, v251, v223\n\t"
    "v_mfma_f32_16x16x32_bf16 v[188:191], v[28:31], v[124:127], v[188:191]\n\t"
    "s_add_u32 m0, s60, 65536\n\t"
    "s_mov_b32 s63, 196608\n\t"
    "buffer_load_dwordx4 v243, s[48:51], s63 offen nt lds\n\t"
    "v_cvt_pk_bf16_f32 v111, v222, v223\n\t"
    "s_waitcnt lgkmcnt(9)\n\t"
    "v_mfma_f32_16x16x32_bf16 v[208:211], v[40:43], v[0:3], v[192:195]\n\t"
    "ds_read_b64_tr_b16 v[8:9], v247 offset:0\n\t"
    "v_exp_f32 v224, v224\n\t"
    "v_exp_f32 v225, v225\n\t"
    "v_mfma_f32_16x16x32_bf16 v[216:219], v[40:43], v[4:7], v[196:199]\n\t"
    "ds_read_b64_tr_b16 v[10:11], v247 offset:4096\n\t"
    "v_add_f32 v250, v250, v224\n\t"
    "v_mbcnt_lo_u32_b32 v200, -1, 0\n\t"
    "v_mfma_f32_16x16x32_bf16 v[212:215], v[44:47], v[0:3], v[192:195]\n\t"
    "ds_read_b128 v[12:15], v202 offset:5120\n\t"
    "v_exp_f32 v226, v226\n\t"
    "v_mfma_f32_16x16x32_bf16 v[220:223], v[44:47], v[4:7], v[196:199]\n\t"
    "v_add_f32 v250, v250, v225\n\t"
    "s_waitcnt lgkmcnt(9)\n\t"
    "v_mfma_f32_16x16x32_bf16 v[128:131], v[56:59], v[104:107], v[128:131]\n\t"
    "ds_read_b64_tr_b16 v[24:25], v244 offset:1024\n\t"
    "v_cvt_pk_bf16_f32 v120, v224, v225\n\t"
    "v_exp_f32 v227, v227\n\t"
    "v_mfma_f32_16x16x32_bf16 v[136:139], v[56:59], v[108:111], v[136:139]\n\t"
    "ds_read_b64_tr_b16 v[26:27], v244 offset:5120\n\t"
    "v_add_f32 v250, v250, v226\n\t"
    "v_mbcnt_hi_u32_b32 v200, -1, v200\n\t"
    "v_mfma_f32_16x16x32_bf16 v[208:211], v[60:63], v[16:19], v[208:211]\n\t"
    "ds_read_b128 v[28:31], v203 offset:1024\n\t"
    "v_exp_f32 v228, v228\n\t"
    "v_mfma_f32_16x16x32_bf16 v[216:219], v[60:63], v[20:23], v[216:219]\n\t"
    "v_add_f32 v250, v250, v227\n\t"
    "s_waitcnt lgkmcnt(9)\n\t"
    "v_mfma_f32_16x16x32_bf16 v[132:135], v[72:75], v[104:107], v[132:135]\n\t"
    "ds_read_b64_tr_b16 v[40:41], v245 offset:1024\n\t"
    "s_add_u32 s63, s67, 0\n\t"
    "buffer_store_dwordx4 v[80:83], v252, s[52:55], s63 offen offset:2048 sc1\n\t"
    "v_cvt_pk_bf16_f32 v121, v226, v227\n\t"
    "v_exp_f32 v229, v229\n\t"
    "v_mfma_f32_16x16x32_bf16 v[140:143], v[72:75], v[108:111], v[140:143]\n\t"
    "ds_read_b64_tr_b16 v[42:43], v245 offset:5120\n\t"
    "v_add_f32 v250, v250, v228\n\t"
    "v_bfe_u32 v253, v200, 4, 1\n\t"
    "v_mfma_f32_16x16x32_bf16 v[212:215], v[76:79], v[16:19], v[212:215]\n\t"
    "ds_read_b128 v[44:47], v203 offset:5120\n\t"
    "v_exp_f32 v230, v230\n\t"
    "v_mfma_f32_16x16x32_bf16 v[220:223], v[76:79], v[20:23], v[220:223]\n\t"
    "s_add_u32 m0, s60, 73728\n\t"
    "s_mov_b32 s63, 204800\n\t"
    "buffer_load_dwordx4 v243, s[48:51], s63 offen nt lds\n\t"
    "v_add_f32 v250, v250, v229\n\t"
    "s_waitcnt lgkmcnt(9)\n\t"
    "v_mfma_f32_16x16x32_bf16 v[144:147], v[88:91], v[104:107], v[144:147]\n\t"
    "ds_read_b64_tr_b16 v[56:57], v246 offset:1024\n\t"
    "v_cvt_pk_bf16_f32 v122, v228, v229\n\t"
    "v_exp_f32 v231, v231\n\t"
    "v_mfma_f32_16x16x32_bf16 v[152:155], v[88:91], v[108:111], v[152:155]\n\t"
    "ds_read_b64_tr_b16 v[58:59], v246 offset:5120\n\t"
    "v_add_f32 v250, v250, v230\n\t"
    "v_lshlrev_b32 v253, 2, v253\n\t"
    "v_mfma_f32_16x16x32_bf16 v[208:211], v[92:95], v[32:35], v[208:211]\n\t"
    "ds_read_b64_tr_b16 v[60:61], v247 offset:1024\n\t"
    "s_setprio 2\n\t"
    "v_exp_f32 v232, v232\n\t"
    "v_mfma_f32_16x16x32_bf16 v[216:219], v[92:95], v[36:39], v[216:219]\n\t"
    "ds_read_b64_tr_b16 v[62:63], v247 offset:5120\n\t"
    "v_add_f32 v250, v250, v231\n\t"
    "s_waitcnt lgkmcnt(10)\n\t"
    "v_mfma_f32_16x16x32_bf16 v[148:151], v[8:11], v[104:107], v[148:151]\n\t"
    "ds_read_b128 v[72:75], v202 offset:8192\n\t"
    "v_cvt_pk_bf16_f32 v123, v230, v231\n\t"
    "v_exp_f32 v233, v233\n\t"
    "v_mfma_f32_16x16x32_bf16 v[156:159], v[8:11], v[108:111], v[156:159]\n\t"
    "v_add_f32 v251, v251, v232\n\t"
    "v_bfe_u32 v254, v200, 5, 1\n\t"
    "v_mfma_f32_16x16x32_bf16 v[212:215], v[12:15], v[32:35], v[212:215]\n\t"
    "ds_read_b128 v[76:79], v202 offset:12288\n\t"
    "v_exp_f32 v234, v234\n\t"
    "v_mfma_f32_16x16x32_bf16 v[220:223], v[12:15], v[36:39], v[220:223]\n\t"
    "v_add_f32 v251, v251, v233\n\t"
    "s_waitcnt lgkmcnt(9)\n\t"
    "v_mfma_f32_16x16x32_bf16 v[160:163], v[24:27], v[104:107], v[160:163]\n\t"
    "ds_read_b64_tr_b16 v[88:89], v244 offset:8192\n\t"
    "v_cvt_pk_bf16_f32 v124, v232, v233\n\t"
    "v_exp_f32 v235, v235\n\t"
    "v_mfma_f32_16x16x32_bf16 v[168:171], v[24:27], v[108:111], v[168:171]\n\t"
    "ds_read_b64_tr_b16 v[90:91], v244 offset:12288\n\t"
    "v_add_f32 v251, v251, v234\n\t"
    "v_lshl_add_u32 v253, v254, 1, v253\n\t"
    "v_mfma_f32_16x16x32_bf16 v[208:211], v[28:31], v[48:51], v[208:211]\n\t"
    "ds_read_b128 v[92:95], v203 offset:8192\n\t"
    "v_exp_f32 v236, v236\n\t"
    "v_mfma_f32_16x16x32_bf16 v[216:219], v[28:31], v[52:55], v[216:219]\n\t"
    "v_add_f32 v251, v251, v235\n\t"
    "s_waitcnt lgkmcnt(9)\n\t"
    "v_mfma_f32_16x16x32_bf16 v[164:167], v[40:43], v[104:107], v[164:167]\n\t"
    "ds_read_b64_tr_b16 v[8:9], v245 offset:8192\n\t"
    "v_cvt_pk_bf16_f32 v125, v234, v235\n\t"
    "v_exp_f32 v237, v237\n\t"
    "v_mfma_f32_16x16x32_bf16 v[172:175], v[40:43], v[108:111], v[172:175]\n\t"
    "ds_read_b64_tr_b16 v[10:11], v245 offset:12288\n\t"
    "v_add_f32 v251, v251, v236\n\t"
    "v_and_b32 v254, 7, v200\n\t"
    "v_mfma_f32_16x16x32_bf16 v[212:215], v[44:47], v[48:51], v[212:215]\n\t"
    "ds_read_b128 v[12:15], v203 offset:12288\n\t"
    "v_exp_f32 v238, v238\n\t"
    "v_mfma_f32_16x16x32_bf16 v[220:223], v[44:47], v[52:55], v[220:223]\n\t"
    "v_add_f32 v251, v251, v237\n\t"
    "s_waitcnt lgkmcnt(8)\n\t"
    "v_mfma_f32_16x16x32_bf16 v[176:179], v[56:59], v[104:107], v[176:179]\n\t"
    "ds_read_b64_tr_b16 v[24:25], v246 offset:8192\n\t"
    "s_add_u32 s63, s62, 0\n\t"
    "buffer_store_dwordx4 v[84:87], v252, s[52:55], s63 offen offset:3072 sc1\n\t"
    "v_cvt_pk_bf16_f32 v126, v236, v237\n\t"
    "v_exp_f32 v239, v239\n\t"
    "v_mfma_f32_16x16x32_bf16 v[184:187], v[56:59], v[108:111], v[184:187]\n\t"
    "ds_read_b64_tr_b16 v[26:27], v246 offset:12288\n\t"
    "v_add_f32 v251, v251, v238\n\t"
    "v_xor_b32 v253, v253, v254\n\t"
    "v_mfma_f32_16x16x32_bf16 v[180:183], v[60:63], v[104:107], v[180:183]\n\t"
    "ds_read_b128 v[28:31], v202 offset:9216\n\t"
    "v_add_f32 v251, v251, v239\n\t"
    "v_mfma_f32_16x16x32_bf16 v[188:191], v[60:63], v[108:111], v[188:191]\n\t"
    "v_cvt_pk_bf16_f32 v127, v238, v239\n\t"
    "s_waitcnt lgkmcnt(9)\n\t"
    "v_mfma_f32_16x16x32_bf16 v[224:227], v[72:75], v[0:3], v[192:195]\n\t"
    "ds_read_b64_tr_b16 v[40:41], v247 offset:8192\n\t"
    "s_setprio 1\n\t"
    "v_exp_f32 v208, v208\n\t"
    "v_mfma_f32_16x16x32_bf16 v[232:235], v[72:75], v[4:7], v[196:199]\n\t"
    "ds_read_b64_tr_b16 v[42:43], v247 offset:12288\n\t"
    "v_exp_f32 v209, v209\n\t"
    "v_add_f32 v250, v250, v208\n\t"
    "v_mfma_f32_16x16x32_bf16 v[228:231], v[76:79], v[0:3], v[192:195]\n\t"
    "ds_read_b128 v[44:47], v202 offset:13312\n\t"
    "v_exp_f32 v210, v210\n\t"
    "v_mfma_f32_16x16x32_bf16 v[236:239], v[76:79], v[4:7], v[196:199]\n\t"
    "v_add_f32 v250, v250, v209\n\t"
    "v_lshlrev_b32 v253, 4, v253\n\t"
    "s_waitcnt lgkmcnt(9)\n\t"
    "v_mfma_f32_16x16x32_bf16 v[128:131], v[88:91], v[120:123], v[128:131]\n\t"
    "ds_read_b64_tr_b16 v[56:57], v244 offset:9216\n\t"
    "v_cvt_pk_bf16_f32 v104, v208, v209\n\t"
    "v_exp_f32 v211, v211\n\t"
    "v_mfma_f32_16x16x32_bf16 v[136:139], v[88:91], v[124:127], v[136:139]\n\t"
    "ds_read_b64_tr_b16 v[58:59], v244 offset:13312\n\t"
    "v_add_f32 v250, v250, v210\n\t"
    "v_mfma_f32_16x16x32_bf16 v[224:227], v[92:95], v[16:19], v[224:227]\n\t"
    "ds_read_b128 v[60:63], v203 offset:9216\n\t"
    "v_exp_f32 v212, v212\n\t"
    "v_mfma_f32_16x16x32_bf16 v[232:235], v[92:95], v[20:23], v[232:235]\n\t"
    "v_add_f32 v250, v250, v211\n\t"
    "v_lshrrev_b32 v254, 3, v200\n\t"
    "s_waitcnt lgkmcnt(9)\n\t"
    "v_mfma_f32_16x16x32_bf16 v[132:135], v[8:11], v[120:123], v[132:135]\n\t"
    "ds_read_b64_tr_b16 v[72:73], v245 offset:9216\n\t"
    "v_cvt_pk_bf16_f32 v105, v210, v211\n\t"
    "v_exp_f32 v213, v213\n\t"
    "v_mfma_f32_16x16x32_bf16 v[140:143], v[8:11], v[124:127], v[140:143]\n\t"
    "ds_read_b64_tr_b16 v[74:75], v245 offset:13312\n\t"
    "v_add_f32 v250, v250, v212\n\t"
    "v_mfma_f32_16x16x32_bf16 v[228:231], v[12:15], v[16:19], v[228:231]\n\t"
    "ds_read_b128 v[76:79], v203 offset:13312\n\t"
    "s_setprio 0\n\t"
    "v_exp_f32 v214, v214\n\t"
    "v_mfma_f32_16x16x32_bf16 v[236:239], v[12:15], v[20:23], v[236:239]\n\t"
    "v_add_f32 v250, v250, v213\n\t"
    "s_lshl_b32 s65, s66, 3\n\t"
    "v_add_u32 v254, s65, v254\n\t"
    "s_waitcnt lgkmcnt(9)\n\t"
    "v_mfma_f32_16x16x32_bf16 v[144:147], v[24:27], v[120:123], v[144:147]\n\t"
    "ds_read_b64_tr_b16 v[88:89], v246 offset:9216\n\t"
    "v_cvt_pk_bf16_f32 v106, v212, v213\n\t"
    "v_exp_f32 v215, v215\n\t"
    "v_mfma_f32_16x16x32_bf16 v[152:155], v[24:27], v[124:127], v[152:155]\n\t"
    "ds_read_b64_tr_b16 v[90:91], v246 offset:13312\n\t"
    "v_add_f32 v250, v250, v214\n\t"
    "v_mfma_f32_16x16x32_bf16 v[224:227], v[28:31], v[32:35], v[224:227]\n\t"
    "ds_read_b64_tr_b16 v[92:93], v247 offset:9216\n\t"
    "v_exp_f32 v216, v216\n\t"
    "v_add_f32 v250, v250, v215\n\t"
    "v_mfma_f32_16x16x32_bf16 v[232:235], v[28:31], v[36:39], v[232:235]\n\t"
    "ds_read_b64_tr_b16 v[94:95], v247 offset:13312\n\t"
    "v_lshl_add_u32 v255, v254, 10, v253\n\t"
    "s_waitcnt lgkmcnt(10)\n\t"
    "v_mfma_f32_16x16x32_bf16 v[148:151], v[40:43], v[120:123], v[148:151]\n\t"
    "s_add_u32 s63, s67, 4096\n\t"
    "buffer_store_dwordx4 v[96:99], v252, s[52:55], s63 offen sc1\n\t"
    "v_cvt_pk_bf16_f32 v107, v214, v215\n\t"
    "v_mfma_f32_16x16x32_bf16 v[156:159], v[40:43], v[124:127], v[156:159]\n\t"
    "v_exp_f32 v217, v217\n\t"
    "v_add_f32 v251, v251, v216\n\t"
    "v_mfma_f32_16x16x32_bf16 v[228:231], v[44:47], v[32:35], v[228:231]\n\t"
    "v_exp_f32 v218, v218\n\t"
    "v_add_f32 v251, v251, v217\n\t"
    "s_mov_b64 s[36:37], %[wo]\n\t"
    "s_and_b32 s37, s37, 0xffff\n\t"
    "s_mov_b32 s38, 4194304\n\t"
    "s_mov_b32 s39, 0x20000\n\t"
    "v_mfma_f32_16x16x32_bf16 v[236:239], v[44:47], v[36:39], v[236:239]\n\t"
    "s_waitcnt vmcnt(9) lgkmcnt(0)\n\t"
    "s_barrier\n\t"
    "s_setprio 3\n\t"
    "ds_read_b128 v[8:11], v202 offset:16384\n\t"
    "ds_read_b128 v[12:15], v202 offset:20480\n\t"
    "v_mfma_f32_16x16x32_bf16 v[160:163], v[56:59], v[120:123], v[160:163]\n\t"
    "ds_read_b64_tr_b16 v[24:25], v244 offset:16384\n\t"
    "v_cvt_pk_bf16_f32 v108, v216, v217\n\t"
    "v_exp_f32 v219, v219\n\t"
    "v_mfma_f32_16x16x32_bf16 v[168:171], v[56:59], v[124:127], v[168:171]\n\t"
    "ds_read_b64_tr_b16 v[26:27], v244 offset:20480\n\t"
    "v_add_f32 v251, v251, v218\n\t"
    "v_mfma_f32_16x16x32_bf16 v[224:227], v[60:63], v[48:51], v[224:227]\n\t"
    "ds_read_b128 v[28:31], v203 offset:16384\n\t"
    "v_exp_f32 v220, v220\n\t"
    "v_mfma_f32_16x16x32_bf16 v[232:235], v[60:63], v[52:55], v[232:235]\n\t"
    "v_add_f32 v251, v251, v219\n\t"
    "s_mov_b64 s[40:41], %[ofr]\n\t"
    "s_and_b32 s41, s41, 0xffff\n\t"
    "s_mov_b32 s42, %[onrec]\n\t"
    "s_mov_b32 s43, 0x20000\n\t"
    "v_mfma_f32_16x16x32_bf16 v[164:167], v[72:75], v[120:123], v[164:167]\n\t"
    "ds_read_b64_tr_b16 v[40:41], v245 offset:16384\n\t"
    "v_cvt_pk_bf16_f32 v109, v218, v219\n\t"
    "v_exp_f32 v221, v221\n\t"
    "v_mfma_f32_16x16x32_bf16 v[172:175], v[72:75], v[124:127], v[172:175]\n\t"
    "ds_read_b64_tr_b16 v[42:43], v245 offset:20480\n\t"
    "v_add_f32 v251, v251, v220\n\t"
    "v_mfma_f32_16x16x32_bf16 v[228:231], v[76:79], v[48:51], v[228:231]\n\t"
    "ds_read_b128 v[44:47], v203 offset:20480\n\t"
    "v_exp_f32 v222, v222\n\t"
    "v_mfma_f32_16x16x32_bf16 v[236:239], v[76:79], v[52:55], v[236:239]\n\t"
    "v_add_f32 v251, v251, v221\n\t"
    "s_bfe_u32 s65, %[wgi], 0x10007\n\t"
    "s_mul_i32 s74, s65, 34816\n\t"
    "s_lshl_b32 s77, s65, 7\n\t"
    "s_lshl_b32 s75, s65, 19\n\t"
    "s_xor_b32 s76, s77, 128\n\t"
    "s_add_u32 s76, s76, s75\n\t"
    "s_add_u32 s75, s75, s77\n\t"
    "v_mfma_f32_16x16x32_bf16 v[176:179], v[88:91], v[120:123], v[176:179]\n\t"
    "ds_read_b64_tr_b16 v[56:57], v246 offset:16384\n\t"
    "v_cvt_pk_bf16_f32 v110, v220, v221\n\t"
    "v_exp_f32 v223, v223\n\t"
    "v_mfma_f32_16x16x32_bf16 v[184:187], v[88:91], v[124:127], v[184:187]\n\t"
    "ds_read_b64_tr_b16 v[58:59], v246 offset:20480\n\t"
    "v_add_f32 v251, v251, v222\n\t"
    "v_mfma_f32_16x16x32_bf16 v[180:183], v[92:95], v[120:123], v[180:183]\n\t"
    "ds_read_b128 v[60:63], v202 offset:17408\n\t"
    "v_add_f32 v251, v251, v223\n\t"
    "v_mfma_f32_16x16x32_bf16 v[188:191], v[92:95], v[124:127], v[188:191]\n\t"
    "s_add_u32 m0, s60, 32768\n\t"
    "s_mov_b32 s63, 212992\n\t"
    "buffer_load_dwordx4 v243, s[48:51], s63 offen nt lds\n\t"
    "v_cvt_pk_bf16_f32 v111, v222, v223\n\t"
    "s_waitcnt lgkmcnt(9)\n\t"
    "v_mfma_f32_16x16x32_bf16 v[208:211], v[8:11], v[0:3], v[192:195]\n\t"
    "ds_read_b64_tr_b16 v[72:73], v247 offset:16384\n\t"
    "v_exp_f32 v224, v224\n\t"
    "v_exp_f32 v225, v225\n\t"
    "v_mfma_f32_16x16x32_bf16 v[216:219], v[8:11], v[4:7], v[196:199]\n\t"
    "ds_read_b64_tr_b16 v[74:75], v247 offset:20480\n\t"
    "v_add_f32 v250, v250, v224\n\t"
    "v_mfma_f32_16x16x32_bf16 v[212:215], v[12:15], v[0:3], v[192:195]\n\t"
    "ds_read_b128 v[76:79], v202 offset:21504\n\t"
    "v_exp_f32 v226, v226\n\t"
    "v_mfma_f32_16x16x32_bf16 v[220:223], v[12:15], v[4:7], v[196:199]\n\t"
    "v_add_f32 v250, v250, v225\n\t"
    "s_waitcnt lgkmcnt(9)\n\t"
    "v_mfma_f32_16x16x32_bf16 v[128:131], v[24:27], v[104:107], v[128:131]\n\t"
    "ds_read_b64_tr_b16 v[88:89], v244 offset:17408\n\t"
    "s_add_u32 s63, s62, 4096\n\t"
    "buffer_store_dwordx4 v[100:103], v252, s[52:55], s63 offen offset:1024 sc1\n\t"
    "v_cvt_pk_bf16_f32 v120, v224, v225\n\t"
    "v_mfma_f32_16x16x32_bf16 v[136:139], v[24:27], v[108:111], v[136:139]\n\t"
    "ds_read_b64_tr_b16 v[90:91], v244 offset:21504\n\t"
    "v_exp_f32 v227, v227\n\t"
    "v_add_f32 v250, v250, v226\n\t"
    "v_mfma_f32_16x16x32_bf16 v[208:211], v[28:31], v[16:19], v[208:211]\n\t"
    "ds_read_b128 v[92:95], v203 offset:17408\n\t"
    "v_exp_f32 v228, v228\n\t"
    "v_mfma_f32_16x16x32_bf16 v[216:219], v[28:31], v[20:23], v[216:219]\n\t"
    "v_add_f32 v250, v250, v227\n\t"
    "s_waitcnt lgkmcnt(9)\n\t"
    "v_mfma_f32_16x16x32_bf16 v[132:135], v[40:43], v[104:107], v[132:135]\n\t"
    "ds_read_b64_tr_b16 v[8:9], v245 offset:17408\n\t"
    "v_cvt_pk_bf16_f32 v121, v226, v227\n\t"
    "v_exp_f32 v229, v229\n\t"
    "v_mfma_f32_16x16x32_bf16 v[140:143], v[40:43], v[108:111], v[140:143]\n\t"
    "ds_read_b64_tr_b16 v[10:11], v245 offset:21504\n\t"
    "v_add_f32 v250, v250, v228\n\t"
    "v_mfma_f32_16x16x32_bf16 v[212:215], v[44:47], v[16:19], v[212:215]\n\t"
    "ds_read_b128 v[12:15], v203 offset:21504\n\t"
    "v_exp_f32 v230, v230\n\t"
    "v_mfma_f32_16x16x32_bf16 v[220:223], v[44:47], v[20:23], v[220:223]\n\t"
    "s_add_u32 m0, s60, 40960\n\t"
    "s_mov_b32 s63, 221184\n\t"
    "buffer_load_dwordx4 v243, s[48:51], s63 offen nt lds\n\t"
    "v_add_f32 v250, v250, v229\n\t"
    "s_waitcnt lgkmcnt(9)\n\t"
    "v_mfma_f32_16x16x32_bf16 v[144:147], v[56:59], v[104:107], v[144:147]\n\t"
    "ds_read_b64_tr_b16 v[24:25], v246 offset:17408\n\t"
    "v_cvt_pk_bf16_f32 v122, v228, v229\n\t"
    "v_exp_f32 v231, v231\n\t"
    "v_mfma_f32_16x16x32_bf16 v[152:155], v[56:59], v[108:111], v[152:155]\n\t"
    "ds_read_b64_tr_b16 v[26:27], v246 offset:21504\n\t"
    "v_add_f32 v250, v250, v230\n\t"
    "v_mfma_f32_16x16x32_bf16 v[208:211], v[60:63], v[32:35], v[208:211]\n\t"
    "ds_read_b64_tr_b16 v[28:29], v247 offset:17408\n\t"
    "s_setprio 2\n\t"
    "v_exp_f32 v232, v232\n\t"
    "v_mfma_f32_16x16x32_bf16 v[216:219], v[60:63], v[36:39], v[216:219]\n\t"
    "ds_read_b64_tr_b16 v[30:31], v247 offset:21504\n\t"
    "v_add_f32 v250, v250, v231\n\t"
    "s_waitcnt lgkmcnt(10)\n\t"
    "v_mfma_f32_16x16x32_bf16 v[148:151], v[72:75], v[104:107], v[148:151]\n\t"
    "ds_read_b128 v[40:43], v202 offset:24576\n\t"
    "v_cvt_pk_bf16_f32 v123, v230, v231\n\t"
    "v_mfma_f32_16x16x32_bf16 v[156:159], v[72:75], v[108:111], v[156:159]\n\t"
    "v_exp_f32 v233, v233\n\t"
    "v_add_f32 v251, v251, v232\n\t"
    "v_mfma_f32_16x16x32_bf16 v[212:215], v[76:79], v[32:35], v[212:215]\n\t"
    "ds_read_b128 v[44:47], v202 offset:28672\n\t"
    "v_exp_f32 v234, v234\n\t"
    "v_mfma_f32_16x16x32_bf16 v[220:223], v[76:79], v[36:39], v[220:223]\n\t"
    "v_add_f32 v251, v251, v233\n\t"
    "s_waitcnt lgkmcnt(9)\n\t"
    "v_mfma_f32_16x16x32_bf16 v[160:163], v[88:91], v[104:107], v[160:163]\n\t"
    "ds_read_b64_tr_b16 v[56:57], v244 offset:24576\n\t"
    "v_cvt_pk_bf16_f32 v124, v232, v233\n\t"
    "v_exp_f32 v235, v235\n\t"
    "v_mfma_f32_16x16x32_bf16 v[168:171], v[88:91], v[108:111], v[168:171]\n\t"
    "ds_read_b64_tr_b16 v[58:59], v244 offset:28672\n\t"
    "v_add_f32 v251, v251, v234\n\t"
    "v_mfma_f32_16x16x32_bf16 v[208:211], v[92:95], v[48:51], v[208:211]\n\t"
    "ds_read_b128 v[60:63], v203 offset:24576\n\t"
    "v_exp_f32 v236, v236\n\t"
    "v_mfma_f32_16x16x32_bf16 v[216:219], v[92:95], v[52:55], v[216:219]\n\t"
    "v_add_f32 v251, v251, v235\n\t"
    "s_waitcnt lgkmcnt(9)\n\t"
    "v_mfma_f32_16x16x32_bf16 v[164:167], v[8:11], v[104:107], v[164:167]\n\t"
    "ds_read_b64_tr_b16 v[72:73], v245 offset:24576\n\t"
    "s_add_u32 s63, s67, 4096\n\t"
    "buffer_store_dwordx4 v[112:115], v252, s[52:55], s63 offen offset:2048 sc1\n\t"
    "v_cvt_pk_bf16_f32 v125, v234, v235\n\t"
    "v_mfma_f32_16x16x32_bf16 v[172:175], v[8:11], v[108:111], v[172:175]\n\t"
    "ds_read_b64_tr_b16 v[74:75], v245 offset:28672\n\t"
    "v_exp_f32 v237, v237\n\t"
    "v_add_f32 v251, v251, v236\n\t"
    "v_mfma_f32_16x16x32_bf16 v[212:215], v[12:15], v[48:51], v[212:215]\n\t"
    "ds_read_b128 v[76:79], v203 offset:28672\n\t"
    "v_exp_f32 v238, v238\n\t"
    "v_mfma_f32_16x16x32_bf16 v[220:223], v[12:15], v[52:55], v[220:223]\n\t"
    "v_add_f32 v251, v251, v237\n\t"
    "s_waitcnt lgkmcnt(8)\n\t"
    "v_mfma_f32_16x16x32_bf16 v[176:179], v[24:27], v[104:107], v[176:179]\n\t"
    "ds_read_b64_tr_b16 v[88:89], v246 offset:24576\n\t"
    "v_cvt_pk_bf16_f32 v126, v236, v237\n\t"
    "v_exp_f32 v239, v239\n\t"
    "v_mfma_f32_16x16x32_bf16 v[184:187], v[24:27], v[108:111], v[184:187]\n\t"
    "ds_read_b64_tr_b16 v[90:91], v246 offset:28672\n\t"
    "v_add_f32 v251, v251, v238\n\t"
    "v_mfma_f32_16x16x32_bf16 v[180:183], v[28:31], v[104:107], v[180:183]\n\t"
    "ds_read_b128 v[92:95], v202 offset:25600\n\t"
    "v_add_f32 v251, v251, v239\n\t"
    "v_mfma_f32_16x16x32_bf16 v[188:191], v[28:31], v[108:111], v[188:191]\n\t"
    "v_cvt_pk_bf16_f32 v127, v238, v239\n\t"
    "s_waitcnt lgkmcnt(9)\n\t"
    "v_mfma_f32_16x16x32_bf16 v[224:227], v[40:43], v[0:3], v[192:195]\n\t"
    "ds_read_b64_tr_b16 v[8:9], v247 offset:24576\n\t"
    "s_setprio 1\n\t"
    "v_exp_f32 v208, v208\n\t"
    "v_mfma_f32_16x16x32_bf16 v[232:235], v[40:43], v[4:7], v[196:199]\n\t"
    "ds_read_b64_tr_b16 v[10:11], v247 offset:28672\n\t"
    "v_exp_f32 v209, v209\n\t"
    "v_add_f32 v250, v250, v208\n\t"
    "v_mfma_f32_16x16x32_bf16 v[228:231], v[44:47], v[0:3], v[192:195]\n\t"
    "ds_read_b128 v[12:15], v202 offset:29696\n\t"
    "v_exp_f32 v210, v210\n\t"
    "v_mfma_f32_16x16x32_bf16 v[236:239], v[44:47], v[4:7], v[196:199]\n\t"
    "v_add_f32 v250, v250, v209\n\t"
    "s_waitcnt lgkmcnt(9)\n\t"
    "v_mfma_f32_16x16x32_bf16 v[128:131], v[56:59], v[120:123], v[128:131]\n\t"
    "ds_read_b64_tr_b16 v[24:25], v244 offset:25600\n\t"
    "v_cvt_pk_bf16_f32 v104, v208, v209\n\t"
    "v_exp_f32 v211, v211\n\t"
    "v_mfma_f32_16x16x32_bf16 v[136:139], v[56:59], v[124:127], v[136:139]\n\t"
    "ds_read_b64_tr_b16 v[26:27], v244 offset:29696\n\t"
    "v_add_f32 v250, v250, v210\n\t"
    "v_mfma_f32_16x16x32_bf16 v[224:227], v[60:63], v[16:19], v[224:227]\n\t"
    "ds_read_b128 v[28:31], v203 offset:25600\n\t"
    "v_exp_f32 v212, v212\n\t"
    "v_mfma_f32_16x16x32_bf16 v[232:235], v[60:63], v[20:23], v[232:235]\n\t"
    "v_add_f32 v250, v250, v211\n\t"
    "s_waitcnt lgkmcnt(9)\n\t"
    "v_mfma_f32_16x16x32_bf16 v[132:135], v[72:75], v[120:123], v[132:135]\n\t"
    "ds_read_b64_tr_b16 v[40:41], v245 offset:25600\n\t"
    "v_cvt_pk_bf16_f32 v105, v210, v211\n\t"
    "v_exp_f32 v213, v213\n\t"
    "v_mfma_f32_16x16x32_bf16 v[140:143], v[72:75], v[124:127], v[140:143]\n\t"
    "ds_read_b64_tr_b16 v[42:43], v245 offset:29696\n\t"
    "v_add_f32 v250, v250, v212\n\t"
    "v_mfma_f32_16x16x32_bf16 v[228:231], v[76:79], v[16:19], v[228:231]\n\t"
    "ds_read_b128 v[44:47], v203 offset:29696\n\t"
    "s_setprio 0\n\t"
    "v_mfma_f32_16x16x32_bf16 v[236:239], v[76:79], v[20:23], v[236:239]\n\t"
    "v_exp_f32 v214, v214\n\t"
    "v_add_f32 v250, v250, v213\n\t"
    "s_waitcnt lgkmcnt(9)\n\t"
    "v_mfma_f32_16x16x32_bf16 v[144:147], v[88:91], v[120:123], v[144:147]\n\t"
    "ds_read_b64_tr_b16 v[56:57], v246 offset:25600\n\t"
    "s_add_u32 s63, s62, 4096\n\t"
    "buffer_store_dwordx4 v[116:119], v252, s[52:55], s63 offen offset:3072 sc1\n\t"
    "v_cvt_pk_bf16_f32 v106, v212, v213\n\t"
    "v_mfma_f32_16x16x32_bf16 v[152:155], v[88:91], v[124:127], v[152:155]\n\t"
    "ds_read_b64_tr_b16 v[58:59], v246 offset:29696\n\t"
    "v_exp_f32 v215, v215\n\t"
    "v_add_f32 v250, v250, v214\n\t"
    "v_mfma_f32_16x16x32_bf16 v[224:227], v[92:95], v[32:35], v[224:227]\n\t"
    "ds_read_b64_tr_b16 v[60:61], v247 offset:25600\n\t"
    "v_exp_f32 v216, v216\n\t"
    "v_mfma_f32_16x16x32_bf16 v[232:235], v[92:95], v[36:39], v[232:235]\n\t"
    "ds_read_b64_tr_b16 v[62:63], v247 offset:29696\n\t"
    "v_add_f32 v250, v250, v215\n\t"
    "s_waitcnt lgkmcnt(10)\n\t"
    "v_mfma_f32_16x16x32_bf16 v[148:151], v[8:11], v[120:123], v[148:151]\n\t"
    "v_cvt_pk_bf16_f32 v107, v214, v215\n\t"
    "v_exp_f32 v217, v217\n\t"
    "v_mfma_f32_16x16x32_bf16 v[156:159], v[8:11], v[124:127], v[156:159]\n\t"
    "v_add_f32 v251, v251, v216\n\t"
    "v_mfma_f32_16x16x32_bf16 v[228:231], v[12:15], v[32:35], v[228:231]\n\t"
    "v_exp_f32 v218, v218\n\t"
    "v_add_f32 v251, v251, v217\n\t"
    "v_mfma_f32_16x16x32_bf16 v[236:239], v[12:15], v[36:39], v[236:239]\n\t"
    "s_waitcnt vmcnt(12) lgkmcnt(0)\n\t"
    "s_barrier\n\t"
    "s_setprio 3\n\t"
    "ds_read_b128 v[72:75], v240 offset:16384\n\t"
    "ds_read_b128 v[76:79], v240 offset:20480\n\t"
    "v_mfma_f32_16x16x32_bf16 v[160:163], v[24:27], v[120:123], v[160:163]\n\t"
    "ds_read_b64_tr_b16 v[88:89], v244 offset:49152\n\t"
    "v_cvt_pk_bf16_f32 v108, v216, v217\n\t"
    "v_exp_f32 v219, v219\n\t"
    "v_mfma_f32_16x16x32_bf16 v[168:171], v[24:27], v[124:127], v[168:171]\n\t"
    "ds_read_b64_tr_b16 v[90:91], v244 offset:53248\n\t"
    "v_add_f32 v251, v251, v218\n\t"
    "v_mfma_f32_16x16x32_bf16 v[224:227], v[28:31], v[48:51], v[224:227]\n\t"
    "ds_read_b128 v[92:95], v241 offset:16384\n\t"
    "v_exp_f32 v220, v220\n\t"
    "v_mfma_f32_16x16x32_bf16 v[232:235], v[28:31], v[52:55], v[232:235]\n\t"
    "v_add_f32 v251, v251, v219\n\t"
    "v_mfma_f32_16x16x32_bf16 v[164:167], v[40:43], v[120:123], v[164:167]\n\t"
    "ds_read_b64_tr_b16 v[8:9], v245 offset:49152\n\t"
    "v_cvt_pk_bf16_f32 v109, v218, v219\n\t"
    "v_exp_f32 v221, v221\n\t"
    "v_mfma_f32_16x16x32_bf16 v[172:175], v[40:43], v[124:127], v[172:175]\n\t"
    "ds_read_b64_tr_b16 v[10:11], v245 offset:53248\n\t"
    "v_add_f32 v251, v251, v220\n\t"
    "v_mfma_f32_16x16x32_bf16 v[228:231], v[44:47], v[48:51], v[228:231]\n\t"
    "ds_read_b128 v[12:15], v241 offset:20480\n\t"
    "v_exp_f32 v222, v222\n\t"
    "v_mfma_f32_16x16x32_bf16 v[236:239], v[44:47], v[52:55], v[236:239]\n\t"
    "v_add_f32 v251, v251, v221\n\t"
    "v_mfma_f32_16x16x32_bf16 v[176:179], v[56:59], v[120:123], v[176:179]\n\t"
    "ds_read_b64_tr_b16 v[24:25], v246 offset:49152\n\t"
    "v_cvt_pk_bf16_f32 v110, v220, v221\n\t"
    "v_exp_f32 v223, v223\n\t"
    "v_mfma_f32_16x16x32_bf16 v[184:187], v[56:59], v[124:127], v[184:187]\n\t"
    "ds_read_b64_tr_b16 v[26:27], v246 offset:53248\n\t"
    "v_add_f32 v251, v251, v222\n\t"
    "v_mfma_f32_16x16x32_bf16 v[180:183], v[60:63], v[120:123], v[180:183]\n\t"
    "ds_read_b128 v[28:31], v240 offset:17408\n\t"
    "PAD7_%=: s_cbranch_execz PADRET_%=\n\t"
    "v_add_f32 v251, v251, v223\n\t"
    "v_mfma_f32_16x16x32_bf16 v[188:191], v[60:63], v[124:127], v[188:191]\n\t"
    "s_add_u32 m0, s60, 147456\n\t"
    "s_mov_b32 s63, 229376\n\t"
    "buffer_load_dwordx4 v242, s[44:47], s63 offen nt lds\n\t"
    "v_cvt_pk_bf16_f32 v111, v222, v223\n\t"
    "s_waitcnt lgkmcnt(9)\n\t"
    "v_mfma_f32_16x16x32_bf16 v[208:211], v[72:75], v[0:3], v[192:195]\n\t"
    "ds_read_b64_tr_b16 v[40:41], v247 offset:49152\n\t"
    "v_exp_f32 v224, v224\n\t"
    "v_exp_f32 v225, v225\n\t"
    "v_mfma_f32_16x16x32_bf16 v[216:219], v[72:75], v[4:7], v[196:199]\n\t"
    "ds_read_b64_tr_b16 v[42:43], v247 offset:53248\n\t"
    "v_add_f32 v250, v250, v224\n\t"
    "v_mfma_f32_16x16x32_bf16 v[212:215], v[76:79], v[0:3], v[192:195]\n\t"
    "ds_read_b128 v[44:47], v240 offset:21504\n\t"
    "v_exp_f32 v226, v226\n\t"
    "v_mfma_f32_16x16x32_bf16 v[220:223], v[76:79], v[4:7], v[196:199]\n\t"
    "v_add_f32 v250, v250, v225\n\t"
    "s_waitcnt lgkmcnt(9)\n\t"
    "v_mfma_f32_16x16x32_bf16 v[128:131], v[88:91], v[104:107], v[128:131]\n\t"
    "ds_read_b64_tr_b16 v[56:57], v244 offset:50176\n\t"
    "v_cvt_pk_bf16_f32 v120, v224, v225\n\t"
    "v_exp_f32 v227, v227\n\t"
    "v_mfma_f32_16x16x32_bf16 v[136:139], v[88:91], v[108:111], v[136:139]\n\t"
    "ds_read_b64_tr_b16 v[58:59], v244 offset:54272\n\t"
    "v_add_f32 v250, v250, v226\n\t"
    "v_mfma_f32_16x16x32_bf16 v[208:211], v[92:95], v[16:19], v[208:211]\n\t"
    "ds_read_b128 v[60:63], v241 offset:17408\n\t"
    "v_exp_f32 v228, v228\n\t"
    "v_mfma_f32_16x16x32_bf16 v[216:219], v[92:95], v[20:23], v[216:219]\n\t"
    "v_add_f32 v250, v250, v227\n\t"
    "s_waitcnt lgkmcnt(9)\n\t"
    "v_mfma_f32_16x16x32_bf16 v[132:135], v[8:11], v[104:107], v[132:135]\n\t"
    "ds_read_b64_tr_b16 v[72:73], v245 offset:50176\n\t"
    "v_cvt_pk_bf16_f32 v121, v226, v227\n\t"
    "v_exp_f32 v229, v229\n\t"
    "v_mfma_f32_16x16x32_bf16 v[140:143], v[8:11], v[108:111], v[140:143]\n\t"
    "ds_read_b64_tr_b16 v[74:75], v245 offset:54272\n\t"
    "v_add_f32 v250, v250, v228\n\t"
    "v_mfma_f32_16x16x32_bf16 v[212:215], v[12:15], v[16:19], v[212:215]\n\t"
    "ds_read_b128 v[76:79], v241 offset:21504\n\t"
    "v_exp_f32 v230, v230\n\t"
    "v_mfma_f32_16x16x32_bf16 v[220:223], v[12:15], v[20:23], v[220:223]\n\t"
    "s_add_u32 m0, s60, 155648\n\t"
    "s_mov_b32 s63, 237568\n\t"
    "buffer_load_dwordx4 v242, s[44:47], s63 offen nt lds\n\t"
    "v_add_f32 v250, v250, v229\n\t"
    "s_waitcnt lgkmcnt(9)\n\t"
    "v_mfma_f32_16x16x32_bf16 v[144:147], v[24:27], v[104:107], v[144:147]\n\t"
    "ds_read_b64_tr_b16 v[88:89], v246 offset:50176\n\t"
    "v_cvt_pk_bf16_f32 v122, v228, v229\n\t"
    "v_exp_f32 v231, v231\n\t"
    "v_mfma_f32_16x16x32_bf16 v[152:155], v[24:27], v[108:111], v[152:155]\n\t"
    "ds_read_b64_tr_b16 v[90:91], v246 offset:54272\n\t"
    "v_add_f32 v250, v250, v230\n\t"
    "v_mfma_f32_16x16x32_bf16 v[208:211], v[28:31], v[32:35], v[208:211]\n\t"
    "ds_read_b64_tr_b16 v[92:93], v247 offset:50176\n\t"
    "s_setprio 2\n\t"
    "v_exp_f32 v232, v232\n\t"
    "v_mfma_f32_16x16x32_bf16 v[216:219], v[28:31], v[36:39], v[216:219]\n\t"
    "ds_read_b64_tr_b16 v[94:95], v247 offset:54272\n\t"
    "v_add_f32 v250, v250, v231\n\t"
    "s_waitcnt lgkmcnt(10)\n\t"
    "v_mfma_f32_16x16x32_bf16 v[148:151], v[40:43], v[104:107], v[148:151]\n\t"
    "ds_read_b128 v[8:11], v240 offset:24576\n\t"
    "v_cvt_pk_bf16_f32 v123, v230, v231\n\t"
    "v_mfma_f32_16x16x32_bf16 v[156:159], v[40:43], v[108:111], v[156:159]\n\t"
    "v_exp_f32 v233, v233\n\t"
    "v_add_f32 v251, v251, v232\n\t"
    "v_mfma_f32_16x16x32_bf16 v[212:215], v[44:47], v[32:35], v[212:215]\n\t"
    "ds_read_b128 v[12:15], v240 offset:28672\n\t"
    "v_exp_f32 v234, v234\n\t"
    "v_mfma_f32_16x16x32_bf16 v[220:223], v[44:47], v[36:39], v[220:223]\n\t"
    "v_add_f32 v251, v251, v233\n\t"
    "s_waitcnt lgkmcnt(9)\n\t"
    "v_mfma_f32_16x16x32_bf16 v[160:163], v[56:59], v[104:107], v[160:163]\n\t"
    "ds_read_b64_tr_b16 v[24:25], v244 offset:57344\n\t"
    "v_cvt_pk_bf16_f32 v124, v232, v233\n\t"
    "v_exp_f32 v235, v235\n\t"
    "v_mfma_f32_16x16x32_bf16 v[168:171], v[56:59], v[108:111], v[168:171]\n\t"
    "ds_read_b64_tr_b16 v[26:27], v244 offset:61440\n\t"
    "v_add_f32 v251, v251, v234\n\t"
    "v_mfma_f32_16x16x32_bf16 v[208:211], v[60:63], v[48:51], v[208:211]\n\t"
    "ds_read_b128 v[28:31], v241 offset:24576\n\t"
    "v_exp_f32 v236, v236\n\t"
    "v_mfma_f32_16x16x32_bf16 v[216:219], v[60:63], v[52:55], v[216:219]\n\t"
    "s_add_u32 m0, s60, 49152\n\t"
    "s_mov_b32 s63, 229376\n\t"
    "buffer_load_dwordx4 v243, s[48:51], s63 offen nt lds\n\t"
    "v_add_f32 v251, v251, v235\n\t"
    "s_waitcnt lgkmcnt(9)\n\t"
    "v_mfma_f32_16x16x32_bf16 v[164:167], v[72:75], v[104:107], v[164:167]\n\t"
    "ds_read_b64_tr_b16 v[40:41], v245 offset:57344\n\t"
    "v_cvt_pk_bf16_f32 v125, v234, v235\n\t"
    "v_exp_f32 v237, v237\n\t"
    "v_mfma_f32_16x16x32_bf16 v[172:175], v[72:75], v[108:111], v[172:175]\n\t"
    "ds_read_b64_tr_b16 v[42:43], v245 offset:61440\n\t"
    "v_add_f32 v251, v251, v236\n\t"
    "v_mfma_f32_16x16x32_bf16 v[212:215], v[76:79], v[48:51], v[212:215]\n\t"
    "ds_read_b128 v[44:47], v241 offset:28672\n\t"
    "v_exp_f32 v238, v238\n\t"
    "v_mfma_f32_16x16x32_bf16 v[220:223], v[76:79], v[52:55], v[220:223]\n\t"
    "v_add_f32 v251, v251, v237\n\t"
    "s_waitcnt lgkmcnt(8)\n\t"
    "v_mfma_f32_16x16x32_bf16 v[176:179], v[88:91], v[104:107], v[176:179]\n\t"
    "ds_read_b64_tr_b16 v[56:57], v246 offset:57344\n\t"
    "v_cvt_pk_bf16_f32 v126, v236, v237\n\t"
    "v_exp_f32 v239, v239\n\t"
    "v_mfma_f32_16x16x32_bf16 v[184:187], v[88:91], v[108:111], v[184:187]\n\t"
    "ds_read_b64_tr_b16 v[58:59], v246 offset:61440\n\t"
    "v_add_f32 v251, v251, v238\n\t"
    "v_mfma_f32_16x16x32_bf16 v[180:183], v[92:95], v[104:107], v[180:183]\n\t"
    "ds_read_b128 v[60:63], v240 offset:25600\n\t"
    "v_add_f32 v251, v251, v239\n\t"
    "v_mfma_f32_16x16x32_bf16 v[188:191], v[92:95], v[108:111], v[188:191]\n\t"
    "v_cvt_pk_bf16_f32 v127, v238, v239\n\t"
    "s_waitcnt lgkmcnt(9)\n\t"
    "v_mfma_f32_16x16x32_bf16 v[224:227], v[8:11], v[0:3], v[192:195]\n\t"
    "ds_read_b64_tr_b16 v[72:73], v247 offset:57344\n\t"
    "s_setprio 1\n\t"
    "v_exp_f32 v208, v208\n\t"
    "v_mfma_f32_16x16x32_bf16 v[232:235], v[8:11], v[4:7], v[196:199]\n\t"
    "ds_read_b64_tr_b16 v[74:75], v247 offset:61440\n\t"
    "v_exp_f32 v209, v209\n\t"
    "v_add_f32 v250, v250, v208\n\t"
    "v_mfma_f32_16x16x32_bf16 v[228:231], v[12:15], v[0:3], v[192:195]\n\t"
    "ds_read_b128 v[76:79], v240 offset:29696\n\t"
    "v_exp_f32 v210, v210\n\t"
    "v_mfma_f32_16x16x32_bf16 v[236:239], v[12:15], v[4:7], v[196:199]\n\t"
    "s_add_u32 m0, s60, 57344\n\t"
    "s_mov_b32 s63, 237568\n\t"
    "buffer_load_dwordx4 v243, s[48:51], s63 offen nt lds\n\t"
    "v_add_f32 v250, v250, v209\n\t"
    "s_waitcnt lgkmcnt(9)\n\t"
    "v_mfma_f32_16x16x32_bf16 v[128:131], v[24:27], v[120:123], v[128:131]\n\t"
    "ds_read_b64_tr_b16 v[88:89], v244 offset:58368\n\t"
    "v_cvt_pk_bf16_f32 v104, v208, v209\n\t"
    "v_exp_f32 v211, v211\n\t"
    "v_mfma_f32_16x16x32_bf16 v[136:139], v[24:27], v[124:127], v[136:139]\n\t"
    "ds_read_b64_tr_b16 v[90:91], v244 offset:62464\n\t"
    "v_add_f32 v250, v250, v210\n\t"
    "v_mfma_f32_16x16x32_bf16 v[224:227], v[28:31], v[16:19], v[224:227]\n\t"
    "ds_read_b128 v[92:95], v241 offset:25600\n\t"
    "v_exp_f32 v212, v212\n\t"
    "v_mfma_f32_16x16x32_bf16 v[232:235], v[28:31], v[20:23], v[232:235]\n\t"
    "v_add_f32 v250, v250, v211\n\t"
    "s_waitcnt lgkmcnt(9)\n\t"
    "v_mfma_f32_16x16x32_bf16 v[132:135], v[40:43], v[120:123], v[132:135]\n\t"
    "ds_read_b64_tr_b16 v[8:9], v245 offset:58368\n\t"
    "v_cvt_pk_bf16_f32 v105, v210, v211\n\t"
    "v_exp_f32 v213, v213\n\t"
    "v_mfma_f32_16x16x32_bf16 v[140:143], v[40:43], v[124:127], v[140:143]\n\t"
    "ds_read_b64_tr_b16 v[10:11], v245 offset:62464\n\t"
    "v_add_f32 v250, v250, v212\n\t"
    "v_mfma_f32_16x16x32_bf16 v[228:231], v[44:47], v[16:19], v[228:231]\n\t"
    "ds_read_b128 v[12:15], v241 offset:29696\n\t"
    "s_setprio 0\n\t"
    "v_mfma_f32_16x16x32_bf16 v[236:239], v[44:47], v[20:23], v[236:239]\n\t"
    "v_exp_f32 v214, v214\n\t"
    "v_add_f32 v250, v250, v213\n\t"
    "s_waitcnt lgkmcnt(9)\n\t"
    "v_mfma_f32_16x16x32_bf16 v[144:147], v[56:59], v[120:123], v[144:147]\n\t"
    "ds_read_b64_tr_b16 v[24:25], v246 offset:58368\n\t"
    "v_cvt_pk_bf16_f32 v106, v212, v213\n\t"
    "v_exp_f32 v215, v215\n\t"
    "v_mfma_f32_16x16x32_bf16 v[152:155], v[56:59], v[124:127], v[152:155]\n\t"
    "ds_read_b64_tr_b16 v[26:27], v246 offset:62464\n\t"
    "v_add_f32 v250, v250, v214\n\t"
    "v_mfma_f32_16x16x32_bf16 v[224:227], v[60:63], v[32:35], v[224:227]\n\t"
    "ds_read_b64_tr_b16 v[28:29], v247 offset:58368\n\t"
    "v_exp_f32 v216, v216\n\t"
    "v_mfma_f32_16x16x32_bf16 v[232:235], v[60:63], v[36:39], v[232:235]\n\t"
    "ds_read_b64_tr_b16 v[30:31], v247 offset:62464\n\t"
    "v_add_f32 v250, v250, v215\n\t"
    "s_waitcnt lgkmcnt(10)\n\t"
    "v_mfma_f32_16x16x32_bf16 v[148:151], v[72:75], v[120:123], v[148:151]\n\t"
    "v_cvt_pk_bf16_f32 v107, v214, v215\n\t"
    "v_exp_f32 v217, v217\n\t"
    "v_mfma_f32_16x16x32_bf16 v[156:159], v[72:75], v[124:127], v[156:159]\n\t"
    "v_add_f32 v251, v251, v216\n\t"
    "v_mfma_f32_16x16x32_bf16 v[228:231], v[76:79], v[32:35], v[228:231]\n\t"
    "v_exp_f32 v218, v218\n\t"
    "v_add_f32 v251, v251, v217\n\t"
    "v_mfma_f32_16x16x32_bf16 v[236:239], v[76:79], v[36:39], v[236:239]\n\t"
    "s_waitcnt vmcnt(11) lgkmcnt(0)\n\t"
    "s_barrier\n\t"
    "s_setprio 3\n\t"
    "ds_read_b128 v[40:43], v240 offset:32768\n\t"
    "ds_read_b128 v[44:47], v240 offset:36864\n\t"
    "v_mfma_f32_16x16x32_bf16 v[160:163], v[88:91], v[120:123], v[160:163]\n\t"
    "ds_read_b64_tr_b16 v[56:57], v244 offset:32768\n\t"
    "v_cvt_pk_bf16_f32 v108, v216, v217\n\t"
    "v_exp_f32 v219, v219\n\t"
    "v_mfma_f32_16x16x32_bf16 v[168:171], v[88:91], v[124:127], v[168:171]\n\t"
    "ds_read_b64_tr_b16 v[58:59], v244 offset:36864\n\t"
    "v_add_f32 v251, v251, v218\n\t"
    "v_mfma_f32_16x16x32_bf16 v[224:227], v[92:95], v[48:51], v[224:227]\n\t"
    "ds_read_b128 v[60:63], v241 offset:32768\n\t"
    "v_exp_f32 v220, v220\n\t"
    "v_mfma_f32_16x16x32_bf16 v[232:235], v[92:95], v[52:55], v[232:235]\n\t"
    "v_add_f32 v251, v251, v219\n\t"
    "v_mfma_f32_16x16x32_bf16 v[164:167], v[8:11], v[120:123], v[164:167]\n\t"
    "ds_read_b64_tr_b16 v[72:73], v245 offset:32768\n\t"
    "v_cvt_pk_bf16_f32 v109, v218, v219\n\t"
    "v_exp_f32 v221, v221\n\t"
    "v_mfma_f32_16x16x32_bf16 v[172:175], v[8:11], v[124:127], v[172:175]\n\t"
    "ds_read_b64_tr_b16 v[74:75], v245 offset:36864\n\t"
    "v_add_f32 v251, v251, v220\n\t"
    "v_mfma_f32_16x16x32_bf16 v[228:231], v[12:15], v[48:51], v[228:231]\n\t"
    "ds_read_b128 v[76:79], v241 offset:36864\n\t"
    "v_exp_f32 v222, v222\n\t"
    "v_mfma_f32_16x16x32_bf16 v[236:239], v[12:15], v[52:55], v[236:239]\n\t"
    "v_add_f32 v251, v251, v221\n\t"
    "v_mfma_f32_16x16x32_bf16 v[176:179], v[24:27], v[120:123], v[176:179]\n\t"
    "ds_read_b64_tr_b16 v[88:89], v246 offset:32768\n\t"
    "v_cvt_pk_bf16_f32 v110, v220, v221\n\t"
    "v_exp_f32 v223, v223\n\t"
    "v_mfma_f32_16x16x32_bf16 v[184:187], v[24:27], v[124:127], v[184:187]\n\t"
    "ds_read_b64_tr_b16 v[90:91], v246 offset:36864\n\t"
    "v_add_f32 v251, v251, v222\n\t"
    "v_mfma_f32_16x16x32_bf16 v[180:183], v[28:31], v[120:123], v[180:183]\n\t"
    "ds_read_b128 v[92:95], v240 offset:33792\n\t"
    "v_add_f32 v251, v251, v223\n\t"
    "v_mfma_f32_16x16x32_bf16 v[188:191], v[28:31], v[124:127], v[188:191]\n\t"
    "s_add_u32 m0, s60, 98304\n\t"
    "s_mov_b32 s63, 245760\n\t"
    "buffer_load_dwordx4 v242, s[44:47], s63 offen nt lds\n\t"
    "v_cvt_pk_bf16_f32 v111, v222, v223\n\t"
    "s_waitcnt lgkmcnt(9)\n\t"
    "v_mfma_f32_16x16x32_bf16 v[208:211], v[40:43], v[0:3], v[192:195]\n\t"
    "ds_read_b64_tr_b16 v[8:9], v247 offset:32768\n\t"
    "v_exp_f32 v224, v224\n\t"
    "v_exp_f32 v225, v225\n\t"
    "v_mfma_f32_16x16x32_bf16 v[216:219], v[40:43], v[4:7], v[196:199]\n\t"
    "ds_read_b64_tr_b16 v[10:11], v247 offset:36864\n\t"
    "v_add_f32 v250, v250, v224\n\t"
    "v_mov_b32 v206, s72\n\t"
    "v_mov_b32 v207, s73\n\t"
    "v_mfma_f32_16x16x32_bf16 v[212:215], v[44:47], v[0:3], v[192:195]\n\t"
    "ds_read_b128 v[12:15], v240 offset:37888\n\t"
    "v_exp_f32 v226, v226\n\t"
    "v_mfma_f32_16x16x32_bf16 v[220:223], v[44:47], v[4:7], v[196:199]\n\t"
    "v_add_f32 v250, v250, v225\n\t"
    "s_waitcnt lgkmcnt(9)\n\t"
    "v_mfma_f32_16x16x32_bf16 v[128:131], v[56:59], v[104:107], v[128:131]\n\t"
    "ds_read_b64_tr_b16 v[24:25], v244 offset:33792\n\t"
    "v_cvt_pk_bf16_f32 v120, v224, v225\n\t"
    "v_exp_f32 v227, v227\n\t"
    "v_mfma_f32_16x16x32_bf16 v[136:139], v[56:59], v[108:111], v[136:139]\n\t"
    "ds_read_b64_tr_b16 v[26:27], v244 offset:37888\n\t"
    "v_add_f32 v250, v250, v226\n\t"
    "v_mfma_f32_16x16x32_bf16 v[208:211], v[60:63], v[16:19], v[208:211]\n\t"
    "ds_read_b128 v[28:31], v241 offset:33792\n\t"
    "v_exp_f32 v228, v228\n\t"
    "v_mfma_f32_16x16x32_bf16 v[216:219], v[60:63], v[20:23], v[216:219]\n\t"
    "v_add_f32 v250, v250, v227\n\t"
    "s_waitcnt lgkmcnt(9)\n\t"
    "v_mfma_f32_16x16x32_bf16 v[132:135], v[72:75], v[104:107], v[132:135]\n\t"
    "ds_read_b64_tr_b16 v[40:41], v245 offset:33792\n\t"
    "v_cvt_pk_bf16_f32 v121, v226, v227\n\t"
    "v_exp_f32 v229, v229\n\t"
    "v_mfma_f32_16x16x32_bf16 v[140:143], v[72:75], v[108:111], v[140:143]\n\t"
    "ds_read_b64_tr_b16 v[42:43], v245 offset:37888\n\t"
    "v_add_f32 v250, v250, v228\n\t"
    "v_mfma_f32_16x16x32_bf16 v[212:215], v[76:79], v[16:19], v[212:215]\n\t"
    "ds_read_b128 v[44:47], v241 offset:37888\n\t"
    "v_exp_f32 v230, v230\n\t"
    "v_mfma_f32_16x16x32_bf16 v[220:223], v[76:79], v[20:23], v[220:223]\n\t"
    "s_add_u32 m0, s60, 106496\n\t"
    "s_mov_b32 s63, 253952\n\t"
    "buffer_load_dwordx4 v242, s[44:47], s63 offen nt lds\n\t"
    "v_add_f32 v250, v250, v229\n\t"
    "v_lshrrev_b32 v202, 1, v252\n\t"
    "v_mov_b32 v203, s67\n\t"
    "s_mov_b64 vcc, 2\n\t"
    "v_mov_b32 v204, s62\n\t"
    "v_cndmask_b32 v203, v203, v204, vcc\n\t"
    "s_mov_b64 vcc, 3\n\t"
    "v_add_u32 v202, v202, v203\n\t"
    "v_bfrev_b32 v203, 1\n\t"
    "v_cndmask_b32 v202, v203, v202, vcc\n\t"
    "s_waitcnt lgkmcnt(9)\n\t"
    "v_mfma_f32_16x16x32_bf16 v[144:147], v[88:91], v[104:107], v[144:147]\n\t"
    "ds_read_b64_tr_b16 v[56:57], v246 offset:33792\n\t"
    "v_cvt_pk_bf16_f32 v122, v228, v229\n\t"
    "v_exp_f32 v231, v231\n\t"
    "v_mfma_f32_16x16x32_bf16 v[152:155], v[88:91], v[108:111], v[152:155]\n\t"
    "ds_read_b64_tr_b16 v[58:59], v246 offset:37888\n\t"
    "v_add_f32 v250, v250, v230\n\t"
    "v_mfma_f32_16x16x32_bf16 v[208:211], v[92:95], v[32:35], v[208:211]\n\t"
    "ds_read_b64_tr_b16 v[60:61], v247 offset:33792\n\t"
    "s_setprio 2\n\t"
    "v_exp_f32 v232, v232\n\t"
    "v_mfma_f32_16x16x32_bf16 v[216:219], v[92:95], v[36:39], v[216:219]\n\t"
    "ds_read_b64_tr_b16 v[62:63], v247 offset:37888\n\t"
    "v_add_f32 v250, v250, v231\n\t"
    "s_waitcnt lgkmcnt(10)\n\t"
    "v_mfma_f32_16x16x32_bf16 v[148:151], v[8:11], v[104:107], v[148:151]\n\t"
    "ds_read_b128 v[72:75], v240 offset:40960\n\t"
    "v_cvt_pk_bf16_f32 v123, v230, v231\n\t"
    "v_mfma_f32_16x16x32_bf16 v[156:159], v[8:11], v[108:111], v[156:159]\n\t"
    "v_exp_f32 v233, v233\n\t"
    "v_add_f32 v251, v251, v232\n\t"
    "v_mfma_f32_16x16x32_bf16 v[212:215], v[12:15], v[32:35], v[212:215]\n\t"
    "ds_read_b128 v[76:79], v240 offset:45056\n\t"
    "v_exp_f32 v234, v234\n\t"
    "v_mfma_f32_16x16x32_bf16 v[220:223], v[12:15], v[36:39], v[220:223]\n\t"
    "v_add_f32 v251, v251, v233\n\t"
    "s_waitcnt lgkmcnt(9)\n\t"
    "v_mfma_f32_16x16x32_bf16 v[160:163], v[24:27], v[104:107], v[160:163]\n\t"
    "ds_read_b64_tr_b16 v[88:89], v244 offset:40960\n\t"
    "v_cvt_pk_bf16_f32 v124, v232, v233\n\t"
    "v_exp_f32 v235, v235\n\t"
    "v_mfma_f32_16x16x32_bf16 v[168:171], v[24:27], v[108:111], v[168:171]\n\t"
    "ds_read_b64_tr_b16 v[90:91], v244 offset:45056\n\t"
    "v_add_f32 v251, v251, v234\n\t"
    "s_waitcnt vmcnt(6)\n\t"
    "s_add_u32 s63, %[myfl], 0\n\t"
    "s_cmp_eq_u32 s64, 0\n\t"
    "s_cselect_b32 s63, s63, 1073741824\n\t"
    "buffer_store_dwordx2 v[206:207], v202, s[56:59], s63 offen sc1\n\t"
    "v_mfma_f32_16x16x32_bf16 v[208:211], v[28:31], v[48:51], v[208:211]\n\t"
    "ds_read_b128 v[92:95], v241 offset:40960\n\t"
    "v_exp_f32 v236, v236\n\t"
    "v_mfma_f32_16x16x32_bf16 v[216:219], v[28:31], v[52:55], v[216:219]\n\t"
    "s_add_u32 m0, s60, 81920\n\t"
    "s_mov_b32 s63, 245760\n\t"
    "buffer_load_dwordx4 v243, s[48:51], s63 offen nt lds\n\t"
    "v_add_f32 v251, v251, v235\n\t"
    "s_waitcnt lgkmcnt(9)\n\t"
    "v_mfma_f32_16x16x32_bf16 v[164:167], v[40:43], v[104:107], v[164:167]\n\t"
    "ds_read_b64_tr_b16 v[8:9], v245 offset:40960\n\t"
    "v_cvt_pk_bf16_f32 v125, v234, v235\n\t"
    "v_exp_f32 v237, v237\n\t"
    "v_mfma_f32_16x16x32_bf16 v[172:175], v[40:43], v[108:111], v[172:175]\n\t"
    "ds_read_b64_tr_b16 v[10:11], v245 offset:45056\n\t"
    "v_add_f32 v251, v251, v236\n\t"
    "v_mfma_f32_16x16x32_bf16 v[212:215], v[44:47], v[48:51], v[212:215]\n\t"
    "ds_read_b128 v[12:15], v241 offset:45056\n\t"
    "v_exp_f32 v238, v238\n\t"
    "v_mfma_f32_16x16x32_bf16 v[220:223], v[44:47], v[52:55], v[220:223]\n\t"
    "v_add_f32 v251, v251, v237\n\t"
    "s_waitcnt lgkmcnt(8)\n\t"
    "v_mfma_f32_16x16x32_bf16 v[176:179], v[56:59], v[104:107], v[176:179]\n\t"
    "ds_read_b64_tr_b16 v[24:25], v246 offset:40960\n\t"
    "v_cvt_pk_bf16_f32 v126, v236, v237\n\t"
    "v_exp_f32 v239, v239\n\t"
    "v_mfma_f32_16x16x32_bf16 v[184:187], v[56:59], v[108:111], v[184:187]\n\t"
    "ds_read_b64_tr_b16 v[26:27], v246 offset:45056\n\t"
    "v_add_f32 v251, v251, v238\n\t"
    "v_mfma_f32_16x16x32_bf16 v[180:183], v[60:63], v[104:107], v[180:183]\n\t"
    "ds_read_b128 v[28:31], v240 offset:41984\n\t"
    "v_add_f32 v251, v251, v239\n\t"
    "v_mfma_f32_16x16x32_bf16 v[188:191], v[60:63], v[108:111], v[188:191]\n\t"
    "v_cvt_pk_bf16_f32 v127, v238, v239\n\t"
    "s_waitcnt lgkmcnt(9)\n\t"
    "v_mfma_f32_16x16x32_bf16 v[224:227], v[72:75], v[0:3], v[192:195]\n\t"
    "ds_read_b64_tr_b16 v[40:41], v247 offset:40960\n\t"
    "s_setprio 1\n\t"
    "v_exp_f32 v208, v208\n\t"
    "v_mfma_f32_16x16x32_bf16 v[232:235], v[72:75], v[4:7], v[196:199]\n\t"
    "ds_read_b64_tr_b16 v[42:43], v247 offset:45056\n\t"
    "v_exp_f32 v209, v209\n\t"
    "v_add_f32 v250, v250, v208\n\t"
    "v_mfma_f32_16x16x32_bf16 v[228:231], v[76:79], v[0:3], v[192:195]\n\t"
    "ds_read_b128 v[44:47], v240 offset:46080\n\t"
    "v_exp_f32 v210, v210\n\t"
    "v_mfma_f32_16x16x32_bf16 v[236:239], v[76:79], v[4:7], v[196:199]\n\t"
    "s_add_u32 m0, s60, 90112\n\t"
    "s_mov_b32 s63, 253952\n\t"
    "buffer_load_dwordx4 v243, s[48:51], s63 offen nt lds\n\t"
    "v_add_f32 v250, v250, v209\n\t"
    "s_waitcnt lgkmcnt(9)\n\t"
    "v_mfma_f32_16x16x32_bf16 v[128:131], v[88:91], v[120:123], v[128:131]\n\t"
    "ds_read_b64_tr_b16 v[56:57], v244 offset:41984\n\t"
    "v_cvt_pk_bf16_f32 v104, v208, v209\n\t"
    "v_exp_f32 v211, v211\n\t"
    "v_mfma_f32_16x16x32_bf16 v[136:139], v[88:91], v[124:127], v[136:139]\n\t"
    "ds_read_b64_tr_b16 v[58:59], v244 offset:46080\n\t"
    "v_add_f32 v250, v250, v210\n\t"
    "v_mfma_f32_16x16x32_bf16 v[224:227], v[92:95], v[16:19], v[224:227]\n\t"
    "ds_read_b128 v[60:63], v241 offset:41984\n\t"
    "v_exp_f32 v212, v212\n\t"
    "v_mfma_f32_16x16x32_bf16 v[232:235], v[92:95], v[20:23], v[232:235]\n\t"
    "v_add_f32 v250, v250, v211\n\t"
    "s_waitcnt lgkmcnt(9)\n\t"
    "v_mfma_f32_16x16x32_bf16 v[132:135], v[8:11], v[120:123], v[132:135]\n\t"
    "ds_read_b64_tr_b16 v[72:73], v245 offset:41984\n\t"
    "v_cvt_pk_bf16_f32 v105, v210, v211\n\t"
    "v_exp_f32 v213, v213\n\t"
    "v_mfma_f32_16x16x32_bf16 v[140:143], v[8:11], v[124:127], v[140:143]\n\t"
    "ds_read_b64_tr_b16 v[74:75], v245 offset:46080\n\t"
    "v_add_f32 v250, v250, v212\n\t"
    "v_mfma_f32_16x16x32_bf16 v[228:231], v[12:15], v[16:19], v[228:231]\n\t"
    "ds_read_b128 v[76:79], v241 offset:46080\n\t"
    "s_setprio 0\n\t"
    "v_mfma_f32_16x16x32_bf16 v[236:239], v[12:15], v[20:23], v[236:239]\n\t"
    "v_exp_f32 v214, v214\n\t"
    "v_add_f32 v250, v250, v213\n\t"
    "s_waitcnt lgkmcnt(9)\n\t"
    "v_mfma_f32_16x16x32_bf16 v[144:147], v[24:27], v[120:123], v[144:147]\n\t"
    "ds_read_b64_tr_b16 v[88:89], v246 offset:41984\n\t"
    "v_cvt_pk_bf16_f32 v106, v212, v213\n\t"
    "v_exp_f32 v215, v215\n\t"
    "v_mfma_f32_16x16x32_bf16 v[152:155], v[24:27], v[124:127], v[152:155]\n\t"
    "ds_read_b64_tr_b16 v[90:91], v246 offset:46080\n\t"
    "v_add_f32 v250, v250, v214\n\t"
    "v_mfma_f32_16x16x32_bf16 v[224:227], v[28:31], v[32:35], v[224:227]\n\t"
    "ds_read_b64_tr_b16 v[92:93], v247 offset:41984\n\t"
    "v_exp_f32 v216, v216\n\t"
    "v_mfma_f32_16x16x32_bf16 v[232:235], v[28:31], v[36:39], v[232:235]\n\t"
    "ds_read_b64_tr_b16 v[94:95], v247 offset:46080\n\t"
    "v_add_f32 v250, v250, v215\n\t"
    "s_waitcnt lgkmcnt(10)\n\t"
    "v_mfma_f32_16x16x32_bf16 v[148:151], v[40:43], v[120:123], v[148:151]\n\t"
    "v_cvt_pk_bf16_f32 v107, v214, v215\n\t"
    "v_exp_f32 v217, v217\n\t"
    "v_mfma_f32_16x16x32_bf16 v[156:159], v[40:43], v[124:127], v[156:159]\n\t"
    "v_add_f32 v251, v251, v216\n\t"
    "v_mfma_f32_16x16x32_bf16 v[228:231], v[44:47], v[32:35], v[228:231]\n\t"
    "v_exp_f32 v218, v218\n\t"
    "v_add_f32 v251, v251, v217\n\t"
    "v_mfma_f32_16x16x32_bf16 v[236:239], v[44:47], v[36:39], v[236:239]\n\t"
    "s_waitcnt vmcnt(7) lgkmcnt(0)\n\t"
    "s_barrier\n\t"
    "s_setprio 3\n\t"
    "ds_read_b128 v[8:11], v240 offset:49152\n\t"
    "ds_read_b128 v[12:15], v240 offset:53248\n\t"
    "v_mfma_f32_16x16x32_bf16 v[160:163], v[56:59], v[120:123], v[160:163]\n\t"
    "ds_read_b64_tr_b16 v[24:25], v244 offset:0\n\t"
    "v_cvt_pk_bf16_f32 v108, v216, v217\n\t"
    "v_exp_f32 v219, v219\n\t"
    "v_mfma_f32_16x16x32_bf16 v[168:171], v[56:59], v[124:127], v[168:171]\n\t"
    "ds_read_b64_tr_b16 v[26:27], v244 offset:4096\n\t"
    "v_add_f32 v251, v251, v218\n\t"
    "v_mfma_f32_16x16x32_bf16 v[224:227], v[60:63], v[48:51], v[224:227]\n\t"
    "ds_read_b128 v[28:31], v241 offset:49152\n\t"
    "v_exp_f32 v220, v220\n\t"
    "v_mfma_f32_16x16x32_bf16 v[232:235], v[60:63], v[52:55], v[232:235]\n\t"
    "v_add_f32 v251, v251, v219\n\t"
    "v_mfma_f32_16x16x32_bf16 v[164:167], v[72:75], v[120:123], v[164:167]\n\t"
    "ds_read_b64_tr_b16 v[40:41], v245 offset:0\n\t"
    "v_cvt_pk_bf16_f32 v109, v218, v219\n\t"
    "v_exp_f32 v221, v221\n\t"
    "v_mfma_f32_16x16x32_bf16 v[172:175], v[72:75], v[124:127], v[172:175]\n\t"
    "ds_read_b64_tr_b16 v[42:43], v245 offset:4096\n\t"
    "v_add_f32 v251, v251, v220\n\t"
    "v_mfma_f32_16x16x32_bf16 v[228:231], v[76:79], v[48:51], v[228:231]\n\t"
    "ds_read_b128 v[44:47], v241 offset:53248\n\t"
    "v_exp_f32 v222, v222\n\t"
    "v_mfma_f32_16x16x32_bf16 v[236:239], v[76:79], v[52:55], v[236:239]\n\t"
    "v_add_f32 v251, v251, v221\n\t"
    "v_mfma_f32_16x16x32_bf16 v[176:179], v[88:91], v[120:123], v[176:179]\n\t"
    "ds_read_b64_tr_b16 v[56:57], v246 offset:0\n\t"
    "v_cvt_pk_bf16_f32 v110, v220, v221\n\t"
    "v_exp_f32 v223, v223\n\t"
    "v_mfma_f32_16x16x32_bf16 v[184:187], v[88:91], v[124:127], v[184:187]\n\t"
    "ds_read_b64_tr_b16 v[58:59], v246 offset:4096\n\t"
    "v_add_f32 v251, v251, v222\n\t"
    "v_mfma_f32_16x16x32_bf16 v[180:183], v[92:95], v[120:123], v[180:183]\n\t"
    "ds_read_b128 v[60:63], v240 offset:50176\n\t"
    "v_add_f32 v251, v251, v223\n\t"
    "v_mfma_f32_16x16x32_bf16 v[188:191], v[92:95], v[124:127], v[188:191]\n\t"
    "s_add_u32 m0, s61, 0\n\t"
    "s_add_u32 s63, s75, 0\n\t"
    "buffer_load_dwordx4 v255, s[36:39], s63 offen lds\n\t"
    "v_cvt_pk_bf16_f32 v111, v222, v223\n\t"
    "s_waitcnt lgkmcnt(9)\n\t"
    "v_mfma_f32_16x16x32_bf16 v[208:211], v[8:11], v[0:3], v[192:195]\n\t"
    "ds_read_b64_tr_b16 v[72:73], v247 offset:0\n\t"
    "v_exp_f32 v224, v224\n\t"
    "v_exp_f32 v225, v225\n\t"
    "v_mfma_f32_16x16x32_bf16 v[216:219], v[8:11], v[4:7], v[196:199]\n\t"
    "ds_read_b64_tr_b16 v[74:75], v247 offset:4096\n\t"
    "v_add_f32 v250, v250, v224\n\t"
    "s_lshr_b32 s70, s77, 2\n\t"
    "s_cmp_eq_u32 %[bump], 1073741824\n\t"
    "s_cselect_b32 s65, 0, 32\n\t"
    "s_cselect_b32 s70, s70, 0\n\t"
    "v_and_b32 v206, 0x100, v252\n\t"
    "v_mul_u32_u24 v206, s65, v206\n\t"
    "s_or_b32 s65, s65, 0xd0\n\t"
    "v_and_b32 v205, s65, v252\n\t"
    "v_or3_b32 v205, v205, v206, s70\n\t"
    "v_mfma_f32_16x16x32_bf16 v[212:215], v[12:15], v[0:3], v[192:195]\n\t"
    "ds_read_b128 v[76:79], v240 offset:54272\n\t"
    "v_exp_f32 v226, v226\n\t"
    "v_mfma_f32_16x16x32_bf16 v[220:223], v[12:15], v[4:7], v[196:199]\n\t"
    "v_add_f32 v250, v250, v225\n\t"
    "s_waitcnt lgkmcnt(9)\n\t"
    "v_mfma_f32_16x16x32_bf16 v[128:131], v[24:27], v[104:107], v[128:131]\n\t"
    "ds_read_b64_tr_b16 v[88:89], v244 offset:1024\n\t"
    "v_cvt_pk_bf16_f32 v120, v224, v225\n\t"
    "v_exp_f32 v227, v227\n\t"
    "v_mfma_f32_16x16x32_bf16 v[136:139], v[24:27], v[108:111], v[136:139]\n\t"
    "ds_read_b64_tr_b16 v[90:91], v244 offset:5120\n\t"
    "v_add_f32 v250, v250, v226\n\t"
    "v_mfma_f32_16x16x32_bf16 v[208:211], v[28:31], v[16:19], v[208:211]\n\t"
    "ds_read_b128 v[92:95], v241 offset:50176\n\t"
    "v_exp_f32 v228, v228\n\t"
    "v_mfma_f32_16x16x32_bf16 v[216:219], v[28:31], v[20:23], v[216:219]\n\t"
    "v_add_f32 v250, v250, v227\n\t"
    "s_waitcnt lgkmcnt(9)\n\t"
    "v_mfma_f32_16x16x32_bf16 v[132:135], v[40:43], v[104:107], v[132:135]\n\t"
    "ds_read_b64_tr_b16 v[8:9], v245 offset:1024\n\t"
    "v_cvt_pk_bf16_f32 v121, v226, v227\n\t"
    "v_exp_f32 v229, v229\n\t"
    "v_mfma_f32_16x16x32_bf16 v[140:143], v[40:43], v[108:111], v[140:143]\n\t"
    "ds_read_b64_tr_b16 v[10:11], v245 offset:5120\n\t"
    "v_add_f32 v250, v250, v228\n\t"
    "v_mfma_f32_16x16x32_bf16 v[212:215], v[44:47], v[16:19], v[212:215]\n\t"
    "ds_read_b128 v[12:15], v241 offset:54272\n\t"
    "v_exp_f32 v230, v230\n\t"
    "v_mfma_f32_16x16x32_bf16 v[220:223], v[44:47], v[20:23], v[220:223]\n\t"
    "s_add_u32 m0, s61, 1024\n\t"
    "s_add_u32 s63, s76, 0\n\t"
    "buffer_load_dwordx4 v255, s[36:39], s63 offen lds\n\t"
    "v_add_f32 v250, v250, v229\n\t"
    "s_waitcnt lgkmcnt(9)\n\t"
    "v_mfma_f32_16x16x32_bf16 v[144:147], v[56:59], v[104:107], v[144:147]\n\t"
    "ds_read_b64_tr_b16 v[24:25], v246 offset:1024\n\t"
    "v_cvt_pk_bf16_f32 v122, v228, v229\n\t"
    "v_exp_f32 v231, v231\n\t"
    "v_mfma_f32_16x16x32_bf16 v[152:155], v[56:59], v[108:111], v[152:155]\n\t"
    "ds_read_b64_tr_b16 v[26:27], v246 offset:5120\n\t"
    "v_add_f32 v250, v250, v230\n\t"
    "v_mfma_f32_16x16x32_bf16 v[208:211], v[60:63], v[32:35], v[208:211]\n\t"
    "ds_read_b64_tr_b16 v[28:29], v247 offset:1024\n\t"
    "s_setprio 2\n\t"
    "v_exp_f32 v232, v232\n\t"
    "v_mfma_f32_16x16x32_bf16 v[216:219], v[60:63], v[36:39], v[216:219]\n\t"
    "ds_read_b64_tr_b16 v[30:31], v247 offset:5120\n\t"
    "v_add_f32 v250, v250, v231\n\t"
    "s_waitcnt lgkmcnt(10)\n\t"
    "v_mfma_f32_16x16x32_bf16 v[148:151], v[72:75], v[104:107], v[148:151]\n\t"
    "ds_read_b128 v[40:43], v240 offset:57344\n\t"
    "v_cvt_pk_bf16_f32 v123, v230, v231\n\t"
    "v_exp_f32 v233, v233\n\t"
    "v_mfma_f32_16x16x32_bf16 v[156:159], v[72:75], v[108:111], v[156:159]\n\t"
    "v_add_f32 v251, v251, v232\n\t"
    "v_mov_b32 v206, s72\n\t"
    "v_mov_b32 v207, s73\n\t"
    "v_mfma_f32_16x16x32_bf16 v[212:215], v[76:79], v[32:35], v[212:215]\n\t"
    "ds_read_b128 v[44:47], v240 offset:61440\n\t"
    "v_exp_f32 v234, v234\n\t"
    "v_mfma_f32_16x16x32_bf16 v[220:223], v[76:79], v[36:39], v[220:223]\n\t"
    "v_add_f32 v251, v251, v233\n\t"
    "s_waitcnt lgkmcnt(9)\n\t"
    "v_mfma_f32_16x16x32_bf16 v[160:163], v[88:91], v[104:107], v[160:163]\n\t"
    "ds_read_b64_tr_b16 v[56:57], v244 offset:8192\n\t"
    "v_cvt_pk_bf16_f32 v124, v232, v233\n\t"
    "v_exp_f32 v235, v235\n\t"
    "v_mfma_f32_16x16x32_bf16 v[168:171], v[88:91], v[108:111], v[168:171]\n\t"
    "ds_read_b64_tr_b16 v[58:59], v244 offset:12288\n\t"
    "v_add_f32 v251, v251, v234\n\t"
    "v_mfma_f32_16x16x32_bf16 v[208:211], v[92:95], v[48:51], v[208:211]\n\t"
    "ds_read_b128 v[60:63], v241 offset:57344\n\t"
    "v_exp_f32 v236, v236\n\t"
    "v_mfma_f32_16x16x32_bf16 v[216:219], v[92:95], v[52:55], v[216:219]\n\t"
    "s_add_u32 m0, s61, 16384\n\t"
    "s_add_u32 s63, s75, 262144\n\t"
    "buffer_load_dwordx4 v255, s[36:39], s63 offen lds\n\t"
    "v_add_f32 v251, v251, v235\n\t"
    "s_waitcnt lgkmcnt(9)\n\t"
    "v_mfma_f32_16x16x32_bf16 v[164:167], v[8:11], v[104:107], v[164:167]\n\t"
    "ds_read_b64_tr_b16 v[72:73], v245 offset:8192\n\t"
    "v_cvt_pk_bf16_f32 v125, v234, v235\n\t"
    "v_exp_f32 v237, v237\n\t"
    "v_mfma_f32_16x16x32_bf16 v[172:175], v[8:11], v[108:111], v[172:175]\n\t"
    "ds_read_b64_tr_b16 v[74:75], v245 offset:12288\n\t"
    "v_add_f32 v251, v251, v236\n\t"
    "v_mfma_f32_16x16x32_bf16 v[212:215], v[12:15], v[48:51], v[212:215]\n\t"
    "ds_read_b128 v[76:79], v241 offset:61440\n\t"
    "v_exp_f32 v238, v238\n\t"
    "v_mfma_f32_16x16x32_bf16 v[220:223], v[12:15], v[52:55], v[220:223]\n\t"
    "v_add_f32 v251, v251, v237\n\t"
    "s_waitcnt lgkmcnt(8)\n\t"
    "v_mfma_f32_16x16x32_bf16 v[176:179], v[24:27], v[104:107], v[176:179]\n\t"
    "ds_read_b64_tr_b16 v[88:89], v246 offset:8192\n\t"
    "v_cvt_pk_bf16_f32 v126, v236, v237\n\t"
    "v_exp_f32 v239, v239\n\t"
    "v_mfma_f32_16x16x32_bf16 v[184:187], v[24:27], v[108:111], v[184:187]\n\t"
    "ds_read_b64_tr_b16 v[90:91], v246 offset:12288\n\t"
    "v_add_f32 v251, v251, v238\n\t"
    "v_mfma_f32_16x16x32_bf16 v[180:183], v[28:31], v[104:107], v[180:183]\n\t"
    "ds_read_b128 v[92:95], v240 offset:58368\n\t"
    "v_add_f32 v251, v251, v239\n\t"
    "v_mfma_f32_16x16x32_bf16 v[188:191], v[28:31], v[108:111], v[188:191]\n\t"
    "v_cvt_pk_bf16_f32 v127, v238, v239\n\t"
    "s_waitcnt lgkmcnt(9)\n\t"
    "v_mfma_f32_16x16x32_bf16 v[224:227], v[40:43], v[0:3], v[192:195]\n\t"
    "ds_read_b64_tr_b16 v[8:9], v247 offset:8192\n\t"
    "s_setprio 1\n\t"
    "v_exp_f32 v208, v208\n\t"
    "v_mfma_f32_16x16x32_bf16 v[232:235], v[40:43], v[4:7], v[196:199]\n\t"
    "ds_read_b64_tr_b16 v[10:11], v247 offset:12288\n\t"
    "v_exp_f32 v209, v209\n\t"
    "v_add_f32 v250, v250, v208\n\t"
    "v_mfma_f32_16x16x32_bf16 v[228:231], v[44:47], v[0:3], v[192:195]\n\t"
    "ds_read_b128 v[12:15], v240 offset:62464\n\t"
    "v_exp_f32 v210, v210\n\t"
    "v_mfma_f32_16x16x32_bf16 v[236:239], v[44:47], v[4:7], v[196:199]\n\t"
    "s_add_u32 m0, s61, 17408\n\t"
    "s_add_u32 s63, s76, 262144\n\t"
    "buffer_load_dwordx4 v255, s[36:39], s63 offen lds\n\t"
    "v_add_f32 v250, v250, v209\n\t"
    "s_waitcnt lgkmcnt(9)\n\t"
    "v_mfma_f32_16x16x32_bf16 v[128:131], v[56:59], v[120:123], v[128:131]\n\t"
    "ds_read_b64_tr_b16 v[24:25], v244 offset:9216\n\t"
    "v_cvt_pk_bf16_f32 v104, v208, v209\n\t"
    "v_exp_f32 v211, v211\n\t"
    "v_mfma_f32_16x16x32_bf16 v[136:139], v[56:59], v[124:127], v[136:139]\n\t"
    "ds_read_b64_tr_b16 v[26:27], v244 offset:13312\n\t"
    "v_add_f32 v250, v250, v210\n\t"
    "v_mfma_f32_16x16x32_bf16 v[224:227], v[60:63], v[16:19], v[224:227]\n\t"
    "ds_read_b128 v[28:31], v241 offset:58368\n\t"
    "v_exp_f32 v212, v212\n\t"
    "v_mfma_f32_16x16x32_bf16 v[232:235], v[60:63], v[20:23], v[232:235]\n\t"
    "v_add_f32 v250, v250, v211\n\t"
    "s_waitcnt lgkmcnt(9)\n\t"
    "v_mfma_f32_16x16x32_bf16 v[132:135], v[72:75], v[120:123], v[132:135]\n\t"
    "ds_read_b64_tr_b16 v[40:41], v245 offset:9216\n\t"
    "v_cvt_pk_bf16_f32 v105, v210, v211\n\t"
    "v_exp_f32 v213, v213\n\t"
    "v_mfma_f32_16x16x32_bf16 v[140:143], v[72:75], v[124:127], v[140:143]\n\t"
    "ds_read_b64_tr_b16 v[42:43], v245 offset:13312\n\t"
    "v_add_f32 v250, v250, v212\n\t"
    "v_mfma_f32_16x16x32_bf16 v[228:231], v[76:79], v[16:19], v[228:231]\n\t"
    "ds_read_b128 v[44:47], v241 offset:62464\n\t"
    "s_setprio 0\n\t"
    "v_mfma_f32_16x16x32_bf16 v[236:239], v[76:79], v[20:23], v[236:239]\n\t"
    "v_exp_f32 v214, v214\n\t"
    "v_add_f32 v250, v250, v213\n\t"
    "s_waitcnt lgkmcnt(9)\n\t"
    "v_mfma_f32_16x16x32_bf16 v[144:147], v[88:91], v[120:123], v[144:147]\n\t"
    "ds_read_b64_tr_b16 v[56:57], v246 offset:9216\n\t"
    "v_cvt_pk_bf16_f32 v106, v212, v213\n\t"
    "PAD8_%=: s_cbranch_execz PADRET_%=\n\t"
    "v_exp_f32 v215, v215\n\t"
    "v_mfma_f32_16x16x32_bf16 v[152:155], v[88:91], v[124:127], v[152:155]\n\t"
    "ds_read_b64_tr_b16 v[58:59], v246 offset:13312\n\t"
    "v_add_f32 v250, v250, v214\n\t"
    "s_add_u32 s63, %[pollfl], 0\n\t"
    "buffer_load_dwordx4 v[200:203], v205, s[56:59], s63 offen sc1\n\t"
    "v_mfma_f32_16x16x32_bf16 v[224:227], v[92:95], v[32:35], v[224:227]\n\t"
    "ds_read_b64_tr_b16 v[60:61], v247 offset:9216\n\t"
    "v_exp_f32 v216, v216\n\t"
    "v_mfma_f32_16x16x32_bf16 v[232:235], v[92:95], v[36:39], v[232:235]\n\t"
    "ds_read_b64_tr_b16 v[62:63], v247 offset:13312\n\t"
    "v_add_f32 v250, v250, v215\n\t"
    "s_waitcnt lgkmcnt(10)\n\t"
    "v_mfma_f32_16x16x32_bf16 v[148:151], v[8:11], v[120:123], v[148:151]\n\t"
    "v_cvt_pk_bf16_f32 v107, v214, v215\n\t"
    "v_exp_f32 v217, v217\n\t"
    "v_mfma_f32_16x16x32_bf16 v[156:159], v[8:11], v[124:127], v[156:159]\n\t"
    "v_add_f32 v251, v251, v216\n\t"
    "v_mfma_f32_16x16x32_bf16 v[228:231], v[12:15], v[32:35], v[228:231]\n\t"
    "v_exp_f32 v218, v218\n\t"
    "v_add_f32 v251, v251, v217\n\t"
    "v_mfma_f32_16x16x32_bf16 v[236:239], v[12:15], v[36:39], v[236:239]\n\t"
    "s_waitcnt vmcnt(8) lgkmcnt(0)\n\t"
    "s_barrier\n\t"
    "s_setprio 3\n\t"
    "ds_read_b128 v[72:75], v240 offset:0\n\t"
    "ds_read_b128 v[76:79], v240 offset:4096\n\t"
    "v_mfma_f32_16x16x32_bf16 v[160:163], v[24:27], v[120:123], v[160:163]\n\t"
    "ds_read_b64_tr_b16 v[88:89], v244 offset:16384\n\t"
    "v_cvt_pk_bf16_f32 v108, v216, v217\n\t"
    "v_exp_f32 v219, v219\n\t"
    "v_mfma_f32_16x16x32_bf16 v[168:171], v[24:27], v[124:127], v[168:171]\n\t"
    "ds_read_b64_tr_b16 v[90:91], v244 offset:20480\n\t"
    "v_add_f32 v251, v251, v218\n\t"
    "v_mfma_f32_16x16x32_bf16 v[224:227], v[28:31], v[48:51], v[224:227]\n\t"
    "ds_read_b128 v[92:95], v241 offset:0\n\t"
    "v_exp_f32 v220, v220\n\t"
    "v_mfma_f32_16x16x32_bf16 v[232:235], v[28:31], v[52:55], v[232:235]\n\t"
    "v_add_f32 v251, v251, v219\n\t"
    "v_mfma_f32_16x16x32_bf16 v[164:167], v[40:43], v[120:123], v[164:167]\n\t"
    "ds_read_b64_tr_b16 v[8:9], v245 offset:16384\n\t"
    "v_cvt_pk_bf16_f32 v109, v218, v219\n\t"
    "v_exp_f32 v221, v221\n\t"
    "v_mfma_f32_16x16x32_bf16 v[172:175], v[40:43], v[124:127], v[172:175]\n\t"
    "ds_read_b64_tr_b16 v[10:11], v245 offset:20480\n\t"
    "v_add_f32 v251, v251, v220\n\t"
    "v_mfma_f32_16x16x32_bf16 v[228:231], v[44:47], v[48:51], v[228:231]\n\t"
    "ds_read_b128 v[12:15], v241 offset:4096\n\t"
    "v_exp_f32 v222, v222\n\t"
    "v_mfma_f32_16x16x32_bf16 v[236:239], v[44:47], v[52:55], v[236:239]\n\t"
    "v_add_f32 v251, v251, v221\n\t"
    "v_mfma_f32_16x16x32_bf16 v[176:179], v[56:59], v[120:123], v[176:179]\n\t"
    "ds_read_b64_tr_b16 v[24:25], v246 offset:16384\n\t"
    "v_cvt_pk_bf16_f32 v110, v220, v221\n\t"
    "v_exp_f32 v223, v223\n\t"
    "v_mfma_f32_16x16x32_bf16 v[184:187], v[56:59], v[124:127], v[184:187]\n\t"
    "ds_read_b64_tr_b16 v[26:27], v246 offset:20480\n\t"
    "v_add_f32 v251, v251, v222\n\t"
    "v_mfma_f32_16x16x32_bf16 v[180:183], v[60:63], v[120:123], v[180:183]\n\t"
    "ds_read_b128 v[28:31], v240 offset:1024\n\t"
    "v_add_f32 v251, v251, v223\n\t"
    "v_mfma_f32_16x16x32_bf16 v[188:191], v[60:63], v[124:127], v[188:191]\n\t"
    "s_add_u32 m0, s61, 131072\n\t"
    "s_add_u32 s63, s75, 65536\n\t"
    "buffer_load_dwordx4 v255, s[36:39], s63 offen lds\n\t"
    "v_cvt_pk_bf16_f32 v111, v222, v223\n\t"
    "s_waitcnt lgkmcnt(9)\n\t"
    "v_mfma_f32_16x16x32_bf16 v[208:211], v[72:75], v[0:3], v[192:195]\n\t"
    "ds_read_b64_tr_b16 v[40:41], v247 offset:16384\n\t"
    "v_exp_f32 v224, v224\n\t"
    "v_exp_f32 v225, v225\n\t"
    "v_mfma_f32_16x16x32_bf16 v[216:219], v[72:75], v[4:7], v[196:199]\n\t"
    "ds_read_b64_tr_b16 v[42:43], v247 offset:20480\n\t"
    "v_add_f32 v250, v250, v224\n\t"
    "v_mfma_f32_16x16x32_bf16 v[212:215], v[76:79], v[0:3], v[192:195]\n\t"
    "ds_read_b128 v[44:47], v240 offset:5120\n\t"
    "v_exp_f32 v226, v226\n\t"
    "v_mfma_f32_16x16x32_bf16 v[220:223], v[76:79], v[4:7], v[196:199]\n\t"
    "v_add_f32 v250, v250, v225\n\t"
    "s_waitcnt lgkmcnt(9)\n\t"
    "v_mfma_f32_16x16x32_bf16 v[128:131], v[88:91], v[104:107], v[128:131]\n\t"
    "ds_read_b64_tr_b16 v[56:57], v244 offset:17408\n\t"
    "v_cvt_pk_bf16_f32 v120, v224, v225\n\t"
    "v_exp_f32 v227, v227\n\t"
    "v_mfma_f32_16x16x32_bf16 v[136:139], v[88:91], v[108:111], v[136:139]\n\t"
    "ds_read_b64_tr_b16 v[58:59], v244 offset:21504\n\t"
    "v_add_f32 v250, v250, v226\n\t"
    "v_mfma_f32_16x16x32_bf16 v[208:211], v[92:95], v[16:19], v[208:211]\n\t"
    "ds_read_b128 v[60:63], v241 offset:1024\n\t"
    "v_exp_f32 v228, v228\n\t"
    "v_mfma_f32_16x16x32_bf16 v[216:219], v[92:95], v[20:23], v[216:219]\n\t"
    "v_add_f32 v250, v250, v227\n\t"
    "s_waitcnt lgkmcnt(9)\n\t"
    "v_mfma_f32_16x16x32_bf16 v[132:135], v[8:11], v[104:107], v[132:135]\n\t"
    "ds_read_b64_tr_b16 v[72:73], v245 offset:17408\n\t"
    "v_cvt_pk_bf16_f32 v121, v226, v227\n\t"
    "v_exp_f32 v229, v229\n\t"
    "v_mfma_f32_16x16x32_bf16 v[140:143], v[8:11], v[108:111], v[140:143]\n\t"
    "ds_read_b64_tr_b16 v[74:75], v245 offset:21504\n\t"
    "v_add_f32 v250, v250, v228\n\t"
    "v_mfma_f32_16x16x32_bf16 v[212:215], v[12:15], v[16:19], v[212:215]\n\t"
    "ds_read_b128 v[76:79], v241 offset:5120\n\t"
    "v_exp_f32 v230, v230\n\t"
    "v_mfma_f32_16x16x32_bf16 v[220:223], v[12:15], v[20:23], v[220:223]\n\t"
    "s_add_u32 m0, s61, 132096\n\t"
    "s_add_u32 s63, s76, 65536\n\t"
    "buffer_load_dwordx4 v255, s[36:39], s63 offen lds\n\t"
    "v_add_f32 v250, v250, v229\n\t"
    "s_waitcnt lgkmcnt(9)\n\t"
    "v_mfma_f32_16x16x32_bf16 v[144:147], v[24:27], v[104:107], v[144:147]\n\t"
    "ds_read_b64_tr_b16 v[88:89], v246 offset:17408\n\t"
    "v_cvt_pk_bf16_f32 v122, v228, v229\n\t"
    "v_exp_f32 v231, v231\n\t"
    "v_mfma_f32_16x16x32_bf16 v[152:155], v[24:27], v[108:111], v[152:155]\n\t"
    "ds_read_b64_tr_b16 v[90:91], v246 offset:21504\n\t"
    "v_add_f32 v250, v250, v230\n\t"
    "v_mfma_f32_16x16x32_bf16 v[208:211], v[28:31], v[32:35], v[208:211]\n\t"
    "ds_read_b64_tr_b16 v[92:93], v247 offset:17408\n\t"
    "s_setprio 2\n\t"
    "v_exp_f32 v232, v232\n\t"
    "v_mfma_f32_16x16x32_bf16 v[216:219], v[28:31], v[36:39], v[216:219]\n\t"
    "ds_read_b64_tr_b16 v[94:95], v247 offset:21504\n\t"
    "v_add_f32 v250, v250, v231\n\t"
    "s_waitcnt lgkmcnt(10)\n\t"
    "v_mfma_f32_16x16x32_bf16 v[148:151], v[40:43], v[104:107], v[148:151]\n\t"
    "ds_read_b128 v[8:11], v240 offset:8192\n\t"
    "v_cvt_pk_bf16_f32 v123, v230, v231\n\t"
    "v_mfma_f32_16x16x32_bf16 v[156:159], v[40:43], v[108:111], v[156:159]\n\t"
    "v_exp_f32 v233, v233\n\t"
    "v_add_f32 v251, v251, v232\n\t"
    "v_mfma_f32_16x16x32_bf16 v[212:215], v[44:47], v[32:35], v[212:215]\n\t"
    "ds_read_b128 v[12:15], v240 offset:12288\n\t"
    "v_exp_f32 v234, v234\n\t"
    "v_mfma_f32_16x16x32_bf16 v[220:223], v[44:47], v[36:39], v[220:223]\n\t"
    "v_add_f32 v251, v251, v233\n\t"
    "s_waitcnt lgkmcnt(9)\n\t"
    "v_mfma_f32_16x16x32_bf16 v[160:163], v[56:59], v[104:107], v[160:163]\n\t"
    "ds_read_b64_tr_b16 v[24:25], v244 offset:24576\n\t"
    "v_cvt_pk_bf16_f32 v124, v232, v233\n\t"
    "v_exp_f32 v235, v235\n\t"
    "v_add_f32 v251, v251, v234\n\t"
    "v_mfma_f32_16x16x32_bf16 v[168:171], v[56:59], v[108:111], v[168:171]\n\t"
    "ds_read_b64_tr_b16 v[26:27], v244 offset:28672\n\t"
    "s_waitcnt vmcnt(2)\n\t"
    "v_xor_b32 v200, v200, v206\n\t"
    "v_xor_b32 v201, v201, v207\n\t"
    "v_mfma_f32_16x16x32_bf16 v[208:211], v[60:63], v[48:51], v[208:211]\n\t"
    "ds_read_b128 v[28:31], v241 offset:8192\n\t"
    "v_exp_f32 v236, v236\n\t"
    "v_add_f32 v251, v251, v235\n\t"
    "v_mfma_f32_16x16x32_bf16 v[216:219], v[60:63], v[52:55], v[216:219]\n\t"
    "s_add_u32 m0, s61, 147456\n\t"
    "s_add_u32 s63, s75, 327680\n\t"
    "buffer_load_dwordx4 v255, s[36:39], s63 offen lds\n\t"
    "v_xor_b32 v202, v202, v206\n\t"
    "v_xor_b32 v203, v203, v207\n\t"
    "s_waitcnt lgkmcnt(9)\n\t"
    "v_mfma_f32_16x16x32_bf16 v[164:167], v[72:75], v[104:107], v[164:167]\n\t"
    "ds_read_b64_tr_b16 v[40:41], v245 offset:24576\n\t"
    "v_cvt_pk_bf16_f32 v125, v234, v235\n\t"
    "v_exp_f32 v237, v237\n\t"
    "v_add_f32 v251, v251, v236\n\t"
    "v_mfma_f32_16x16x32_bf16 v[172:175], v[72:75], v[108:111], v[172:175]\n\t"
    "ds_read_b64_tr_b16 v[42:43], v245 offset:28672\n\t"
    "v_or3_b32 v200, v200, v201, v202\n\t"
    "v_or_b32 v200, v200, v203\n\t"
    "v_mfma_f32_16x16x32_bf16 v[212:215], v[76:79], v[48:51], v[212:215]\n\t"
    "ds_read_b128 v[44:47], v241 offset:12288\n\t"
    "v_exp_f32 v238, v238\n\t"
    "v_mfma_f32_16x16x32_bf16 v[220:223], v[76:79], v[52:55], v[220:223]\n\t"
    "v_add_f32 v251, v251, v237\n\t"
    "v_cmp_ne_u32 vcc, 0, v200\n\t"
    "s_nop 1\n\t"
    "s_cmp_eq_u64 vcc, 0\n\t"
    "s_cselect_b32 s42, %[onrec], 0\n\t"
    "s_cselect_b32 s71, 1, 0\n\t"
    "s_waitcnt lgkmcnt(8)\n\t"
    "v_mfma_f32_16x16x32_bf16 v[176:179], v[88:91], v[104:107], v[176:179]\n\t"
    "ds_read_b64_tr_b16 v[56:57], v246 offset:24576\n\t"
    "v_cvt_pk_bf16_f32 v126, v236, v237\n\t"
    "v_exp_f32 v239, v239\n\t"
    "v_mfma_f32_16x16x32_bf16 v[184:187], v[88:91], v[108:111], v[184:187]\n\t"
    "ds_read_b64_tr_b16 v[58:59], v246 offset:28672\n\t"
    "v_add_f32 v251, v251, v238\n\t"
    "v_mfma_f32_16x16x32_bf16 v[180:183], v[92:95], v[104:107], v[180:183]\n\t"
    "ds_read_b128 v[60:63], v240 offset:9216\n\t"
    "v_add_f32 v251, v251, v239\n\t"
    "v_mfma_f32_16x16x32_bf16 v[188:191], v[92:95], v[108:111], v[188:191]\n\t"
    "v_cvt_pk_bf16_f32 v127, v238, v239\n\t"
    "s_waitcnt lgkmcnt(9)\n\t"
    "v_mfma_f32_16x16x32_bf16 v[224:227], v[8:11], v[0:3], v[192:195]\n\t"
    "ds_read_b64_tr_b16 v[72:73], v247 offset:24576\n\t"
    "s_setprio 1\n\t"
    "v_exp_f32 v208, v208\n\t"
    "v_exp_f32 v209, v209\n\t"
    "v_mfma_f32_16x16x32_bf16 v[232:235], v[8:11], v[4:7], v[196:199]\n\t"
    "ds_read_b64_tr_b16 v[74:75], v247 offset:28672\n\t"
    "v_add_f32 v250, v250, v208\n\t"
    "s_add_u32 s63, s74, 0\n\t"
    "buffer_load_dwordx4 v[64:67], v252, s[40:43], s63 offen sc1\n\t"
    "v_mfma_f32_16x16x32_bf16 v[228:231], v[12:15], v[0:3], v[192:195]\n\t"
    "ds_read_b128 v[76:79], v240 offset:13312\n\t"
    "v_exp_f32 v210, v210\n\t"
    "v_mfma_f32_16x16x32_bf16 v[236:239], v[12:15], v[4:7], v[196:199]\n\t"
    "s_add_u32 m0, s61, 148480\n\t"
    "s_add_u32 s63, s76, 327680\n\t"
    "buffer_load_dwordx4 v255, s[36:39], s63 offen lds\n\t"
    "v_add_f32 v250, v250, v209\n\t"
    "s_add_u32 s63, s74, 0\n\t"
    "buffer_load_dwordx4 v[68:71], v252, s[40:43], s63 offen offset:1024 sc1\n\t"
    "s_waitcnt lgkmcnt(9)\n\t"
    "v_mfma_f32_16x16x32_bf16 v[128:131], v[24:27], v[120:123], v[128:131]\n\t"
    "ds_read_b64_tr_b16 v[88:89], v244 offset:25600\n\t"
    "v_cvt_pk_bf16_f32 v104, v208, v209\n\t"
    "v_exp_f32 v211, v211\n\t"
    "v_mfma_f32_16x16x32_bf16 v[136:139], v[24:27], v[124:127], v[136:139]\n\t"
    "ds_read_b64_tr_b16 v[90:91], v244 offset:29696\n\t"
    "v_add_f32 v250, v250, v210\n\t"
    "s_add_u32 s63, s74, 0\n\t"
    "buffer_load_dwordx4 v[80:83], v252, s[40:43], s63 offen offset:2048 sc1\n\t"
    "v_mfma_f32_16x16x32_bf16 v[224:227], v[28:31], v[16:19], v[224:227]\n\t"
    "ds_read_b128 v[92:95], v241 offset:9216\n\t"
    "v_exp_f32 v212, v212\n\t"
    "v_mfma_f32_16x16x32_bf16 v[232:235], v[28:31], v[20:23], v[232:235]\n\t"
    "v_add_f32 v250, v250, v211\n\t"
    "s_add_u32 s63, s74, 0\n\t"
    "buffer_load_dwordx4 v[84:87], v252, s[40:43], s63 offen offset:3072 sc1\n\t"
    "s_waitcnt lgkmcnt(9)\n\t"
    "v_mfma_f32_16x16x32_bf16 v[132:135], v[40:43], v[120:123], v[132:135]\n\t"
    "ds_read_b64_tr_b16 v[8:9], v245 offset:25600\n\t"
    "v_cvt_pk_bf16_f32 v105, v210, v211\n\t"
    "v_exp_f32 v213, v213\n\t"
    "v_mfma_f32_16x16x32_bf16 v[140:143], v[40:43], v[124:127], v[140:143]\n\t"
    "ds_read_b64_tr_b16 v[10:11], v245 offset:29696\n\t"
    "v_add_f32 v250, v250, v212\n\t"
    "s_add_u32 s63, s74, 4096\n\t"
    "buffer_load_dwordx4 v[96:99], v252, s[40:43], s63 offen sc1\n\t"
    "v_mfma_f32_16x16x32_bf16 v[228:231], v[44:47], v[16:19], v[228:231]\n\t"
    "ds_read_b128 v[12:15], v241 offset:13312\n\t"
    "s_setprio 0\n\t"
    "v_exp_f32 v214, v214\n\t"
    "v_mfma_f32_16x16x32_bf16 v[236:239], v[44:47], v[20:23], v[236:239]\n\t"
    "v_add_f32 v250, v250, v213\n\t"
    "s_add_u32 s63, s74, 4096\n\t"
    "buffer_load_dwordx4 v[100:103], v252, s[40:43], s63 offen offset:1024 sc1\n\t"
    "s_waitcnt lgkmcnt(9)\n\t"
    "v_mfma_f32_16x16x32_bf16 v[144:147], v[56:59], v[120:123], v[144:147]\n\t"
    "ds_read_b64_tr_b16 v[24:25], v246 offset:25600\n\t"
    "v_cvt_pk_bf16_f32 v106, v212, v213\n\t"
    "v_exp_f32 v215, v215\n\t"
    "v_mfma_f32_16x16x32_bf16 v[152:155], v[56:59], v[124:127], v[152:155]\n\t"
    "ds_read_b64_tr_b16 v[26:27], v246 offset:29696\n\t"
    "v_add_f32 v250, v250, v214\n\t"
    "s_add_u32 s63, s74, 4096\n\t"
    "buffer_load_dwordx4 v[112:115], v252, s[40:43], s63 offen offset:2048 sc1\n\t"
    "v_mfma_f32_16x16x32_bf16 v[224:227], v[60:63], v[32:35], v[224:227]\n\t"
    "ds_read_b64_tr_b16 v[28:29], v247 offset:25600\n\t"
    "v_exp_f32 v216, v216\n\t"
    "v_add_f32 v250, v250, v215\n\t"
    "v_mfma_f32_16x16x32_bf16 v[232:235], v[60:63], v[36:39], v[232:235]\n\t"
    "ds_read_b64_tr_b16 v[30:31], v247 offset:29696\n\t"
    "s_add_u32 s63, s74, 4096\n\t"
    "buffer_load_dwordx4 v[116:119], v252, s[40:43], s63 offen offset:3072 sc1\n\t"
    "s_waitcnt lgkmcnt(10)\n\t"
    "v_mfma_f32_16x16x32_bf16 v[148:151], v[72:75], v[120:123], v[148:151]\n\t"
    "v_cvt_pk_bf16_f32 v107, v214, v215\n\t"
    "v_exp_f32 v217, v217\n\t"
    "v_mfma_f32_16x16x32_bf16 v[156:159], v[72:75], v[124:127], v[156:159]\n\t"
    "v_add_f32 v251, v251, v216\n\t"
    "v_mfma_f32_16x16x32_bf16 v[228:231], v[76:79], v[32:35], v[228:231]\n\t"
    "v_exp_f32 v218, v218\n\t"
    "v_add_f32 v251, v251, v217\n\t"
    "v_mfma_f32_16x16x32_bf16 v[236:239], v[76:79], v[36:39], v[236:239]\n\t"
    "s_waitcnt lgkmcnt(0)\n\t"
    "s_barrier\n\t"
    "s_setprio 3\n\t"
    "ds_read_b64_tr_b16 v[40:41], v244 offset:49152\n\t"
    "ds_read_b64_tr_b16 v[42:43], v244 offset:53248\n\t"
    "ds_read_b64_tr_b16 v[44:45], v245 offset:49152\n\t"
    "ds_read_b64_tr_b16 v[46:47], v245 offset:53248\n\t"
    "v_mfma_f32_16x16x32_bf16 v[160:163], v[88:91], v[120:123], v[160:163]\n\t"
    "ds_read_b64_tr_b16 v[56:57], v246 offset:49152\n\t"
    "v_cvt_pk_bf16_f32 v108, v216, v217\n\t"
    "v_exp_f32 v219, v219\n\t"
    "v_mfma_f32_16x16x32_bf16 v[168:171], v[88:91], v[124:127], v[168:171]\n\t"
    "ds_read_b64_tr_b16 v[58:59], v246 offset:53248\n\t"
    "s_add_u32 m0, s61, 32768\n\t"
    "s_add_u32 s63, s75, 1048576\n\t"
    "buffer_load_dwordx4 v255, s[36:39], s63 offen lds\n\t"
    "v_add_f32 v251, v251, v218\n\t"
    "v_mfma_f32_16x16x32_bf16 v[224:227], v[92:95], v[48:51], v[224:227]\n\t"
    "ds_read_b64_tr_b16 v[60:61], v247 offset:49152\n\t"
    "v_exp_f32 v220, v220\n\t"
    "v_mfma_f32_16x16x32_bf16 v[232:235], v[92:95], v[52:55], v[232:235]\n\t"
    "ds_read_b64_tr_b16 v[62:63], v247 offset:53248\n\t"
    "s_add_u32 m0, s61, 33792\n\t"
    "s_add_u32 s63, s76, 1048576\n\t"
    "buffer_load_dwordx4 v255, s[36:39], s63 offen lds\n\t"
    "v_add_f32 v251, v251, v219\n\t"
    "v_mfma_f32_16x16x32_bf16 v[164:167], v[8:11], v[120:123], v[164:167]\n\t"
    "ds_read_b64_tr_b16 v[72:73], v244 offset:50176\n\t"
    "v_cvt_pk_bf16_f32 v109, v218, v219\n\t"
    "v_exp_f32 v221, v221\n\t"
    "v_mfma_f32_16x16x32_bf16 v[172:175], v[8:11], v[124:127], v[172:175]\n\t"
    "ds_read_b64_tr_b16 v[74:75], v244 offset:54272\n\t"
    "s_add_u32 m0, s61, 49152\n\t"
    "s_add_u32 s63, s75, 1310720\n\t"
    "buffer_load_dwordx4 v255, s[36:39], s63 offen lds\n\t"
    "v_add_f32 v251, v251, v220\n\t"
    "v_mfma_f32_16x16x32_bf16 v[228:231], v[12:15], v[48:51], v[228:231]\n\t"
    "ds_read_b64_tr_b16 v[76:77], v245 offset:50176\n\t"
    "v_exp_f32 v222, v222\n\t"
    "v_mfma_f32_16x16x32_bf16 v[236:239], v[12:15], v[52:55], v[236:239]\n\t"
    "ds_read_b64_tr_b16 v[78:79], v245 offset:54272\n\t"
    "s_add_u32 m0, s61, 50176\n\t"
    "s_add_u32 s63, s76, 1310720\n\t"
    "buffer_load_dwordx4 v255, s[36:39], s63 offen lds\n\t"
    "v_add_f32 v251, v251, v221\n\t"
    "v_mfma_f32_16x16x32_bf16 v[176:179], v[24:27], v[120:123], v[176:179]\n\t"
    "ds_read_b64_tr_b16 v[88:89], v246 offset:50176\n\t"
    "v_cvt_pk_bf16_f32 v110, v220, v221\n\t"
    "v_exp_f32 v223, v223\n\t"
    "v_mfma_f32_16x16x32_bf16 v[184:187], v[24:27], v[124:127], v[184:187]\n\t"
    "ds_read_b64_tr_b16 v[90:91], v246 offset:54272\n\t"
    "s_add_u32 m0, s61, 98304\n\t"
    "s_add_u32 s63, s75, 1114112\n\t"
    "buffer_load_dwordx4 v255, s[36:39], s63 offen lds\n\t"
    "v_add_f32 v251, v251, v222\n\t"
    "v_mfma_f32_16x16x32_bf16 v[180:183], v[28:31], v[120:123], v[180:183]\n\t"
    "ds_read_b64_tr_b16 v[92:93], v247 offset:50176\n\t"
    "v_add_f32 v251, v251, v223\n\t"
    "v_mfma_f32_16x16x32_bf16 v[188:191], v[28:31], v[124:127], v[188:191]\n\t"
    "ds_read_b64_tr_b16 v[94:95], v247 offset:54272\n\t"
    "s_add_u32 m0, s61, 99328\n\t"
    "s_add_u32 s63, s76, 1114112\n\t"
    "buffer_load_dwordx4 v255, s[36:39], s63 offen lds\n\t"
    "v_cvt_pk_bf16_f32 v111, v222, v223\n\t"
    "s_waitcnt lgkmcnt(12)\n\t"
    "v_mfma_f32_16x16x32_bf16 v[128:131], v[40:43], v[104:107], v[128:131]\n\t"
    "ds_read_b64_tr_b16 v[8:9], v246 offset:58368\n\t"
    "s_setprio 2\n\t"
    "v_exp_f32 v224, v224\n\t"
    "v_exp_f32 v225, v225\n\t"
    "v_add_f32 v250, v250, v224\n\t"
    "v_exp_f32 v226, v226\n\t"
    "v_add_f32 v250, v250, v225\n\t"
    "v_mbcnt_lo_u32_b32 v192, -1, 0\n\t"
    "v_mfma_f32_16x16x32_bf16 v[136:139], v[40:43], v[108:111], v[136:139]\n\t"
    "ds_read_b64_tr_b16 v[10:11], v246 offset:62464\n\t"
    "s_add_u32 m0, s61, 114688\n\t"
    "s_add_u32 s63, s75, 1376256\n\t"
    "buffer_load_dwordx4 v255, s[36:39], s63 offen lds\n\t"
    "v_mbcnt_hi_u32_b32 v192, -1, v192\n\t"
    "v_bfe_u32 v253, v192, 1, 1\n\t"
    "v_bfe_u32 v254, v192, 3, 1\n\t"
    "v_lshl_add_u32 v253, v254, 2, v253\n\t"
    "v_bfe_u32 v254, v192, 4, 1\n\t"
    "v_lshl_add_u32 v253, v254, 1, v253\n\t"
    "v_mfma_f32_16x16x32_bf16 v[132:135], v[44:47], v[104:107], v[132:135]\n\t"
    "ds_read_b64_tr_b16 v[12:13], v247 offset:58368\n\t"
    "v_cvt_pk_bf16_f32 v120, v224, v225\n\t"
    "v_exp_f32 v227, v227\n\t"
    "v_add_f32 v250, v250, v226\n\t"
    "v_exp_f32 v228, v228\n\t"
    "v_add_f32 v250, v250, v227\n\t"
    "s_and_b32 s65, s66, 4\n\t"
    "v_xor_b32 v253, s65, v253\n\t"
    "v_mfma_f32_16x16x32_bf16 v[140:143], v[44:47], v[108:111], v[140:143]\n\t"
    "ds_read_b64_tr_b16 v[14:15], v247 offset:62464\n\t"
    "s_add_u32 m0, s61, 115712\n\t"
    "s_add_u32 s63, s76, 1376256\n\t"
    "buffer_load_dwordx4 v255, s[36:39], s63 offen lds\n\t"
    "v_lshlrev_b32 v253, 4, v253\n\t"
    "v_bfe_u32 v254, v192, 2, 2\n\t"
    "v_lshlrev_b32 v254, 7, v254\n\t"
    "v_and_b32 v193, 1, v192\n\t"
    "v_lshl_add_u32 v254, v193, 3, v254\n\t"
    "s_waitcnt lgkmcnt(12)\n\t"
    "v_mfma_f32_16x16x32_bf16 v[144:147], v[56:59], v[104:107], v[144:147]\n\t"
    "ds_read_b64_tr_b16 v[24:25], v244 offset:57344\n\t"
    "v_cvt_pk_bf16_f32 v121, v226, v227\n\t"
    "v_exp_f32 v229, v229\n\t"
    "v_add_f32 v250, v250, v228\n\t"
    "v_exp_f32 v230, v230\n\t"
    "v_add_f32 v250, v250, v229\n\t"
    "v_bfe_u32 v193, v192, 5, 1\n\t"
    "v_mfma_f32_16x16x32_bf16 v[152:155], v[56:59], v[108:111], v[152:155]\n\t"
    "ds_read_b64_tr_b16 v[26:27], v244 offset:61440\n\t"
    "v_lshl_add_u32 v254, v193, 11, v254\n\t"
    "v_bfe_u32 v193, v192, 4, 1\n\t"
    "v_lshl_add_u32 v254, v193, 9, v254\n\t"
    "s_and_b32 s65, s66, 4\n\t"
    "s_lshl_b32 s65, s65, 12\n\t"
    "v_add_u32 v254, s65, v254\n\t"
    "v_xor_b32 v240, 0, v253\n\t"
    "s_mov_b32 s65, 0\n\t"
    "v_add3_u32 v240, v240, v254, s65\n\t"
    "v_mfma_f32_16x16x32_bf16 v[148:151], v[60:63], v[104:107], v[148:151]\n\t"
    "ds_read_b64_tr_b16 v[28:29], v245 offset:57344\n\t"
    "v_cvt_pk_bf16_f32 v122, v228, v229\n\t"
    "v_exp_f32 v231, v231\n\t"
    "v_add_f32 v250, v250, v230\n\t"
    "v_exp_f32 v232, v232\n\t"
    "v_add_f32 v250, v250, v231\n\t"
    "v_xor_b32 v241, 32, v253\n\t"
    "v_mfma_f32_16x16x32_bf16 v[156:159], v[60:63], v[108:111], v[156:159]\n\t"
    "ds_read_b64_tr_b16 v[30:31], v245 offset:61440\n\t"
    "s_mov_b32 s65, 0\n\t"
    "v_add3_u32 v241, v241, v254, s65\n\t"
    "v_xor_b32 v242, 64, v253\n\t"
    "s_mov_b32 s65, 0\n\t"
    "v_add3_u32 v242, v242, v254, s65\n\t"
    "v_xor_b32 v243, 96, v253\n\t"
    "s_mov_b32 s65, 0\n\t"
    "v_add3_u32 v243, v243, v254, s65\n\t"
    "s_waitcnt lgkmcnt(12)\n\t"
    "v_mfma_f32_16x16x32_bf16 v[160:163], v[72:75], v[104:107], v[160:163]\n\t"
    "ds_read_b64_tr_b16 v[40:41], v246 offset:57344\n\t"
    "s_setprio 1\n\t"
    "v_cvt_pk_bf16_f32 v123, v230, v231\n\t"
    "v_exp_f32 v233, v233\n\t"
    "v_add_f32 v251, v251, v232\n\t"
    "v_mfma_f32_16x16x32_bf16 v[168:171], v[72:75], v[108:111], v[168:171]\n\t"
    "ds_read_b64_tr_b16 v[42:43], v246 offset:61440\n\t"
    "v_exp_f32 v234, v234\n\t"
    "v_add_f32 v251, v251, v233\n\t"
    "s_add_u32 s63, s74, 69632\n\t"
    "buffer_load_dwordx4 v[0:3], v252, s[40:43], s63 offen sc1\n\t"
    "s_add_u32 s63, s74, 69632\n\t"
    "buffer_load_dwordx4 v[4:7], v252, s[40:43], s63 offen offset:1024 sc1\n\t"
    "v_mfma_f32_16x16x32_bf16 v[164:167], v[76:79], v[104:107], v[164:167]\n\t"
    "ds_read_b64_tr_b16 v[44:45], v247 offset:57344\n\t"
    "v_cvt_pk_bf16_f32 v124, v232, v233\n\t"
    "v_exp_f32 v235, v235\n\t"
    "v_add_f32 v251, v251, v234\n\t"
    "v_exp_f32 v236, v236\n\t"
    "v_mfma_f32_16x16x32_bf16 v[172:175], v[76:79], v[108:111], v[172:175]\n\t"
    "ds_read_b64_tr_b16 v[46:47], v247 offset:61440\n\t"
    "v_add_f32 v251, v251, v235\n\t"
    "s_add_u32 s63, s74, 69632\n\t"
    "buffer_load_dwordx4 v[16:19], v252, s[40:43], s63 offen offset:2048 sc1\n\t"
    "s_add_u32 s63, s74, 69632\n\t"
    "buffer_load_dwordx4 v[20:23], v252, s[40:43], s63 offen offset:3072 sc1\n\t"
    "s_waitcnt lgkmcnt(12)\n\t"
    "v_mfma_f32_16x16x32_bf16 v[176:179], v[88:91], v[104:107], v[176:179]\n\t"
    "ds_read_b64_tr_b16 v[56:57], v244 offset:58368\n\t"
    "v_cvt_pk_bf16_f32 v125, v234, v235\n\t"
    "v_exp_f32 v237, v237\n\t"
    "v_add_f32 v251, v251, v236\n\t"
    "v_exp_f32 v238, v238\n\t"
    "v_mfma_f32_16x16x32_bf16 v[184:187], v[88:91], v[108:111], v[184:187]\n\t"
    "ds_read_b64_tr_b16 v[58:59], v244 offset:62464\n\t"
    "v_add_f32 v251, v251, v237\n\t"
    "s_add_u32 s63, s74, 73728\n\t"
    "buffer_load_dwordx4 v[32:35], v252, s[40:43], s63 offen sc1\n\t"
    "s_add_u32 s63, s74, 73728\n\t"
    "buffer_load_dwordx4 v[36:39], v252, s[40:43], s63 offen offset:1024 sc1\n\t"
    "v_mfma_f32_16x16x32_bf16 v[180:183], v[92:95], v[104:107], v[180:183]\n\t"
    "ds_read_b64_tr_b16 v[60:61], v245 offset:58368\n\t"
    "s_setprio 0\n\t"
    "v_cvt_pk_bf16_f32 v126, v236, v237\n\t"
    "v_exp_f32 v239, v239\n\t"
    "v_add_f32 v251, v251, v238\n\t"
    "v_mfma_f32_16x16x32_bf16 v[188:191], v[92:95], v[108:111], v[188:191]\n\t"
    "ds_read_b64_tr_b16 v[62:63], v245 offset:62464\n\t"
    "v_add_f32 v251, v251, v239\n\t"
    "v_cvt_pk_bf16_f32 v127, v238, v239\n\t"
    "s_add_u32 s63, s74, 73728\n\t"
    "buffer_load_dwordx4 v[48:51], v252, s[40:43], s63 offen offset:2048 sc1\n\t"
    "s_add_u32 s63, s74, 73728\n\t"
    "buffer_load_dwordx4 v[52:55], v252, s[40:43], s63 offen offset:3072 sc1\n\t"
    "s_waitcnt lgkmcnt(12)\n\t"
    "v_mfma_f32_16x16x32_bf16 v[176:179], v[8:11], v[120:123], v[176:179]\n\t"
    "v_mfma_f32_16x16x32_bf16 v[184:187], v[8:11], v[124:127], v[184:187]\n\t"
    "v_mov_b32 v253, v250\n\t"
    "v_mov_b32 v254, v251\n\t"
    "s_nop 0\n\t"
    "v_permlane32_swap_b32 v250, v253\n\t"
    "v_permlane32_swap_b32 v251, v254\n\t"
    "v_add_f32 v250, v250, v253\n\t"
    "v_add_f32 v251, v251, v254\n\t"
    "v_mov_b32 v253, v250\n\t"
    "v_mov_b32 v254, v251\n\t"
    "s_nop 0\n\t"
    "v_permlane16_swap_b32 v250, v253\n\t"
    "v_permlane16_swap_b32 v251, v254\n\t"
    "v_add_f32 v250, v250, v253\n\t"
    "v_add_f32 v251, v251, v254\n\t"
    "v_add_f32 v254, v250, v251\n\t"
    "v_rcp_f32 v250, v250\n\t"
    "v_rcp_f32 v251, v251\n\t"
    "v_cmp_nge_f32 vcc, 0x57800000, v254\n\t"
    "s_nop 1\n\t"
    "s_cmp_lg_u64 vcc, 0\n\t"
    "s_cselect_b32 s65, 2, 0\n\t"
    "s_or_b32 s64, s64, s65\n\t"
    "s_cmp_lg_u32 s64, 0\n\t"
    "s_cbranch_scc1 KEND_%=\n\t"
    "s_cmp_eq_u32 s71, 1\n\t"
    "s_cbranch_scc1 PH_%=\n\t"
    "s_waitcnt vmcnt(0)\n\t"
    "s_mov_b32 s70, 0x200000\n\t"
    "PSP0_%=:\n\t"
    "s_sleep 4\n\t"
    "s_add_u32 s63, %[pollfl], 0\n\t"
    "buffer_load_dwordx4 v[200:203], v205, s[56:59], s63 offen sc1\n\t"
    "s_waitcnt vmcnt(0)\n\t"
    "v_xor_b32 v200, v200, v206\n\t"
    "v_xor_b32 v201, v201, v207\n\t"
    "v_xor_b32 v202, v202, v206\n\t"
    "v_xor_b32 v203, v203, v207\n\t"
    "v_or3_b32 v200, v200, v201, v202\n\t"
    "v_or_b32 v200, v200, v203\n\t"
    "v_cmp_ne_u32 vcc, 0, v200\n\t"
    "s_sub_u32 s70, s70, 1\n\t"
    "s_cbranch_scc1 PGO0_%=\n\t"
    "s_nop 4\n\t"
    "s_cbranch_vccnz PSP0_%=\n\t"
    "PGO0_%=:\n\t"
    "s_nop 1\n\t"
    "s_mov_b32 s42, %[onrec]\n\t"
    "s_add_u32 s63, s74, 0\n\t"
    "s_nop 1\n\t"
    "buffer_load_dwordx4 v[64:67], v252, s[40:43], s63 offen sc1\n\t"
    "s_nop 1\n\t"
    "s_add_u32 s63, s74, 0\n\t"
    "s_nop 1\n\t"
    "buffer_load_dwordx4 v[68:71], v252, s[40:43], s63 offen offset:1024 sc1\n\t"
    "s_nop 1\n\t"
    "s_add_u32 s63, s74, 0\n\t"
    "s_nop 1\n\t"
    "buffer_load_dwordx4 v[80:83], v252, s[40:43], s63 offen offset:2048 sc1\n\t"
    "s_nop 1\n\t"
    "s_add_u32 s63, s74, 0\n\t"
    "s_nop 1\n\t"
    "buffer_load_dwordx4 v[84:87], v252, s[40:43], s63 offen offset:3072 sc1\n\t"
    "s_nop 1\n\t"
    "s_add_u32 s63, s74, 4096\n\t"
    "s_nop 1\n\t"
    "buffer_load_dwordx4 v[96:99], v252, s[40:43], s63 offen sc1\n\t"
    "s_nop 1\n\t"
    "s_add_u32 s63, s74, 4096\n\t"
    "s_nop 1\n\t"
    "buffer_load_dwordx4 v[100:103], v252, s[40:43], s63 offen offset:1024 sc1\n\t"
    "s_nop 1\n\t"
    "s_add_u32 s63, s74, 4096\n\t"
    "s_nop 1\n\t"
    "buffer_load_dwordx4 v[112:115], v252, s[40:43], s63 offen offset:2048 sc1\n\t"
    "s_nop 1\n\t"
    "s_add_u32 s63, s74, 4096\n\t"
    "s_nop 1\n\t"
    "buffer_load_dwordx4 v[116:119], v252, s[40:43], s63 offen offset:3072 sc1\n\t"
    "s_nop 1\n\t"
    "s_add_u32 s63, s74, 69632\n\t"
    "s_nop 1\n\t"
    "buffer_load_dwordx4 v[0:3], v252, s[40:43], s63 offen sc1\n\t"
    "s_nop 1\n\t"
    "s_add_u32 s63, s74, 69632\n\t"
    "s_nop 1\n\t"
    "buffer_load_dwordx4 v[4:7], v252, s[40:43], s63 offen offset:1024 sc1\n\t"
    "s_nop 1\n\t"
    "s_add_u32 s63, s74, 69632\n\t"
    "s_nop 1\n\t"
    "buffer_load_dwordx4 v[16:19], v252, s[40:43], s63 offen offset:2048 sc1\n\t"
    "s_nop 1\n\t"
    "s_add_u32 s63, s74, 69632\n\t"
    "s_nop 1\n\t"
    "buffer_load_dwordx4 v[20:23], v252, s[40:43], s63 offen offset:3072 sc1\n\t"
    "s_nop 1\n\t"
    "s_add_u32 s63, s74, 73728\n\t"
    "s_nop 1\n\t"
    "buffer_load_dwordx4 v[32:35], v252, s[40:43], s63 offen sc1\n\t"
    "s_nop 1\n\t"
    "s_add_u32 s63, s74, 73728\n\t"
    "s_nop 1\n\t"
    "buffer_load_dwordx4 v[36:39], v252, s[40:43], s63 offen offset:1024 sc1\n\t"
    "s_nop 1\n\t"
    "s_add_u32 s63, s74, 73728\n\t"
    "s_nop 1\n\t"
    "buffer_load_dwordx4 v[48:51], v252, s[40:43], s63 offen offset:2048 sc1\n\t"
    "s_nop 1\n\t"
    "s_add_u32 s63, s74, 73728\n\t"
    "s_nop 1\n\t"
    "buffer_load_dwordx4 v[52:55], v252, s[40:43], s63 offen offset:3072 sc1\n\t"
    "s_nop 1\n\t"
    "s_waitcnt vmcnt(0)\n\t"
    "PH_%=:\n\t"
    "s_nop 1\n\t"
    "v_mov_b32 v204, 0\n\t"
    "buffer_store_dwordx2 v[206:207], v204, s[56:59], %[bump] offen sc1\n\t"
    "v_mfma_f32_16x16x32_bf16 v[180:183], v[12:15], v[120:123], v[180:183]\n\t"
    "v_mfma_f32_16x16x32_bf16 v[188:191], v[12:15], v[124:127], v[188:191]\n\t"
    "s_waitcnt lgkmcnt(0)\n\t"
    "s_barrier\n\t"
    "s_setprio 3\n\t"
    "ds_read_b64_tr_b16 v[8:9], v240 offset:0\n\t"
    "ds_read_b64_tr_b16 v[10:11], v240 offset:4096\n\t"
    "v_mfma_f32_16x16x32_bf16 v[128:131], v[24:27], v[120:123], v[128:131]\n\t"
    "ds_read_b64_tr_b16 v[12:13], v241 offset:0\n\t"
    "v_mfma_f32_16x16x32_bf16 v[136:139], v[24:27], v[124:127], v[136:139]\n\t"
    "ds_read_b64_tr_b16 v[14:15], v241 offset:4096\n\t"
    "v_mfma_f32_16x16x32_bf16 v[132:135], v[28:31], v[120:123], v[132:135]\n\t"
    "ds_read_b64_tr_b16 v[24:25], v240 offset:8192\n\t"
    "v_mfma_f32_16x16x32_bf16 v[140:143], v[28:31], v[124:127], v[140:143]\n\t"
    "ds_read_b64_tr_b16 v[26:27], v240 offset:12288\n\t"
    "s_add_u32 m0, s61, 65536\n\t"
    "s_add_u32 s63, s75, 2097152\n\t"
    "buffer_load_dwordx4 v255, s[36:39], s63 offen lds\n\t"
    "v_mfma_f32_16x16x32_bf16 v[144:147], v[40:43], v[120:123], v[144:147]\n\t"
    "ds_read_b64_tr_b16 v[28:29], v241 offset:8192\n\t"
    "v_mul_f32 v176, v250, v176\n\t"
    "v_mul_f32 v177, v250, v177\n\t"
    "v_cvt_pk_bf16_f32 v176, v176, v177\n\t"
    "v_mfma_f32_16x16x32_bf16 v[152:155], v[40:43], v[124:127], v[152:155]\n\t"
    "ds_read_b64_tr_b16 v[30:31], v241 offset:12288\n\t"
    "v_mul_f32 v178, v250, v178\n\t"
    "v_mul_f32 v179, v250, v179\n\t"
    "v_mfma_f32_16x16x32_bf16 v[148:151], v[44:47], v[120:123], v[148:151]\n\t"
    "ds_read_b64_tr_b16 v[40:41], v242 offset:0\n\t"
    "v_cvt_pk_bf16_f32 v177, v178, v179\n\t"
    "v_mul_f32 v180, v250, v180\n\t"
    "v_mfma_f32_16x16x32_bf16 v[156:159], v[44:47], v[124:127], v[156:159]\n\t"
    "ds_read_b64_tr_b16 v[42:43], v242 offset:4096\n\t"
    "s_add_u32 m0, s61, 66560\n\t"
    "s_add_u32 s63, s76, 2097152\n\t"
    "buffer_load_dwordx4 v255, s[36:39], s63 offen lds\n\t"
    "v_mul_f32 v181, v250, v181\n\t"
    "v_cvt_pk_bf16_f32 v178, v180, v181\n\t"
    "v_mfma_f32_16x16x32_bf16 v[160:163], v[56:59], v[120:123], v[160:163]\n\t"
    "ds_read_b64_tr_b16 v[44:45], v243 offset:0\n\t"
    "v_mul_f32 v182, v250, v182\n\t"
    "v_mul_f32 v183, v250, v183\n\t"
    "v_mfma_f32_16x16x32_bf16 v[168:171], v[56:59], v[124:127], v[168:171]\n\t"
    "ds_read_b64_tr_b16 v[46:47], v243 offset:4096\n\t"
    "v_cvt_pk_bf16_f32 v179, v182, v183\n\t"
    "v_mul_f32 v184, v251, v184\n\t"
    "v_mfma_f32_16x16x32_bf16 v[164:167], v[60:63], v[120:123], v[164:167]\n\t"
    "ds_read_b64_tr_b16 v[56:57], v242 offset:8192\n\t"
    "v_mul_f32 v185, v251, v185\n\t"
    "v_cvt_pk_bf16_f32 v180, v184, v185\n\t"
    "v_mul_f32 v186, v251, v186\n\t"
    "v_mfma_f32_16x16x32_bf16 v[172:175], v[60:63], v[124:127], v[172:175]\n\t"
    "ds_read_b64_tr_b16 v[58:59], v242 offset:12288\n\t"
    "s_add_u32 m0, s61, 81920\n\t"
    "s_add_u32 s63, s75, 2359296\n\t"
    "buffer_load_dwordx4 v255, s[36:39], s63 offen lds\n\t"
    "v_mul_f32 v187, v251, v187\n\t"
    "v_cvt_pk_bf16_f32 v181, v186, v187\n\t"
    "s_waitcnt vmcnt(28) lgkmcnt(10)\n\t"
    "v_mfma_f32_16x16x32_bf16 v[192:195], v[8:11], v[64:67], 0\n\t"
    "ds_read_b64_tr_b16 v[60:61], v243 offset:8192\n\t"
    "PAD9_%=: s_cbranch_execz PADRET_%=\n\t"
    "s_setprio 2\n\t"
    "v_mul_f32 v188, v251, v188\n\t"
    "v_mul_f32 v189, v251, v189\n\t"
    "s_waitcnt vmcnt(26)\n\t"
    "v_mfma_f32_16x16x32_bf16 v[200:203], v[8:11], v[68:71], 0\n\t"
    "ds_read_b64_tr_b16 v[62:63], v243 offset:12288\n\t"
    "v_cvt_pk_bf16_f32 v182, v188, v189\n\t"
    "v_mul_f32 v190, v251, v190\n\t"
    "v_mfma_f32_16x16x32_bf16 v[196:199], v[12:15], v[64:67], 0\n\t"
    "ds_read_b64_tr_b16 v[8:9], v240 offset:1024\n\t"
    "v_mul_f32 v191, v251, v191\n\t"
    "v_cvt_pk_bf16_f32 v183, v190, v191\n\t"
    "v_mfma_f32_16x16x32_bf16 v[204:207], v[12:15], v[68:71], 0\n\t"
    "ds_read_b64_tr_b16 v[10:11], v240 offset:5120\n\t"
    "s_add_u32 m0, s61, 82944\n\t"
    "s_add_u32 s63, s76, 2359296\n\t"
    "buffer_load_dwordx4 v255, s[36:39], s63 offen lds\n\t"
    "s_add_u32 s63, s67, 12288\n\t"
    "buffer_store_dwordx4 v[176:179], v252, s[52:55], s63 offen offset:2048 sc1\n\t"
    "s_add_u32 s63, s62, 12288\n\t"
    "buffer_store_dwordx4 v[180:183], v252, s[52:55], s63 offen offset:3072 sc1\n\t"
    "s_waitcnt vmcnt(28) lgkmcnt(10)\n\t"
    "v_mfma_f32_16x16x32_bf16 v[192:195], v[24:27], v[80:83], v[192:195]\n\t"
    "ds_read_b64_tr_b16 v[12:13], v241 offset:1024\n\t"
    "v_mul_f32 v128, v250, v128\n\t"
    "v_mul_f32 v129, v250, v129\n\t"
    "s_waitcnt vmcnt(27)\n\t"
    "v_mfma_f32_16x16x32_bf16 v[200:203], v[24:27], v[84:87], v[200:203]\n\t"
    "ds_read_b64_tr_b16 v[14:15], v241 offset:5120\n\t"
    "v_cvt_pk_bf16_f32 v128, v128, v129\n\t"
    "v_mul_f32 v130, v250, v130\n\t"
    "v_mfma_f32_16x16x32_bf16 v[196:199], v[28:31], v[80:83], v[196:199]\n\t"
    "ds_read_b64_tr_b16 v[24:25], v240 offset:9216\n\t"
    "v_mul_f32 v131, v250, v131\n\t"
    "v_cvt_pk_bf16_f32 v129, v130, v131\n\t"
    "v_mfma_f32_16x16x32_bf16 v[204:207], v[28:31], v[84:87], v[204:207]\n\t"
    "ds_read_b64_tr_b16 v[26:27], v240 offset:13312\n\t"
    "v_mul_f32 v132, v250, v132\n\t"
    "v_mul_f32 v133, v250, v133\n\t"
    "s_waitcnt lgkmcnt(10)\n\t"
    "v_mfma_f32_16x16x32_bf16 v[208:211], v[40:43], v[64:67], 0\n\t"
    "ds_read_b64_tr_b16 v[28:29], v241 offset:9216\n\t"
    "s_setprio 1\n\t"
    "v_cvt_pk_bf16_f32 v130, v132, v133\n\t"
    "v_mul_f32 v134, v250, v134\n\t"
    "v_mul_f32 v135, v250, v135\n\t"
    "v_cvt_pk_bf16_f32 v131, v134, v135\n\t"
    "v_add_u32 v104, 131072, v240\n\t"
    "v_add_u32 v105, 131072, v241\n\t"
    "v_mfma_f32_16x16x32_bf16 v[216:219], v[40:43], v[68:71], 0\n\t"
    "ds_read_b64_tr_b16 v[30:31], v241 offset:13312\n\t"
    "v_add_u32 v106, 131072, v242\n\t"
    "v_add_u32 v107, 131072, v243\n\t"
    "v_add_u32 v244, 65536, v240\n\t"
    "v_add_u32 v245, 65536, v241\n\t"
    "v_add_u32 v246, 65536, v242\n\t"
    "v_add_u32 v247, 65536, v243\n\t"
    "v_mfma_f32_16x16x32_bf16 v[212:215], v[44:47], v[64:67], 0\n\t"
    "ds_read_b64_tr_b16 v[40:41], v242 offset:1024\n\t"
    "v_mul_f32 v136, v251, v136\n\t"
    "v_mul_f32 v137, v251, v137\n\t"
    "v_mfma_f32_16x16x32_bf16 v[220:223], v[44:47], v[68:71], 0\n\t"
    "ds_read_b64_tr_b16 v[42:43], v242 offset:5120\n\t"
    "v_cvt_pk_bf16_f32 v132, v136, v137\n\t"
    "v_mul_f32 v138, v251, v138\n\t"
    "s_waitcnt lgkmcnt(10)\n\t"
    "v_mfma_f32_16x16x32_bf16 v[208:211], v[56:59], v[80:83], v[208:211]\n\t"
    "ds_read_b64_tr_b16 v[44:45], v243 offset:1024\n\t"
    "v_mul_f32 v139, v251, v139\n\t"
    "v_cvt_pk_bf16_f32 v133, v138, v139\n\t"
    "v_mfma_f32_16x16x32_bf16 v[216:219], v[56:59], v[84:87], v[216:219]\n\t"
    "ds_read_b64_tr_b16 v[46:47], v243 offset:5120\n\t"
    "v_mul_f32 v140, v251, v140\n\t"
    "v_mul_f32 v141, v251, v141\n\t"
    "v_mfma_f32_16x16x32_bf16 v[212:215], v[60:63], v[80:83], v[212:215]\n\t"
    "ds_read_b64_tr_b16 v[56:57], v242 offset:9216\n\t"
    "s_setprio 0\n\t"
    "v_cvt_pk_bf16_f32 v134, v140, v141\n\t"
    "v_mul_f32 v142, v251, v142\n\t"
    "v_mfma_f32_16x16x32_bf16 v[220:223], v[60:63], v[84:87], v[220:223]\n\t"
    "ds_read_b64_tr_b16 v[58:59], v242 offset:13312\n\t"
    "v_mul_f32 v143, v251, v143\n\t"
    "v_cvt_pk_bf16_f32 v135, v142, v143\n\t"
    "s_waitcnt lgkmcnt(10)\n\t"
    "v_mfma_f32_16x16x32_bf16 v[224:227], v[8:11], v[64:67], 0\n\t"
    "ds_read_b64_tr_b16 v[60:61], v243 offset:9216\n\t"
    "v_mul_f32 v144, v250, v144\n\t"
    "v_mul_f32 v145, v250, v145\n\t"
    "v_mfma_f32_16x16x32_bf16 v[232:235], v[8:11], v[68:71], 0\n\t"
    "ds_read_b64_tr_b16 v[62:63], v243 offset:13312\n\t"
    "v_cvt_pk_bf16_f32 v144, v144, v145\n\t"
    "v_mul_f32 v146, v250, v146\n\t"
    "v_mfma_f32_16x16x32_bf16 v[228:231], v[12:15], v[64:67], 0\n\t"
    "v_mul_f32 v147, v250, v147\n\t"
    "v_cvt_pk_bf16_f32 v145, v146, v147\n\t"
    "v_mul_f32 v148, v250, v148\n\t"
    "v_mfma_f32_16x16x32_bf16 v[236:239], v[12:15], v[68:71], 0\n\t"
    "v_mul_f32 v149, v250, v149\n\t"
    "s_add_u32 s63, s67, 8192\n\t"
    "buffer_store_dwordx4 v[128:131], v252, s[52:55], s63 offen sc1\n\t"
    "s_waitcnt lgkmcnt(8)\n\t"
    "v_mfma_f32_16x16x32_bf16 v[224:227], v[24:27], v[80:83], v[224:227]\n\t"
    "v_cvt_pk_bf16_f32 v146, v148, v149\n\t"
    "v_mul_f32 v150, v250, v150\n\t"
    "v_mul_f32 v151, v250, v151\n\t"
    "v_cvt_pk_bf16_f32 v147, v150, v151\n\t"
    "v_mfma_f32_16x16x32_bf16 v[232:235], v[24:27], v[84:87], v[232:235]\n\t"
    "s_waitcnt lgkmcnt(0)\n\t"
    "s_barrier\n\t"
    "s_setprio 3\n\t"
    "ds_read_b64_tr_b16 v[8:9], v104 offset:0\n\t"
    "ds_read_b64_tr_b16 v[10:11], v104 offset:4096\n\t"
    "ds_read_b64_tr_b16 v[12:13], v105 offset:0\n\t"
    "ds_read_b64_tr_b16 v[14:15], v105 offset:4096\n\t"
    "v_mfma_f32_16x16x32_bf16 v[228:231], v[28:31], v[80:83], v[228:231]\n\t"
    "ds_read_b64_tr_b16 v[24:25], v106 offset:0\n\t"
    "v_mul_f32 v152, v251, v152\n\t"
    "v_mul_f32 v153, v251, v153\n\t"
    "v_cvt_pk_bf16_f32 v148, v152, v153\n\t"
    "v_mfma_f32_16x16x32_bf16 v[236:239], v[28:31], v[84:87], v[236:239]\n\t"
    "ds_read_b64_tr_b16 v[26:27], v106 offset:4096\n\t"
    "v_mul_f32 v154, v251, v154\n\t"
    "s_add_u32 s63, s62, 8192\n\t"
    "buffer_store_dwordx4 v[132:135], v252, s[52:55], s63 offen offset:1024 sc1\n\t"
    "v_mfma_f32_16x16x32_bf16 v[176:179], v[40:43], v[64:67], 0\n\t"
    "ds_read_b64_tr_b16 v[28:29], v107 offset:0\n\t"
    "v_mul_f32 v155, v251, v155\n\t"
    "v_cvt_pk_bf16_f32 v149, v154, v155\n\t"
    "v_mfma_f32_16x16x32_bf16 v[184:187], v[40:43], v[68:71], 0\n\t"
    "ds_read_b64_tr_b16 v[30:31], v107 offset:4096\n\t"
    "v_mul_f32 v156, v251, v156\n\t"
    "v_mul_f32 v157, v251, v157\n\t"
    "v_mfma_f32_16x16x32_bf16 v[180:183], v[44:47], v[64:67], 0\n\t"
    "ds_read_b64_tr_b16 v[40:41], v104 offset:1024\n\t"
    "v_cvt_pk_bf16_f32 v150, v156, v157\n\t"
    "v_mul_f32 v158, v251, v158\n\t"
    "v_mfma_f32_16x16x32_bf16 v[188:191], v[44:47], v[68:71], 0\n\t"
    "ds_read_b64_tr_b16 v[42:43], v104 offset:5120\n\t"
    "v_mul_f32 v159, v251, v159\n\t"
    "v_cvt_pk_bf16_f32 v151, v158, v159\n\t"
    "v_mfma_f32_16x16x32_bf16 v[176:179], v[56:59], v[80:83], v[176:179]\n\t"
    "ds_read_b64_tr_b16 v[44:45], v105 offset:1024\n\t"
    "v_mul_f32 v160, v250, v160\n\t"
    "v_mul_f32 v161, v250, v161\n\t"
    "v_mfma_f32_16x16x32_bf16 v[184:187], v[56:59], v[84:87], v[184:187]\n\t"
    "ds_read_b64_tr_b16 v[46:47], v105 offset:5120\n\t"
    "v_cvt_pk_bf16_f32 v160, v160, v161\n\t"
    "v_mul_f32 v162, v250, v162\n\t"
    "v_mfma_f32_16x16x32_bf16 v[180:183], v[60:63], v[80:83], v[180:183]\n\t"
    "ds_read_b64_tr_b16 v[56:57], v106 offset:1024\n\t"
    "v_mul_f32 v163, v250, v163\n\t"
    "v_cvt_pk_bf16_f32 v161, v162, v163\n\t"
    "v_mul_f32 v164, v250, v164\n\t"
    "v_mfma_f32_16x16x32_bf16 v[188:191], v[60:63], v[84:87], v[188:191]\n\t"
    "ds_read_b64_tr_b16 v[58:59], v106 offset:5120\n\t"
    "v_mul_f32 v165, v250, v165\n\t"
    "s_add_u32 s63, s67, 8192\n\t"
    "buffer_store_dwordx4 v[144:147], v252, s[52:55], s63 offen offset:2048 sc1\n\t"
    "s_waitcnt vmcnt(29) lgkmcnt(10)\n\t"
    "v_mfma_f32_16x16x32_bf16 v[192:195], v[8:11], v[96:99], v[192:195]\n\t"
    "ds_read_b64_tr_b16 v[60:61], v107 offset:1024\n\t"
    "v_cvt_pk_bf16_f32 v162, v164, v165\n\t"
    "v_mul_f32 v166, v250, v166\n\t"
    "v_mul_f32 v167, v250, v167\n\t"
    "s_waitcnt vmcnt(28)\n\t"
    "v_mfma_f32_16x16x32_bf16 v[200:203], v[8:11], v[100:103], v[200:203]\n\t"
    "ds_read_b64_tr_b16 v[62:63], v107 offset:5120\n\t"
    "v_cvt_pk_bf16_f32 v163, v166, v167\n\t"
    "s_add_u32 s63, s62, 8192\n\t"
    "buffer_store_dwordx4 v[148:151], v252, s[52:55], s63 offen offset:3072 sc1\n\t"
    "v_mfma_f32_16x16x32_bf16 v[196:199], v[12:15], v[96:99], v[196:199]\n\t"
    "ds_read_b64_tr_b16 v[8:9], v104 offset:8192\n\t"
    "s_setprio 2\n\t"
    "v_mul_f32 v168, v251, v168\n\t"
    "v_mul_f32 v169, v251, v169\n\t"
    "v_mfma_f32_16x16x32_bf16 v[204:207], v[12:15], v[100:103], v[204:207]\n\t"
    "ds_read_b64_tr_b16 v[10:11], v104 offset:12288\n\t"
    "v_cvt_pk_bf16_f32 v164, v168, v169\n\t"
    "v_mul_f32 v170, v251, v170\n\t"
    "s_waitcnt lgkmcnt(10)\n\t"
    "v_mfma_f32_16x16x32_bf16 v[208:211], v[24:27], v[96:99], v[208:211]\n\t"
    "ds_read_b64_tr_b16 v[12:13], v105 offset:8192\n\t"
    "v_mul_f32 v171, v251, v171\n\t"
    "v_cvt_pk_bf16_f32 v165, v170, v171\n\t"
    "v_mfma_f32_16x16x32_bf16 v[216:219], v[24:27], v[100:103], v[216:219]\n\t"
    "ds_read_b64_tr_b16 v[14:15], v105 offset:12288\n\t"
    "v_mul_f32 v172, v251, v172\n\t"
    "v_mul_f32 v173, v251, v173\n\t"
    "v_mfma_f32_16x16x32_bf16 v[212:215], v[28:31], v[96:99], v[212:215]\n\t"
    "ds_read_b64_tr_b16 v[24:25], v106 offset:8192\n\t"
    "v_cvt_pk_bf16_f32 v166, v172, v173\n\t"
    "v_mul_f32 v174, v251, v174\n\t"
    "v_mfma_f32_16x16x32_bf16 v[220:223], v[28:31], v[100:103], v[220:223]\n\t"
    "ds_read_b64_tr_b16 v[26:27], v106 offset:12288\n\t"
    "v_mul_f32 v175, v251, v175\n\t"
    "v_cvt_pk_bf16_f32 v167, v174, v175\n\t"
    "s_waitcnt lgkmcnt(10)\n\t"
    "v_mfma_f32_16x16x32_bf16 v[224:227], v[40:43], v[96:99], v[224:227]\n\t"
    "ds_read_b64_tr_b16 v[28:29], v107 offset:8192\n\t"
    "v_mfma_f32_16x16x32_bf16 v[232:235], v[40:43], v[100:103], v[232:235]\n\t"
    "ds_read_b64_tr_b16 v[30:31], v107 offset:12288\n\t"
    "v_mfma_f32_16x16x32_bf16 v[228:231], v[44:47], v[96:99], v[228:231]\n\t"
    "ds_read_b64_tr_b16 v[40:41], v104 offset:9216\n\t"
    "s_setprio 1\n\t"
    "v_mfma_f32_16x16x32_bf16 v[236:239], v[44:47], v[100:103], v[236:239]\n\t"
    "ds_read_b64_tr_b16 v[42:43], v104 offset:13312\n\t"
    "s_add_u32 s63, s67, 12288\n\t"
    "buffer_store_dwordx4 v[160:163], v252, s[52:55], s63 offen sc1\n\t"
    "s_waitcnt lgkmcnt(10)\n\t"
    "v_mfma_f32_16x16x32_bf16 v[176:179], v[56:59], v[96:99], v[176:179]\n\t"
    "ds_read_b64_tr_b16 v[44:45], v105 offset:9216\n\t"
    "s_add_u32 s63, s62, 12288\n\t"
    "buffer_store_dwordx4 v[164:167], v252, s[52:55], s63 offen offset:1024 sc1\n\t"
    "v_mfma_f32_16x16x32_bf16 v[184:187], v[56:59], v[100:103], v[184:187]\n\t"
    "ds_read_b64_tr_b16 v[46:47], v105 offset:13312\n\t"
    "v_mfma_f32_16x16x32_bf16 v[180:183], v[60:63], v[96:99], v[180:183]\n\t"
    "ds_read_b64_tr_b16 v[56:57], v106 offset:9216\n\t"
    "v_mfma_f32_16x16x32_bf16 v[188:191], v[60:63], v[100:103], v[188:191]\n\t"
    "ds_read_b64_tr_b16 v[58:59], v106 offset:13312\n\t"
    "s_waitcnt vmcnt(30) lgkmcnt(10)\n\t"
    "v_mfma_f32_16x16x32_bf16 v[192:195], v[8:11], v[112:115], v[192:195]\n\t"
    "ds_read_b64_tr_b16 v[60:61], v107 offset:9216\n\t"
    "s_setprio 0\n\t"
    "s_waitcnt vmcnt(29)\n\t"
    "v_mfma_f32_16x16x32_bf16 v[200:203], v[8:11], v[116:119], v[200:203]\n\t"
    "ds_read_b64_tr_b16 v[62:63], v107 offset:13312\n\t"
    "v_mfma_f32_16x16x32_bf16 v[196:199], v[12:15], v[112:115], v[196:199]\n\t"
    "v_mfma_f32_16x16x32_bf16 v[204:207], v[12:15], v[116:119], v[204:207]\n\t"
    "s_waitcnt lgkmcnt(8)\n\t"
    "v_mfma_f32_16x16x32_bf16 v[208:211], v[24:27], v[112:115], v[208:211]\n\t"
    "v_mfma_f32_16x16x32_bf16 v[216:219], v[24:27], v[116:119], v[216:219]\n\t"
    "s_waitcnt vmcnt(25) lgkmcnt(0)\n\t"
    "s_barrier\n\t"
    "s_setprio 3\n\t"
    "ds_read_b64_tr_b16 v[8:9], v240 offset:32768\n\t"
    "ds_read_b64_tr_b16 v[10:11], v240 offset:36864\n\t"
    "ds_read_b64_tr_b16 v[12:13], v241 offset:32768\n\t"
    "ds_read_b64_tr_b16 v[14:15], v241 offset:36864\n\t"
    "v_mfma_f32_16x16x32_bf16 v[212:215], v[28:31], v[112:115], v[212:215]\n\t"
    "ds_read_b64_tr_b16 v[24:25], v242 offset:32768\n\t"
    "v_mfma_f32_16x16x32_bf16 v[220:223], v[28:31], v[116:119], v[220:223]\n\t"
    "ds_read_b64_tr_b16 v[26:27], v242 offset:36864\n\t"
    "v_mfma_f32_16x16x32_bf16 v[224:227], v[40:43], v[112:115], v[224:227]\n\t"
    "ds_read_b64_tr_b16 v[28:29], v243 offset:32768\n\t"
    "s_add_u32 s63, s74, 139264\n\t"
    "buffer_load_dwordx4 v[72:75], v252, s[40:43], s63 offen sc1\n\t"
    "v_mfma_f32_16x16x32_bf16 v[232:235], v[40:43], v[116:119], v[232:235]\n\t"
    "ds_read_b64_tr_b16 v[30:31], v243 offset:36864\n\t"
    "s_add_u32 m0, s61, 0\n\t"
    "s_add_u32 s63, s75, 2162688\n\t"
    "buffer_load_dwordx4 v255, s[36:39], s63 offen lds\n\t"
    "v_mfma_f32_16x16x32_bf16 v[228:231], v[44:47], v[112:115], v[228:231]\n\t"
    "ds_read_b64_tr_b16 v[40:41], v240 offset:33792\n\t"
    "v_mfma_f32_16x16x32_bf16 v[236:239], v[44:47], v[116:119], v[236:239]\n\t"
    "ds_read_b64_tr_b16 v[42:43], v240 offset:37888\n\t"
    "v_mfma_f32_16x16x32_bf16 v[176:179], v[56:59], v[112:115], v[176:179]\n\t"
    "ds_read_b64_tr_b16 v[44:45], v241 offset:33792\n\t"
    "v_mfma_f32_16x16x32_bf16 v[184:187], v[56:59], v[116:119], v[184:187]\n\t"
    "ds_read_b64_tr_b16 v[46:47], v241 offset:37888\n\t"
    "s_add_u32 m0, s61, 1024\n\t"
    "s_add_u32 s63, s76, 2162688\n\t"
    "buffer_load_dwordx4 v255, s[36:39], s63 offen lds\n\t"
    "v_mfma_f32_16x16x32_bf16 v[180:183], v[60:63], v[112:115], v[180:183]\n\t"
    "ds_read_b64_tr_b16 v[56:57], v242 offset:33792\n\t"
    "s_add_u32 s63, s74, 139264\n\t"
    "buffer_load_dwordx4 v[76:79], v252, s[40:43], s63 offen offset:1024 sc1\n\t"
    "v_mfma_f32_16x16x32_bf16 v[188:191], v[60:63], v[116:119], v[188:191]\n\t"
    "ds_read_b64_tr_b16 v[58:59], v242 offset:37888\n\t"
    "s_waitcnt vmcnt(24) lgkmcnt(10)\n\t"
    "v_mfma_f32_16x16x32_bf16 v[192:195], v[8:11], v[0:3], v[192:195]\n\t"
    "ds_read_b64_tr_b16 v[60:61], v243 offset:33792\n\t"
    "s_waitcnt vmcnt(23)\n\t"
    "v_mfma_f32_16x16x32_bf16 v[200:203], v[8:11], v[4:7], v[200:203]\n\t"
    "ds_read_b64_tr_b16 v[62:63], v243 offset:37888\n\t"
    "s_add_u32 m0, s61, 16384\n\t"
    "s_add_u32 s63, s75, 2424832\n\t"
    "buffer_load_dwordx4 v255, s[36:39], s63 offen lds\n\t"
    "v_mfma_f32_16x16x32_bf16 v[196:199], v[12:15], v[0:3], v[196:199]\n\t"
    "ds_read_b64_tr_b16 v[8:9], v240 offset:40960\n\t"
    "s_setprio 2\n\t"
    "v_mfma_f32_16x16x32_bf16 v[204:207], v[12:15], v[4:7], v[204:207]\n\t"
    "ds_read_b64_tr_b16 v[10:11], v240 offset:45056\n\t"
    "s_waitcnt lgkmcnt(10)\n\t"
    "v_mfma_f32_16x16x32_bf16 v[208:211], v[24:27], v[0:3], v[208:211]\n\t"
    "ds_read_b64_tr_b16 v[12:13], v241 offset:40960\n\t"
    "s_add_u32 s63, s74, 139264\n\t"
    "buffer_load_dwordx4 v[88:91], v252, s[40:43], s63 offen offset:2048 sc1\n\t"
    "v_mfma_f32_16x16x32_bf16 v[216:219], v[24:27], v[4:7], v[216:219]\n\t"
    "ds_read_b64_tr_b16 v[14:15], v241 offset:45056\n\t"
    "s_add_u32 m0, s61, 17408\n\t"
    "s_add_u32 s63, s76, 2424832\n\t"
    "buffer_load_dwordx4 v255, s[36:39], s63 offen lds\n\t"
    "v_mfma_f32_16x16x32_bf16 v[212:215], v[28:31], v[0:3], v[212:215]\n\t"
    "ds_read_b64_tr_b16 v[24:25], v242 offset:40960\n\t"
    "v_mfma_f32_16x16x32_bf16 v[220:223], v[28:31], v[4:7], v[220:223]\n\t"
    "ds_read_b64_tr_b16 v[26:27], v242 offset:45056\n\t"
    "s_waitcnt lgkmcnt(10)\n\t"
    "v_mfma_f32_16x16x32_bf16 v[224:227], v[40:43], v[0:3], v[224:227]\n\t"
    "ds_read_b64_tr_b16 v[28:29], v243 offset:40960\n\t"
    "v_mov_b32 v120, s72\n\t"
    "v_mov_b32 v121, s73\n\t"
    "v_mfma_f32_16x16x32_bf16 v[232:235], v[40:43], v[4:7], v[232:235]\n\t"
    "ds_read_b64_tr_b16 v[30:31], v243 offset:45056\n\t"
    "v_mfma_f32_16x16x32_bf16 v[228:231], v[44:47], v[0:3], v[228:231]\n\t"
    "ds_read_b64_tr_b16 v[40:41], v240 offset:41984\n\t"
    "s_setprio 1\n\t"
    "v_lshrrev_b32 v122, 1, v252\n\t"
    "v_mov_b32 v123, s67\n\t"
    "s_mov_b64 vcc, 2\n\t"
    "v_mov_b32 v124, s62\n\t"
    "v_cndmask_b32 v123, v123, v124, vcc\n\t"
    "s_mov_b64 vcc, 3\n\t"
    "v_add_u32 v122, v122, v123\n\t"
    "v_bfrev_b32 v123, 1\n\t"
    "v_cndmask_b32 v122, v123, v122, vcc\n\t"
    "v_mfma_f32_16x16x32_bf16 v[236:239], v[44:47], v[4:7], v[236:239]\n\t"
    "ds_read_b64_tr_b16 v[42:43], v240 offset:46080\n\t"
    "s_add_u32 s63, s74, 139264\n\t"
    "buffer_load_dwordx4 v[92:95], v252, s[40:43], s63 offen offset:3072 sc1\n\t"
    "s_waitcnt lgkmcnt(10)\n\t"
    "v_mfma_f32_16x16x32_bf16 v[176:179], v[56:59], v[0:3], v[176:179]\n\t"
    "ds_read_b64_tr_b16 v[44:45], v241 offset:41984\n\t"
    "v_mfma_f32_16x16x32_bf16 v[184:187], v[56:59], v[4:7], v[184:187]\n\t"
    "ds_read_b64_tr_b16 v[46:47], v241 offset:46080\n\t"
    "v_mfma_f32_16x16x32_bf16 v[180:183], v[60:63], v[0:3], v[180:183]\n\t"
    "ds_read_b64_tr_b16 v[56:57], v242 offset:41984\n\t"
    "s_waitcnt vmcnt(8)\n\t"
    "s_add_u32 s63, %[myfl], 4096\n\t"
    "s_cmp_eq_u32 s64, 0\n\t"
    "s_cselect_b32 s63, s63, 1073741824\n\t"
    "buffer_store_dwordx2 v[120:121], v122, s[56:59], s63 offen sc1\n\t"
    "v_mfma_f32_16x16x32_bf16 v[188:191], v[60:63], v[4:7], v[188:191]\n\t"
    "ds_read_b64_tr_b16 v[58:59], v242 offset:46080\n\t"
    "s_waitcnt lgkmcnt(10)\n\t"
    "v_mfma_f32_16x16x32_bf16 v[192:195], v[8:11], v[16:19], v[192:195]\n\t"
    "ds_read_b64_tr_b16 v[60:61], v243 offset:41984\n\t"
    "s_setprio 0\n\t"
    "v_mfma_f32_16x16x32_bf16 v[200:203], v[8:11], v[20:23], v[200:203]\n\t"
    "ds_read_b64_tr_b16 v[62:63], v243 offset:46080\n\t"
    "s_add_u32 s63, s74, 143360\n\t"
    "buffer_load_dwordx4 v[136:139], v252, s[40:43], s63 offen sc1\n\t"
    "v_mfma_f32_16x16x32_bf16 v[196:199], v[12:15], v[16:19], v[196:199]\n\t"
    "v_mfma_f32_16x16x32_bf16 v[204:207], v[12:15], v[20:23], v[204:207]\n\t"
    "s_waitcnt lgkmcnt(8)\n\t"
    "v_mfma_f32_16x16x32_bf16 v[208:211], v[24:27], v[16:19], v[208:211]\n\t"
    "v_mfma_f32_16x16x32_bf16 v[216:219], v[24:27], v[20:23], v[216:219]\n\t"
    "s_waitcnt lgkmcnt(0)\n\t"
    "s_barrier\n\t"
    "s_setprio 3\n\t"
    "ds_read_b64_tr_b16 v[8:9], v244 offset:32768\n\t"
    "ds_read_b64_tr_b16 v[10:11], v244 offset:36864\n\t"
    "ds_read_b64_tr_b16 v[12:13], v245 offset:32768\n\t"
    "ds_read_b64_tr_b16 v[14:15], v245 offset:36864\n\t"
    "v_mfma_f32_16x16x32_bf16 v[212:215], v[28:31], v[16:19], v[212:215]\n\t"
    "ds_read_b64_tr_b16 v[24:25], v246 offset:32768\n\t"
    "s_add_u32 s63, s74, 143360\n\t"
    "buffer_load_dwordx4 v[140:143], v252, s[40:43], s63 offen offset:1024 sc1\n\t"
    "v_mfma_f32_16x16x32_bf16 v[220:223], v[28:31], v[20:23], v[220:223]\n\t"
    "ds_read_b64_tr_b16 v[26:27], v246 offset:36864\n\t"
    "v_mfma_f32_16x16x32_bf16 v[224:227], v[40:43], v[16:19], v[224:227]\n\t"
    "ds_read_b64_tr_b16 v[28:29], v247 offset:32768\n\t"
    "v_mfma_f32_16x16x32_bf16 v[232:235], v[40:43], v[20:23], v[232:235]\n\t"
    "ds_read_b64_tr_b16 v[30:31], v247 offset:36864\n\t"
    "s_add_u32 m0, s61, 131072\n\t"
    "s_add_u32 s63, s75, 3145728\n\t"
    "buffer_load_dwordx4 v255, s[36:39], s63 offen lds\n\t"
    "v_mfma_f32_16x16x32_bf16 v[228:231], v[44:47], v[16:19], v[228:231]\n\t"
    "ds_read_b64_tr_b16 v[40:41], v244 offset:33792\n\t"
    "v_mfma_f32_16x16x32_bf16 v[236:239], v[44:47], v[20:23], v[236:239]\n\t"
    "ds_read_b64_tr_b16 v[42:43], v244 offset:37888\n\t"
    "v_mfma_f32_16x16x32_bf16 v[176:179], v[56:59], v[16:19], v[176:179]\n\t"
    "ds_read_b64_tr_b16 v[44:45], v245 offset:33792\n\t"
    "s_add_u32 s63, s74, 143360\n\t"
    "buffer_load_dwordx4 v[128:131], v252, s[40:43], s63 offen offset:2048 sc1\n\t"
    "v_mfma_f32_16x16x32_bf16 v[184:187], v[56:59], v[20:23], v[184:187]\n\t"
    "ds_read_b64_tr_b16 v[46:47], v245 offset:37888\n\t"
    "s_add_u32 m0, s61, 132096\n\t"
    "s_add_u32 s63, s76, 3145728\n\t"
    "buffer_load_dwordx4 v255, s[36:39], s63 offen lds\n\t"
    "v_mfma_f32_16x16x32_bf16 v[180:183], v[60:63], v[16:19], v[180:183]\n\t"
    "ds_read_b64_tr_b16 v[56:57], v246 offset:33792\n\t"
    "v_mfma_f32_16x16x32_bf16 v[188:191], v[60:63], v[20:23], v[188:191]\n\t"
    "ds_read_b64_tr_b16 v[58:59], v246 offset:37888\n\t"
    "s_waitcnt lgkmcnt(10)\n\t"
    "v_mfma_f32_16x16x32_bf16 v[192:195], v[8:11], v[32:35], v[192:195]\n\t"
    "ds_read_b64_tr_b16 v[60:61], v247 offset:33792\n\t"
    "v_mfma_f32_16x16x32_bf16 v[200:203], v[8:11], v[36:39], v[200:203]\n\t"
    "ds_read_b64_tr_b16 v[62:63], v247 offset:37888\n\t"
    "s_add_u32 m0, s61, 147456\n\t"
    "s_add_u32 s63, s75, 3407872\n\t"
    "buffer_load_dwordx4 v255, s[36:39], s63 offen lds\n\t"
    "v_mfma_f32_16x16x32_bf16 v[196:199], v[12:15], v[32:35], v[196:199]\n\t"
    "ds_read_b64_tr_b16 v[8:9], v244 offset:40960\n\t"
    "s_setprio 2\n\t"
    "v_mfma_f32_16x16x32_bf16 v[204:207], v[12:15], v[36:39], v[204:207]\n\t"
    "ds_read_b64_tr_b16 v[10:11], v244 offset:45056\n\t"
    "s_add_u32 s63, s74, 143360\n\t"
    "buffer_load_dwordx4 v[132:135], v252, s[40:43], s63 offen offset:3072 sc1\n\t"
    "s_waitcnt lgkmcnt(10)\n\t"
    "v_mfma_f32_16x16x32_bf16 v[208:211], v[24:27], v[32:35], v[208:211]\n\t"
    "ds_read_b64_tr_b16 v[12:13], v245 offset:40960\n\t"
    "v_mfma_f32_16x16x32_bf16 v[216:219], v[24:27], v[36:39], v[216:219]\n\t"
    "ds_read_b64_tr_b16 v[14:15], v245 offset:45056\n\t"
    "s_add_u32 m0, s61, 148480\n\t"
    "s_add_u32 s63, s76, 3407872\n\t"
    "buffer_load_dwordx4 v255, s[36:39], s63 offen lds\n\t"
    "v_mfma_f32_16x16x32_bf16 v[212:215], v[28:31], v[32:35], v[212:215]\n\t"
    "ds_read_b64_tr_b16 v[24:25], v246 offset:40960\n\t"
    "v_mfma_f32_16x16x32_bf16 v[220:223], v[28:31], v[36:39], v[220:223]\n\t"
    "ds_read_b64_tr_b16 v[26:27], v246 offset:45056\n\t"
    "s_waitcnt lgkmcnt(10)\n\t"
    "v_mfma_f32_16x16x32_bf16 v[224:227], v[40:43], v[32:35], v[224:227]\n\t"
    "ds_read_b64_tr_b16 v[28:29], v247 offset:40960\n\t"
    "s_lshr_b32 s70, s77, 2\n\t"
    "s_cmp_eq_u32 %[bump], 1073741824\n\t"
    "s_cselect_b32 s65, 0, 32\n\t"
    "s_cselect_b32 s70, s70, 0\n\t"
    "v_and_b32 v126, 0x100, v252\n\t"
    "v_mul_u32_u24 v126, s65, v126\n\t"
    "s_or_b32 s65, s65, 0xd0\n\t"
    "v_and_b32 v125, s65, v252\n\t"
    "v_or3_b32 v125, v125, v126, s70\n\t"
    "v_mfma_f32_16x16x32_bf16 v[232:235], v[40:43], v[36:39], v[232:235]\n\t"
    "ds_read_b64_tr_b16 v[30:31], v247 offset:45056\n\t"
    "s_add_u32 s63, s74, 208896\n\t"
    "buffer_load_dwordx4 v[64:67], v252, s[40:43], s63 offen sc1\n\t"
    "v_mfma_f32_16x16x32_bf16 v[228:231], v[44:47], v[32:35], v[228:231]\n\t"
    "ds_read_b64_tr_b16 v[40:41], v244 offset:41984\n\t"
    "s_setprio 1\n\t"
    "v_mfma_f32_16x16x32_bf16 v[236:239], v[44:47], v[36:39], v[236:239]\n\t"
    "ds_read_b64_tr_b16 v[42:43], v244 offset:46080\n\t"
    "s_waitcnt lgkmcnt(10)\n\t"
    "v_mfma_f32_16x16x32_bf16 v[176:179], v[56:59], v[32:35], v[176:179]\n\t"
    "ds_read_b64_tr_b16 v[44:45], v245 offset:41984\n\t"
    "v_mfma_f32_16x16x32_bf16 v[184:187], v[56:59], v[36:39], v[184:187]\n\t"
    "ds_read_b64_tr_b16 v[46:47], v245 offset:46080\n\t"
    "v_mfma_f32_16x16x32_bf16 v[180:183], v[60:63], v[32:35], v[180:183]\n\t"
    "ds_read_b64_tr_b16 v[56:57], v246 offset:41984\n\t"
    "s_add_u32 s63, s74, 208896\n\t"
    "buffer_load_dwordx4 v[68:71], v252, s[40:43], s63 offen offset:1024 sc1\n\t"
    "v_mfma_f32_16x16x32_bf16 v[188:191], v[60:63], v[36:39], v[188:191]\n\t"
    "ds_read_b64_tr_b16 v[58:59], v246 offset:46080\n\t"
    "s_waitcnt lgkmcnt(10)\n\t"
    "v_mfma_f32_16x16x32_bf16 v[192:195], v[8:11], v[48:51], v[192:195]\n\t"
    "ds_read_b64_tr_b16 v[60:61], v247 offset:41984\n\t"
    "s_setprio 0\n\t"
    "v_mfma_f32_16x16x32_bf16 v[200:203], v[8:11], v[52:55], v[200:203]\n\t"
    "ds_read_b64_tr_b16 v[62:63], v247 offset:46080\n\t"
    "v_mov_b32 v126, s72\n\t"
    "v_mov_b32 v127, s73\n\t"
    "v_mfma_f32_16x16x32_bf16 v[196:199], v[12:15], v[48:51], v[196:199]\n\t"
    "v_mfma_f32_16x16x32_bf16 v[204:207], v[12:15], v[52:55], v[204:207]\n\t"
    "s_waitcnt lgkmcnt(8)\n\t"
    "v_mfma_f32_16x16x32_bf16 v[208:211], v[24:27], v[48:51], v[208:211]\n\t"
    "s_add_u32 s63, s74, 208896\n\t"
    "buffer_load_dwordx4 v[152:155], v252, s[40:43], s63 offen offset:2048 sc1\n\t"
    "v_mfma_f32_16x16x32_bf16 v[216:219], v[24:27], v[52:55], v[216:219]\n\t"
    "s_waitcnt lgkmcnt(0)\n\t"
    "s_barrier\n\t"
    "s_setprio 3\n\t"
    "ds_read_b64_tr_b16 v[8:9], v244 offset:0\n\t"
    "ds_read_b64_tr_b16 v[10:11], v244 offset:4096\n\t"
    "ds_read_b64_tr_b16 v[12:13], v245 offset:0\n\t"
    "ds_read_b64_tr_b16 v[14:15], v245 offset:4096\n\t"
    "v_mfma_f32_16x16x32_bf16 v[212:215], v[28:31], v[48:51], v[212:215]\n\t"
    "ds_read_b64_tr_b16 v[24:25], v246 offset:0\n\t"
    "v_mfma_f32_16x16x32_bf16 v[220:223], v[28:31], v[52:55], v[220:223]\n\t"
    "ds_read_b64_tr_b16 v[26:27], v246 offset:4096\n\t"
    "v_mfma_f32_16x16x32_bf16 v[224:227], v[40:43], v[48:51], v[224:227]\n\t"
    "ds_read_b64_tr_b16 v[28:29], v247 offset:0\n\t"
    "s_add_u32 s63, %[pollfl], 4096\n\t"
    "buffer_load_dwordx4 v[108:111], v125, s[56:59], s63 offen sc1\n\t"
    "v_mfma_f32_16x16x32_bf16 v[232:235], v[40:43], v[52:55], v[232:235]\n\t"
    "ds_read_b64_tr_b16 v[30:31], v247 offset:4096\n\t"
    "s_add_u32 m0, s61, 32768\n\t"
    "s_add_u32 s63, s75, 3211264\n\t"
    "buffer_load_dwordx4 v255, s[36:39], s63 offen lds\n\t"
    "v_mfma_f32_16x16x32_bf16 v[228:231], v[44:47], v[48:51], v[228:231]\n\t"
    "ds_read_b64_tr_b16 v[40:41], v244 offset:1024\n\t"
    "s_add_u32 s63, s74, 208896\n\t"
    "buffer_load_dwordx4 v[156:159], v252, s[40:43], s63 offen offset:3072 sc1\n\t"
    "v_mfma_f32_16x16x32_bf16 v[236:239], v[44:47], v[52:55], v[236:239]\n\t"
    "ds_read_b64_tr_b16 v[42:43], v244 offset:5120\n\t"
    "v_mfma_f32_16x16x32_bf16 v[176:179], v[56:59], v[48:51], v[176:179]\n\t"
    "ds_read_b64_tr_b16 v[44:45], v245 offset:1024\n\t"
    "v_mfma_f32_16x16x32_bf16 v[184:187], v[56:59], v[52:55], v[184:187]\n\t"
    "ds_read_b64_tr_b16 v[46:47], v245 offset:5120\n\t"
    "s_add_u32 m0, s61, 33792\n\t"
    "s_add_u32 s63, s76, 3211264\n\t"
    "buffer_load_dwordx4 v255, s[36:39], s63 offen lds\n\t"
    "v_mfma_f32_16x16x32_bf16 v[180:183], v[60:63], v[48:51], v[180:183]\n\t"
    "ds_read_b64_tr_b16 v[56:57], v246 offset:1024\n\t"
    "v_mfma_f32_16x16x32_bf16 v[188:191], v[60:63], v[52:55], v[188:191]\n\t"
    "ds_read_b64_tr_b16 v[58:59], v246 offset:5120\n\t"
    "s_waitcnt vmcnt(23) lgkmcnt(10)\n\t"
    "v_mfma_f32_16x16x32_bf16 v[192:195], v[8:11], v[72:75], v[192:195]\n\t"
    "ds_read_b64_tr_b16 v[60:61], v247 offset:1024\n\t"
    "s_add_u32 s63, s74, 212992\n\t"
    "buffer_load_dwordx4 v[80:83], v252, s[40:43], s63 offen sc1\n\t"
    "s_waitcnt vmcnt(21)\n\t"
    "v_mfma_f32_16x16x32_bf16 v[200:203], v[8:11], v[76:79], v[200:203]\n\t"
    "ds_read_b64_tr_b16 v[62:63], v247 offset:5120\n\t"
    "s_add_u32 m0, s61, 49152\n\t"
    "s_add_u32 s63, s75, 3473408\n\t"
    "buffer_load_dwordx4 v255, s[36:39], s63 offen lds\n\t"
    "v_mfma_f32_16x16x32_bf16 v[196:199], v[12:15], v[72:75], v[196:199]\n\t"
    "ds_read_b64_tr_b16 v[8:9], v244 offset:8192\n\t"
    "s_setprio 2\n\t"
    "v_mfma_f32_16x16x32_bf16 v[204:207], v[12:15], v[76:79], v[204:207]\n\t"
    "ds_read_b64_tr_b16 v[10:11], v244 offset:12288\n\t"
    "s_waitcnt lgkmcnt(10)\n\t"
    "v_mfma_f32_16x16x32_bf16 v[208:211], v[24:27], v[72:75], v[208:211]\n\t"
    "ds_read_b64_tr_b16 v[12:13], v245 offset:8192\n\t"
    "v_mfma_f32_16x16x32_bf16 v[216:219], v[24:27], v[76:79], v[216:219]\n\t"
    "ds_read_b64_tr_b16 v[14:15], v245 offset:12288\n\t"
    "s_add_u32 m0, s61, 50176\n\t"
    "s_add_u32 s63, s76, 3473408\n\t"
    "buffer_load_dwordx4 v255, s[36:39], s63 offen lds\n\t"
    "v_mfma_f32_16x16x32_bf16 v[212:215], v[28:31], v[72:75], v[212:215]\n\t"
    "ds_read_b64_tr_b16 v[24:25], v246 offset:8192\n\t"
    "s_add_u32 s63, s74, 212992\n\t"
    "buffer_load_dwordx4 v[84:87], v252, s[40:43], s63 offen offset:1024 sc1\n\t"
    "v_mfma_f32_16x16x32_bf16 v[220:223], v[28:31], v[76:79], v[220:223]\n\t"
    "ds_read_b64_tr_b16 v[26:27], v246 offset:12288\n\t"
    "s_waitcnt lgkmcnt(10)\n\t"
    "v_mfma_f32_16x16x32_bf16 v[224:227], v[40:43], v[72:75], v[224:227]\n\t"
    "ds_read_b64_tr_b16 v[28:29], v247 offset:8192\n\t"
    "v_mfma_f32_16x16x32_bf16 v[232:235], v[40:43], v[76:79], v[232:235]\n\t"
    "ds_read_b64_tr_b16 v[30:31], v247 offset:12288\n\t"
    "v_mfma_f32_16x16x32_bf16 v[228:231], v[44:47], v[72:75], v[228:231]\n\t"
    "ds_read_b64_tr_b16 v[40:41], v244 offset:9216\n\t"
    "s_setprio 1\n\t"
    "v_mfma_f32_16x16x32_bf16 v[236:239], v[44:47], v[76:79], v[236:239]\n\t"
    "ds_read_b64_tr_b16 v[42:43], v244 offset:13312\n\t"
    "s_waitcnt lgkmcnt(10)\n\t"
    "v_mfma_f32_16x16x32_bf16 v[176:179], v[56:59], v[72:75], v[176:179]\n\t"
    "ds_read_b64_tr_b16 v[44:45], v245 offset:9216\n\t"
    "s_add_u32 s63, s74, 212992\n\t"
    "buffer_load_dwordx4 v[144:147], v252, s[40:43], s63 offen offset:2048 sc1\n\t"
    "v_mfma_f32_16x16x32_bf16 v[184:187], v[56:59], v[76:79], v[184:187]\n\t"
    "ds_read_b64_tr_b16 v[46:47], v245 offset:13312\n\t"
    "v_mfma_f32_16x16x32_bf16 v[180:183], v[60:63], v[72:75], v[180:183]\n\t"
    "ds_read_b64_tr_b16 v[56:57], v246 offset:9216\n\t"
    "v_mfma_f32_16x16x32_bf16 v[188:191], v[60:63], v[76:79], v[188:191]\n\t"
    "ds_read_b64_tr_b16 v[58:59], v246 offset:13312\n\t"
    "s_waitcnt vmcnt(23) lgkmcnt(10)\n\t"
    "v_mfma_f32_16x16x32_bf16 v[192:195], v[8:11], v[88:91], v[192:195]\n\t"
    "ds_read_b64_tr_b16 v[60:61], v247 offset:9216\n\t"
    "s_setprio 0\n\t"
    "s_waitcnt vmcnt(21)\n\t"
    "v_mfma_f32_16x16x32_bf16 v[200:203], v[8:11], v[92:95], v[200:203]\n\t"
    "ds_read_b64_tr_b16 v[62:63], v247 offset:13312\n\t"
    "v_mfma_f32_16x16x32_bf16 v[196:199], v[12:15], v[88:91], v[196:199]\n\t"
    "s_add_u32 s63, s74, 212992\n\t"
    "buffer_load_dwordx4 v[148:151], v252, s[40:43], s63 offen offset:3072 sc1\n\t"
    "v_mfma_f32_16x16x32_bf16 v[204:207], v[12:15], v[92:95], v[204:207]\n\t"
    "s_waitcnt lgkmcnt(8)\n\t"
    "v_mfma_f32_16x16x32_bf16 v[208:211], v[24:27], v[88:91], v[208:211]\n\t"
    "v_mfma_f32_16x16x32_bf16 v[216:219], v[24:27], v[92:95], v[216:219]\n\t"
    "s_waitcnt lgkmcnt(0)\n\t"
    "s_barrier\n\t"
    "s_setprio 3\n\t"
    "ds_read_b64_tr_b16 v[8:9], v240 offset:0\n\t"
    "ds_read_b64_tr_b16 v[10:11], v240 offset:4096\n\t"
    "ds_read_b64_tr_b16 v[12:13], v241 offset:0\n\t"
    "ds_read_b64_tr_b16 v[14:15], v241 offset:4096\n\t"
    "v_mfma_f32_16x16x32_bf16 v[212:215], v[28:31], v[88:91], v[212:215]\n\t"
    "ds_read_b64_tr_b16 v[24:25], v242 offset:0\n\t"
    "v_mfma_f32_16x16x32_bf16 v[220:223], v[28:31], v[92:95], v[220:223]\n\t"
    "ds_read_b64_tr_b16 v[26:27], v242 offset:4096\n\t"
    "v_mfma_f32_16x16x32_bf16 v[224:227], v[40:43], v[88:91], v[224:227]\n\t"
    "ds_read_b64_tr_b16 v[28:29], v243 offset:0\n\t"
    "s_waitcnt vmcnt(9)\n\t"
    "v_xor_b32 v108, v108, v126\n\t"
    "v_xor_b32 v109, v109, v127\n\t"
    "v_xor_b32 v110, v110, v126\n\t"
    "v_xor_b32 v111, v111, v127\n\t"
    "v_or3_b32 v108, v108, v109, v110\n\t"
    "v_or_b32 v108, v108, v111\n\t"
    "v_cmp_ne_u32 vcc, 0, v108\n\t"
    "s_nop 4\n\t"
    "s_cbranch_vccz PGO1_%=\n\t"
    "s_mov_b32 s70, 0x200000\n\t"
    "PSP1_%=:\n\t"
    "s_sleep 4\n\t"
    "s_add_u32 s63, %[pollfl], 4096\n\t"
    "buffer_load_dwordx4 v[108:111], v125, s[56:59], s63 offen sc1\n\t"
    "s_waitcnt vmcnt(0)\n\t"
    "PAD10_%=: s_cbranch_execz PADRET_%=\n\t"
    "v_xor_b32 v108, v108, v126\n\t"
    "v_xor_b32 v109, v109, v127\n\t"
    "v_xor_b32 v110, v110, v126\n\t"
    "v_xor_b32 v111, v111, v127\n\t"
    "v_or3_b32 v108, v108, v109, v110\n\t"
    "v_or_b32 v108, v108, v111\n\t"
    "v_cmp_ne_u32 vcc, 0, v108\n\t"
    "s_sub_u32 s70, s70, 1\n\t"
    "s_cbranch_scc1 PGO1_%=\n\t"
    "s_nop 4\n\t"
    "s_cbranch_vccnz PSP1_%=\n\t"
    "PGO1_%=:\n\t"
    "s_nop 1\n\t"
    "v_mfma_f32_16x16x32_bf16 v[232:235], v[40:43], v[92:95], v[232:235]\n\t"
    "ds_read_b64_tr_b16 v[30:31], v243 offset:4096\n\t"
    "s_add_u32 m0, s61, 98304\n\t"
    "s_add_u32 s63, s76, 131072\n\t"
    "buffer_load_dwordx4 v255, s[36:39], s63 offen lds\n\t"
    "v_mfma_f32_16x16x32_bf16 v[228:231], v[44:47], v[88:91], v[228:231]\n\t"
    "ds_read_b64_tr_b16 v[40:41], v240 offset:1024\n\t"
    "s_add_u32 s63, s74, 8192\n\t"
    "buffer_load_dwordx4 v[168:171], v252, s[40:43], s63 offen sc1\n\t"
    "v_mfma_f32_16x16x32_bf16 v[236:239], v[44:47], v[92:95], v[236:239]\n\t"
    "ds_read_b64_tr_b16 v[42:43], v240 offset:5120\n\t"
    "v_mfma_f32_16x16x32_bf16 v[176:179], v[56:59], v[88:91], v[176:179]\n\t"
    "ds_read_b64_tr_b16 v[44:45], v241 offset:1024\n\t"
    "v_mfma_f32_16x16x32_bf16 v[184:187], v[56:59], v[92:95], v[184:187]\n\t"
    "ds_read_b64_tr_b16 v[46:47], v241 offset:5120\n\t"
    "s_add_u32 m0, s61, 99328\n\t"
    "s_add_u32 s63, s76, 196608\n\t"
    "buffer_load_dwordx4 v255, s[36:39], s63 offen lds\n\t"
    "v_mfma_f32_16x16x32_bf16 v[180:183], v[60:63], v[88:91], v[180:183]\n\t"
    "ds_read_b64_tr_b16 v[56:57], v242 offset:1024\n\t"
    "v_mfma_f32_16x16x32_bf16 v[188:191], v[60:63], v[92:95], v[188:191]\n\t"
    "ds_read_b64_tr_b16 v[58:59], v242 offset:5120\n\t"
    "s_waitcnt lgkmcnt(10)\n\t"
    "v_mfma_f32_16x16x32_bf16 v[192:195], v[8:11], v[136:139], v[192:195]\n\t"
    "ds_read_b64_tr_b16 v[60:61], v243 offset:1024\n\t"
    "s_add_u32 s63, s74, 8192\n\t"
    "buffer_load_dwordx4 v[172:175], v252, s[40:43], s63 offen offset:1024 sc1\n\t"
    "v_mfma_f32_16x16x32_bf16 v[200:203], v[8:11], v[140:143], v[200:203]\n\t"
    "ds_read_b64_tr_b16 v[62:63], v243 offset:5120\n\t"
    "s_add_u32 m0, s61, 114688\n\t"
    "s_add_u32 s63, s76, 393216\n\t"
    "buffer_load_dwordx4 v255, s[36:39], s63 offen lds\n\t"
    "v_mfma_f32_16x16x32_bf16 v[196:199], v[12:15], v[136:139], v[196:199]\n\t"
    "ds_read_b64_tr_b16 v[8:9], v240 offset:8192\n\t"
    "s_setprio 2\n\t"
    "v_mfma_f32_16x16x32_bf16 v[204:207], v[12:15], v[140:143], v[204:207]\n\t"
    "ds_read_b64_tr_b16 v[10:11], v240 offset:12288\n\t"
    "s_waitcnt lgkmcnt(10)\n\t"
    "v_mfma_f32_16x16x32_bf16 v[208:211], v[24:27], v[136:139], v[208:211]\n\t"
    "ds_read_b64_tr_b16 v[12:13], v241 offset:8192\n\t"
    "v_mfma_f32_16x16x32_bf16 v[216:219], v[24:27], v[140:143], v[216:219]\n\t"
    "ds_read_b64_tr_b16 v[14:15], v241 offset:12288\n\t"
    "s_add_u32 m0, s61, 115712\n\t"
    "s_add_u32 s63, s76, 458752\n\t"
    "buffer_load_dwordx4 v255, s[36:39], s63 offen lds\n\t"
    "v_mfma_f32_16x16x32_bf16 v[212:215], v[28:31], v[136:139], v[212:215]\n\t"
    "ds_read_b64_tr_b16 v[24:25], v242 offset:8192\n\t"
    "s_add_u32 s63, s74, 8192\n\t"
    "buffer_load_dwordx4 v[160:163], v252, s[40:43], s63 offen offset:2048 sc1\n\t"
    "v_mfma_f32_16x16x32_bf16 v[220:223], v[28:31], v[140:143], v[220:223]\n\t"
    "ds_read_b64_tr_b16 v[26:27], v242 offset:12288\n\t"
    "s_waitcnt lgkmcnt(10)\n\t"
    "v_mfma_f32_16x16x32_bf16 v[224:227], v[40:43], v[136:139], v[224:227]\n\t"
    "ds_read_b64_tr_b16 v[28:29], v243 offset:8192\n\t"
    "v_mfma_f32_16x16x32_bf16 v[232:235], v[40:43], v[140:143], v[232:235]\n\t"
    "ds_read_b64_tr_b16 v[30:31], v243 offset:12288\n\t"
    "v_mfma_f32_16x16x32_bf16 v[228:231], v[44:47], v[136:139], v[228:231]\n\t"
    "ds_read_b64_tr_b16 v[40:41], v240 offset:9216\n\t"
    "s_setprio 1\n\t"
    "v_mfma_f32_16x16x32_bf16 v[236:239], v[44:47], v[140:143], v[236:239]\n\t"
    "ds_read_b64_tr_b16 v[42:43], v240 offset:13312\n\t"
    "s_waitcnt lgkmcnt(10)\n\t"
    "v_mfma_f32_16x16x32_bf16 v[176:179], v[56:59], v[136:139], v[176:179]\n\t"
    "ds_read_b64_tr_b16 v[44:45], v241 offset:9216\n\t"
    "s_add_u32 s63, s74, 8192\n\t"
    "buffer_load_dwordx4 v[164:167], v252, s[40:43], s63 offen offset:3072 sc1\n\t"
    "v_mfma_f32_16x16x32_bf16 v[184:187], v[56:59], v[140:143], v[184:187]\n\t"
    "ds_read_b64_tr_b16 v[46:47], v241 offset:13312\n\t"
    "v_mfma_f32_16x16x32_bf16 v[180:183], v[60:63], v[136:139], v[180:183]\n\t"
    "ds_read_b64_tr_b16 v[56:57], v242 offset:9216\n\t"
    "v_mfma_f32_16x16x32_bf16 v[188:191], v[60:63], v[140:143], v[188:191]\n\t"
    "ds_read_b64_tr_b16 v[58:59], v242 offset:13312\n\t"
    "s_waitcnt lgkmcnt(10)\n\t"
    "v_mfma_f32_16x16x32_bf16 v[192:195], v[8:11], v[128:131], v[192:195]\n\t"
    "ds_read_b64_tr_b16 v[60:61], v243 offset:9216\n\t"
    "s_setprio 0\n\t"
    "v_mfma_f32_16x16x32_bf16 v[200:203], v[8:11], v[132:135], v[200:203]\n\t"
    "ds_read_b64_tr_b16 v[62:63], v243 offset:13312\n\t"
    "v_mfma_f32_16x16x32_bf16 v[196:199], v[12:15], v[128:131], v[196:199]\n\t"
    "s_add_u32 s63, s74, 12288\n\t"
    "buffer_load_dwordx4 v[96:99], v252, s[40:43], s63 offen sc1\n\t"
    "v_mfma_f32_16x16x32_bf16 v[204:207], v[12:15], v[132:135], v[204:207]\n\t"
    "s_waitcnt lgkmcnt(8)\n\t"
    "v_mfma_f32_16x16x32_bf16 v[208:211], v[24:27], v[128:131], v[208:211]\n\t"
    "v_mfma_f32_16x16x32_bf16 v[216:219], v[24:27], v[132:135], v[216:219]\n\t"
    "s_waitcnt lgkmcnt(0)\n\t"
    "s_barrier\n\t"
    "s_setprio 3\n\t"
    "ds_read_b64_tr_b16 v[8:9], v104 offset:0\n\t"
    "ds_read_b64_tr_b16 v[10:11], v104 offset:4096\n\t"
    "ds_read_b64_tr_b16 v[12:13], v105 offset:0\n\t"
    "ds_read_b64_tr_b16 v[14:15], v105 offset:4096\n\t"
    "v_mfma_f32_16x16x32_bf16 v[212:215], v[28:31], v[128:131], v[212:215]\n\t"
    "ds_read_b64_tr_b16 v[24:25], v106 offset:0\n\t"
    "v_mfma_f32_16x16x32_bf16 v[220:223], v[28:31], v[132:135], v[220:223]\n\t"
    "ds_read_b64_tr_b16 v[26:27], v106 offset:4096\n\t"
    "v_mfma_f32_16x16x32_bf16 v[224:227], v[40:43], v[128:131], v[224:227]\n\t"
    "ds_read_b64_tr_b16 v[28:29], v107 offset:0\n\t"
    "s_add_u32 s63, s74, 12288\n\t"
    "buffer_load_dwordx4 v[100:103], v252, s[40:43], s63 offen offset:1024 sc1\n\t"
    "v_mfma_f32_16x16x32_bf16 v[232:235], v[40:43], v[132:135], v[232:235]\n\t"
    "ds_read_b64_tr_b16 v[30:31], v107 offset:4096\n\t"
    "s_add_u32 m0, s61, 65536\n\t"
    "s_add_u32 s63, s76, 1179648\n\t"
    "buffer_load_dwordx4 v255, s[36:39], s63 offen lds\n\t"
    "v_mfma_f32_16x16x32_bf16 v[228:231], v[44:47], v[128:131], v[228:231]\n\t"
    "ds_read_b64_tr_b16 v[40:41], v104 offset:1024\n\t"
    "v_mfma_f32_16x16x32_bf16 v[236:239], v[44:47], v[132:135], v[236:239]\n\t"
    "ds_read_b64_tr_b16 v[42:43], v104 offset:5120\n\t"
    "v_mfma_f32_16x16x32_bf16 v[176:179], v[56:59], v[128:131], v[176:179]\n\t"
    "ds_read_b64_tr_b16 v[44:45], v105 offset:1024\n\t"
    "v_mfma_f32_16x16x32_bf16 v[184:187], v[56:59], v[132:135], v[184:187]\n\t"
    "ds_read_b64_tr_b16 v[46:47], v105 offset:5120\n\t"
    "s_add_u32 m0, s61, 66560\n\t"
    "s_add_u32 s63, s76, 1245184\n\t"
    "buffer_load_dwordx4 v255, s[36:39], s63 offen lds\n\t"
    "v_mfma_f32_16x16x32_bf16 v[180:183], v[60:63], v[128:131], v[180:183]\n\t"
    "ds_read_b64_tr_b16 v[56:57], v106 offset:1024\n\t"
    "s_add_u32 s63, s74, 12288\n\t"
    "buffer_load_dwordx4 v[112:115], v252, s[40:43], s63 offen offset:2048 sc1\n\t"
    "v_mfma_f32_16x16x32_bf16 v[188:191], v[60:63], v[132:135], v[188:191]\n\t"
    "ds_read_b64_tr_b16 v[58:59], v106 offset:5120\n\t"
    "s_waitcnt lgkmcnt(10)\n\t"
    "v_mfma_f32_16x16x32_bf16 v[192:195], v[8:11], v[64:67], v[192:195]\n\t"
    "ds_read_b64_tr_b16 v[60:61], v107 offset:1024\n\t"
    "v_mfma_f32_16x16x32_bf16 v[200:203], v[8:11], v[68:71], v[200:203]\n\t"
    "ds_read_b64_tr_b16 v[62:63], v107 offset:5120\n\t"
    "s_add_u32 m0, s61, 81920\n\t"
    "s_add_u32 s63, s76, 1441792\n\t"
    "buffer_load_dwordx4 v255, s[36:39], s63 offen lds\n\t"
    "v_mfma_f32_16x16x32_bf16 v[196:199], v[12:15], v[64:67], v[196:199]\n\t"
    "ds_read_b64_tr_b16 v[8:9], v104 offset:8192\n\t"
    "s_setprio 2\n\t"
    "v_mfma_f32_16x16x32_bf16 v[204:207], v[12:15], v[68:71], v[204:207]\n\t"
    "ds_read_b64_tr_b16 v[10:11], v104 offset:12288\n\t"
    "s_waitcnt lgkmcnt(10)\n\t"
    "v_mfma_f32_16x16x32_bf16 v[208:211], v[24:27], v[64:67], v[208:211]\n\t"
    "ds_read_b64_tr_b16 v[12:13], v105 offset:8192\n\t"
    "s_add_u32 s63, s74, 12288\n\t"
    "buffer_load_dwordx4 v[116:119], v252, s[40:43], s63 offen offset:3072 sc1\n\t"
    "v_mfma_f32_16x16x32_bf16 v[216:219], v[24:27], v[68:71], v[216:219]\n\t"
    "ds_read_b64_tr_b16 v[14:15], v105 offset:12288\n\t"
    "s_add_u32 m0, s61, 82944\n\t"
    "s_add_u32 s63, s76, 1507328\n\t"
    "buffer_load_dwordx4 v255, s[36:39], s63 offen lds\n\t"
    "v_mfma_f32_16x16x32_bf16 v[212:215], v[28:31], v[64:67], v[212:215]\n\t"
    "ds_read_b64_tr_b16 v[24:25], v106 offset:8192\n\t"
    "v_mfma_f32_16x16x32_bf16 v[220:223], v[28:31], v[68:71], v[220:223]\n\t"
    "ds_read_b64_tr_b16 v[26:27], v106 offset:12288\n\t"
    "s_waitcnt lgkmcnt(10)\n\t"
    "v_mfma_f32_16x16x32_bf16 v[224:227], v[40:43], v[64:67], v[224:227]\n\t"
    "ds_read_b64_tr_b16 v[28:29], v107 offset:8192\n\t"
    "v_mfma_f32_16x16x32_bf16 v[232:235], v[40:43], v[68:71], v[232:235]\n\t"
    "ds_read_b64_tr_b16 v[30:31], v107 offset:12288\n\t"
    "v_mfma_f32_16x16x32_bf16 v[228:231], v[44:47], v[64:67], v[228:231]\n\t"
    "ds_read_b64_tr_b16 v[40:41], v104 offset:9216\n\t"
    "s_setprio 1\n\t"
    "v_mfma_f32_16x16x32_bf16 v[236:239], v[44:47], v[68:71], v[236:239]\n\t"
    "ds_read_b64_tr_b16 v[42:43], v104 offset:13312\n\t"
    "s_add_u32 s63, s74, 77824\n\t"
    "buffer_load_dwordx4 v[0:3], v252, s[40:43], s63 offen sc1\n\t"
    "s_waitcnt lgkmcnt(10)\n\t"
    "v_mfma_f32_16x16x32_bf16 v[176:179], v[56:59], v[64:67], v[176:179]\n\t"
    "ds_read_b64_tr_b16 v[44:45], v105 offset:9216\n\t"
    "v_mfma_f32_16x16x32_bf16 v[184:187], v[56:59], v[68:71], v[184:187]\n\t"
    "ds_read_b64_tr_b16 v[46:47], v105 offset:13312\n\t"
    "v_mfma_f32_16x16x32_bf16 v[180:183], v[60:63], v[64:67], v[180:183]\n\t"
    "ds_read_b64_tr_b16 v[56:57], v106 offset:9216\n\t"
    "v_mfma_f32_16x16x32_bf16 v[188:191], v[60:63], v[68:71], v[188:191]\n\t"
    "ds_read_b64_tr_b16 v[58:59], v106 offset:13312\n\t"
    "s_waitcnt lgkmcnt(10)\n\t"
    "v_mfma_f32_16x16x32_bf16 v[192:195], v[8:11], v[152:155], v[192:195]\n\t"
    "ds_read_b64_tr_b16 v[60:61], v107 offset:9216\n\t"
    "s_setprio 0\n\t"
    "s_waitcnt vmcnt(24)\n\t"
    "v_mfma_f32_16x16x32_bf16 v[200:203], v[8:11], v[156:159], v[200:203]\n\t"
    "ds_read_b64_tr_b16 v[62:63], v107 offset:13312\n\t"
    "s_add_u32 s63, s74, 77824\n\t"
    "buffer_load_dwordx4 v[4:7], v252, s[40:43], s63 offen offset:1024 sc1\n\t"
    "v_mfma_f32_16x16x32_bf16 v[196:199], v[12:15], v[152:155], v[196:199]\n\t"
    "v_mfma_f32_16x16x32_bf16 v[204:207], v[12:15], v[156:159], v[204:207]\n\t"
    "s_waitcnt lgkmcnt(8)\n\t"
    "v_mfma_f32_16x16x32_bf16 v[208:211], v[24:27], v[152:155], v[208:211]\n\t"
    "v_mfma_f32_16x16x32_bf16 v[216:219], v[24:27], v[156:159], v[216:219]\n\t"
    "s_waitcnt vmcnt(21) lgkmcnt(0)\n\t"
    "s_barrier\n\t"
    "s_setprio 3\n\t"
    "ds_read_b64_tr_b16 v[8:9], v240 offset:32768\n\t"
    "ds_read_b64_tr_b16 v[10:11], v240 offset:36864\n\t"
    "ds_read_b64_tr_b16 v[12:13], v241 offset:32768\n\t"
    "ds_read_b64_tr_b16 v[14:15], v241 offset:36864\n\t"
    "v_mfma_f32_16x16x32_bf16 v[212:215], v[28:31], v[152:155], v[212:215]\n\t"
    "ds_read_b64_tr_b16 v[24:25], v242 offset:32768\n\t"
    "s_add_u32 s63, s74, 77824\n\t"
    "buffer_load_dwordx4 v[16:19], v252, s[40:43], s63 offen offset:2048 sc1\n\t"
    "v_mfma_f32_16x16x32_bf16 v[220:223], v[28:31], v[156:159], v[220:223]\n\t"
    "ds_read_b64_tr_b16 v[26:27], v242 offset:36864\n\t"
    "v_mfma_f32_16x16x32_bf16 v[224:227], v[40:43], v[152:155], v[224:227]\n\t"
    "ds_read_b64_tr_b16 v[28:29], v243 offset:32768\n\t"
    "v_mfma_f32_16x16x32_bf16 v[232:235], v[40:43], v[156:159], v[232:235]\n\t"
    "ds_read_b64_tr_b16 v[30:31], v243 offset:36864\n\t"
    "s_add_u32 m0, s61, 0\n\t"
    "s_add_u32 s63, s76, 2228224\n\t"
    "buffer_load_dwordx4 v255, s[36:39], s63 offen lds\n\t"
    "v_mfma_f32_16x16x32_bf16 v[228:231], v[44:47], v[152:155], v[228:231]\n\t"
    "ds_read_b64_tr_b16 v[40:41], v240 offset:33792\n\t"
    "v_mfma_f32_16x16x32_bf16 v[236:239], v[44:47], v[156:159], v[236:239]\n\t"
    "ds_read_b64_tr_b16 v[42:43], v240 offset:37888\n\t"
    "v_mfma_f32_16x16x32_bf16 v[176:179], v[56:59], v[152:155], v[176:179]\n\t"
    "ds_read_b64_tr_b16 v[44:45], v241 offset:33792\n\t"
    "s_add_u32 s63, s74, 77824\n\t"
    "buffer_load_dwordx4 v[20:23], v252, s[40:43], s63 offen offset:3072 sc1\n\t"
    "v_mfma_f32_16x16x32_bf16 v[184:187], v[56:59], v[156:159], v[184:187]\n\t"
    "ds_read_b64_tr_b16 v[46:47], v241 offset:37888\n\t"
    "s_add_u32 m0, s61, 1024\n\t"
    "s_add_u32 s63, s76, 2293760\n\t"
    "buffer_load_dwordx4 v255, s[36:39], s63 offen lds\n\t"
    "v_mfma_f32_16x16x32_bf16 v[180:183], v[60:63], v[152:155], v[180:183]\n\t"
    "ds_read_b64_tr_b16 v[56:57], v242 offset:33792\n\t"
    "v_mfma_f32_16x16x32_bf16 v[188:191], v[60:63], v[156:159], v[188:191]\n\t"
    "ds_read_b64_tr_b16 v[58:59], v242 offset:37888\n\t"
    "s_waitcnt lgkmcnt(10)\n\t"
    "v_mfma_f32_16x16x32_bf16 v[192:195], v[8:11], v[80:83], v[192:195]\n\t"
    "ds_read_b64_tr_b16 v[60:61], v243 offset:33792\n\t"
    "s_waitcnt vmcnt(24)\n\t"
    "v_mfma_f32_16x16x32_bf16 v[200:203], v[8:11], v[84:87], v[200:203]\n\t"
    "ds_read_b64_tr_b16 v[62:63], v243 offset:37888\n\t"
    "s_add_u32 m0, s61, 16384\n\t"
    "s_add_u32 s63, s76, 2490368\n\t"
    "buffer_load_dwordx4 v255, s[36:39], s63 offen lds\n\t"
    "v_mfma_f32_16x16x32_bf16 v[196:199], v[12:15], v[80:83], v[196:199]\n\t"
    "ds_read_b64_tr_b16 v[8:9], v240 offset:40960\n\t"
    "s_setprio 2\n\t"
    "v_mfma_f32_16x16x32_bf16 v[204:207], v[12:15], v[84:87], v[204:207]\n\t"
    "ds_read_b64_tr_b16 v[10:11], v240 offset:45056\n\t"
    "s_add_u32 s63, s74, 81920\n\t"
    "buffer_load_dwordx4 v[32:35], v252, s[40:43], s63 offen sc1\n\t"
    "s_waitcnt lgkmcnt(10)\n\t"
    "v_mfma_f32_16x16x32_bf16 v[208:211], v[24:27], v[80:83], v[208:211]\n\t"
    "ds_read_b64_tr_b16 v[12:13], v241 offset:40960\n\t"
    "v_mfma_f32_16x16x32_bf16 v[216:219], v[24:27], v[84:87], v[216:219]\n\t"
    "ds_read_b64_tr_b16 v[14:15], v241 offset:45056\n\t"
    "s_add_u32 m0, s61, 17408\n\t"
    "s_add_u32 s63, s76, 2555904\n\t"
    "buffer_load_dwordx4 v255, s[36:39], s63 offen lds\n\t"
    "v_mfma_f32_16x16x32_bf16 v[212:215], v[28:31], v[80:83], v[212:215]\n\t"
    "ds_read_b64_tr_b16 v[24:25], v242 offset:40960\n\t"
    "v_mfma_f32_16x16x32_bf16 v[220:223], v[28:31], v[84:87], v[220:223]\n\t"
    "ds_read_b64_tr_b16 v[26:27], v242 offset:45056\n\t"
    "s_waitcnt lgkmcnt(10)\n\t"
    "v_mfma_f32_16x16x32_bf16 v[224:227], v[40:43], v[80:83], v[224:227]\n\t"
    "ds_read_b64_tr_b16 v[28:29], v243 offset:40960\n\t"
    "s_add_u32 s63, s74, 81920\n\t"
    "buffer_load_dwordx4 v[36:39], v252, s[40:43], s63 offen offset:1024 sc1\n\t"
    "v_mfma_f32_16x16x32_bf16 v[232:235], v[40:43], v[84:87], v[232:235]\n\t"
    "ds_read_b64_tr_b16 v[30:31], v243 offset:45056\n\t"
    "v_mfma_f32_16x16x32_bf16 v[228:231], v[44:47], v[80:83], v[228:231]\n\t"
    "ds_read_b64_tr_b16 v[40:41], v240 offset:41984\n\t"
    "s_setprio 1\n\t"
    "v_mfma_f32_16x16x32_bf16 v[236:239], v[44:47], v[84:87], v[236:239]\n\t"
    "ds_read_b64_tr_b16 v[42:43], v240 offset:46080\n\t"
    "s_waitcnt lgkmcnt(10)\n\t"
    "v_mfma_f32_16x16x32_bf16 v[176:179], v[56:59], v[80:83], v[176:179]\n\t"
    "ds_read_b64_tr_b16 v[44:45], v241 offset:41984\n\t"
    "v_mfma_f32_16x16x32_bf16 v[184:187], v[56:59], v[84:87], v[184:187]\n\t"
    "ds_read_b64_tr_b16 v[46:47], v241 offset:46080\n\t"
    "v_mfma_f32_16x16x32_bf16 v[180:183], v[60:63], v[80:83], v[180:183]\n\t"
    "ds_read_b64_tr_b16 v[56:57], v242 offset:41984\n\t"
    "s_add_u32 s63, s74, 81920\n\t"
    "buffer_load_dwordx4 v[48:51], v252, s[40:43], s63 offen offset:2048 sc1\n\t"
    "v_mfma_f32_16x16x32_bf16 v[188:191], v[60:63], v[84:87], v[188:191]\n\t"
    "ds_read_b64_tr_b16 v[58:59], v242 offset:46080\n\t"
    "s_waitcnt vmcnt(28) lgkmcnt(10)\n\t"
    "v_mfma_f32_16x16x32_bf16 v[192:195], v[8:11], v[144:147], v[192:195]\n\t"
    "ds_read_b64_tr_b16 v[60:61], v243 offset:41984\n\t"
    "s_setprio 0\n\t"
    "s_waitcnt vmcnt(27)\n\t"
    "v_mfma_f32_16x16x32_bf16 v[200:203], v[8:11], v[148:151], v[200:203]\n\t"
    "ds_read_b64_tr_b16 v[62:63], v243 offset:46080\n\t"
    "v_mfma_f32_16x16x32_bf16 v[196:199], v[12:15], v[144:147], v[196:199]\n\t"
    "v_mfma_f32_16x16x32_bf16 v[204:207], v[12:15], v[148:151], v[204:207]\n\t"
    "s_waitcnt lgkmcnt(8)\n\t"
    "v_mfma_f32_16x16x32_bf16 v[208:211], v[24:27], v[144:147], v[208:211]\n\t"
    "s_add_u32 s63, s74, 81920\n\t"
    "buffer_load_dwordx4 v[52:55], v252, s[40:43], s63 offen offset:3072 sc1\n\t"
    "v_mfma_f32_16x16x32_bf16 v[216:219], v[24:27], v[148:151], v[216:219]\n\t"
    "s_waitcnt vmcnt(22) lgkmcnt(0)\n\t"
    "s_barrier\n\t"
    "s_setprio 3\n\t"
    "ds_read_b64_tr_b16 v[8:9], v244 offset:32768\n\t"
    "ds_read_b64_tr_b16 v[10:11], v244 offset:36864\n\t"
    "ds_read_b64_tr_b16 v[12:13], v245 offset:32768\n\t"
    "ds_read_b64_tr_b16 v[14:15], v245 offset:36864\n\t"
    "v_mfma_f32_16x16x32_bf16 v[212:215], v[28:31], v[144:147], v[212:215]\n\t"
    "ds_read_b64_tr_b16 v[24:25], v246 offset:32768\n\t"
    "v_mfma_f32_16x16x32_bf16 v[220:223], v[28:31], v[148:151], v[220:223]\n\t"
    "ds_read_b64_tr_b16 v[26:27], v246 offset:36864\n\t"
    "v_mfma_f32_16x16x32_bf16 v[224:227], v[40:43], v[144:147], v[224:227]\n\t"
    "ds_read_b64_tr_b16 v[28:29], v247 offset:32768\n\t"
    "v_mfma_f32_16x16x32_bf16 v[232:235], v[40:43], v[148:151], v[232:235]\n\t"
    "ds_read_b64_tr_b16 v[30:31], v247 offset:36864\n\t"
    "s_add_u32 m0, s61, 131072\n\t"
    "s_add_u32 s63, s76, 3276800\n\t"
    "buffer_load_dwordx4 v255, s[36:39], s63 offen lds\n\t"
    "v_mfma_f32_16x16x32_bf16 v[228:231], v[44:47], v[144:147], v[228:231]\n\t"
    "ds_read_b64_tr_b16 v[40:41], v244 offset:40960\n\t"
    "s_add_u32 s63, s74, 147456\n\t"
    "buffer_load_dwordx4 v[72:75], v252, s[40:43], s63 offen sc1\n\t"
    "v_mfma_f32_16x16x32_bf16 v[236:239], v[44:47], v[148:151], v[236:239]\n\t"
    "ds_read_b64_tr_b16 v[42:43], v244 offset:45056\n\t"
    "v_mfma_f32_16x16x32_bf16 v[176:179], v[56:59], v[144:147], v[176:179]\n\t"
    "ds_read_b64_tr_b16 v[44:45], v245 offset:40960\n\t"
    "v_mfma_f32_16x16x32_bf16 v[184:187], v[56:59], v[148:151], v[184:187]\n\t"
    "ds_read_b64_tr_b16 v[46:47], v245 offset:45056\n\t"
    "s_add_u32 m0, s61, 132096\n\t"
    "s_add_u32 s63, s76, 3342336\n\t"
    "buffer_load_dwordx4 v255, s[36:39], s63 offen lds\n\t"
    "v_mfma_f32_16x16x32_bf16 v[180:183], v[60:63], v[144:147], v[180:183]\n\t"
    "ds_read_b64_tr_b16 v[56:57], v246 offset:40960\n\t"
    "v_mfma_f32_16x16x32_bf16 v[188:191], v[60:63], v[148:151], v[188:191]\n\t"
    "ds_read_b64_tr_b16 v[58:59], v246 offset:45056\n\t"
    "s_waitcnt lgkmcnt(10)\n\t"
    "v_mfma_f32_16x16x32_bf16 v[224:227], v[8:11], v[168:171], v[224:227]\n\t"
    "ds_read_b64_tr_b16 v[60:61], v247 offset:40960\n\t"
    "s_add_u32 s63, s74, 147456\n\t"
    "buffer_load_dwordx4 v[76:79], v252, s[40:43], s63 offen offset:1024 sc1\n\t"
    "v_mfma_f32_16x16x32_bf16 v[232:235], v[8:11], v[172:175], v[232:235]\n\t"
    "ds_read_b64_tr_b16 v[62:63], v247 offset:45056\n\t"
    "s_add_u32 m0, s61, 147456\n\t"
    "s_add_u32 s63, s76, 3538944\n\t"
    "buffer_load_dwordx4 v255, s[36:39], s63 offen lds\n\t"
    "v_mfma_f32_16x16x32_bf16 v[228:231], v[12:15], v[168:171], v[228:231]\n\t"
    "ds_read_b64_tr_b16 v[8:9], v244 offset:33792\n\t"
    "s_setprio 2\n\t"
    "v_mfma_f32_16x16x32_bf16 v[236:239], v[12:15], v[172:175], v[236:239]\n\t"
    "ds_read_b64_tr_b16 v[10:11], v244 offset:37888\n\t"
    "s_waitcnt lgkmcnt(10)\n\t"
    "v_mfma_f32_16x16x32_bf16 v[176:179], v[24:27], v[168:171], v[176:179]\n\t"
    "ds_read_b64_tr_b16 v[12:13], v245 offset:33792\n\t"
    "v_bfe_u32 v253, v252, 4, 4\n\t"
    "v_mfma_f32_16x16x32_bf16 v[184:187], v[24:27], v[172:175], v[184:187]\n\t"
    "ds_read_b64_tr_b16 v[14:15], v245 offset:37888\n\t"
    "s_add_u32 m0, s61, 148480\n\t"
    "s_add_u32 s63, s76, 3604480\n\t"
    "buffer_load_dwordx4 v255, s[36:39], s63 offen lds\n\t"
    "v_mfma_f32_16x16x32_bf16 v[180:183], v[28:31], v[168:171], v[180:183]\n\t"
    "ds_read_b64_tr_b16 v[24:25], v246 offset:33792\n\t"
    "s_add_u32 s63, s74, 147456\n\t"
    "buffer_load_dwordx4 v[88:91], v252, s[40:43], s63 offen offset:2048 sc1\n\t"
    "v_mfma_f32_16x16x32_bf16 v[188:191], v[28:31], v[172:175], v[188:191]\n\t"
    "ds_read_b64_tr_b16 v[26:27], v246 offset:37888\n\t"
    "s_waitcnt vmcnt(28) lgkmcnt(10)\n\t"
    "v_mfma_f32_16x16x32_bf16 v[224:227], v[40:43], v[160:163], v[224:227]\n\t"
    "ds_read_b64_tr_b16 v[28:29], v247 offset:33792\n\t"
    "v_lshrrev_b32 v254, 8, v252\n\t"
    "s_waitcnt vmcnt(27)\n\t"
    "v_mfma_f32_16x16x32_bf16 v[232:235], v[40:43], v[164:167], v[232:235]\n\t"
    "ds_read_b64_tr_b16 v[30:31], v247 offset:37888\n\t"
    "v_mfma_f32_16x16x32_bf16 v[228:231], v[44:47], v[160:163], v[228:231]\n\t"
    "ds_read_b64_tr_b16 v[40:41], v244 offset:41984\n\t"
    "s_setprio 1\n\t"
    "v_mfma_f32_16x16x32_bf16 v[236:239], v[44:47], v[164:167], v[236:239]\n\t"
    "ds_read_b64_tr_b16 v[42:43], v244 offset:46080\n\t"
    "s_waitcnt lgkmcnt(10)\n\t"
    "v_mfma_f32_16x16x32_bf16 v[176:179], v[56:59], v[160:163], v[176:179]\n\t"
    "ds_read_b64_tr_b16 v[44:45], v245 offset:41984\n\t"
    "s_add_u32 s63, s74, 147456\n\t"
    "buffer_load_dwordx4 v[92:95], v252, s[40:43], s63 offen offset:3072 sc1\n\t"
    "v_mfma_f32_16x16x32_bf16 v[184:187], v[56:59], v[164:167], v[184:187]\n\t"
    "ds_read_b64_tr_b16 v[46:47], v245 offset:46080\n\t"
    "v_lshlrev_b32 v254, 4, v254\n\t"
    "v_mfma_f32_16x16x32_bf16 v[180:183], v[60:63], v[160:163], v[180:183]\n\t"
    "ds_read_b64_tr_b16 v[56:57], v246 offset:41984\n\t"
    "v_mfma_f32_16x16x32_bf16 v[188:191], v[60:63], v[164:167], v[188:191]\n\t"
    "ds_read_b64_tr_b16 v[58:59], v246 offset:46080\n\t"
    "s_waitcnt vmcnt(27) lgkmcnt(10)\n\t"
    "v_mfma_f32_16x16x32_bf16 v[224:227], v[8:11], v[96:99], v[224:227]\n\t"
    "ds_read_b64_tr_b16 v[60:61], v247 offset:41984\n\t"
    "s_setprio 0\n\t"
    "s_waitcnt vmcnt(26)\n\t"
    "v_mfma_f32_16x16x32_bf16 v[232:235], v[8:11], v[100:103], v[232:235]\n\t"
    "ds_read_b64_tr_b16 v[62:63], v247 offset:46080\n\t"
    "v_lshl_add_u32 v248, v253, 10, v254\n\t"
    "v_mfma_f32_16x16x32_bf16 v[228:231], v[12:15], v[96:99], v[228:231]\n\t"
    "s_add_u32 s63, s74, 151552\n\t"
    "buffer_load_dwordx4 v[136:139], v252, s[40:43], s63 offen sc1\n\t"
    "v_mfma_f32_16x16x32_bf16 v[236:239], v[12:15], v[100:103], v[236:239]\n\t"
    "s_waitcnt lgkmcnt(8)\n\t"
    "v_mfma_f32_16x16x32_bf16 v[176:179], v[24:27], v[96:99], v[176:179]\n\t"
    "s_xor_b32 s65, s66, 4\n\t"
    "s_lshl_b32 s65, s65, 12\n\t"
    "v_add_u32 v249, s65, v252\n\t"
    "v_mfma_f32_16x16x32_bf16 v[184:187], v[24:27], v[100:103], v[184:187]\n\t"
    "s_waitcnt vmcnt(21) lgkmcnt(0)\n\t"
    "s_barrier\n\t"
    "s_setprio 3\n\t"
    "ds_read_b64_tr_b16 v[8:9], v244 offset:0\n\t"
    "ds_read_b64_tr_b16 v[10:11], v244 offset:4096\n\t"
    "ds_read_b64_tr_b16 v[12:13], v245 offset:0\n\t"
    "ds_read_b64_tr_b16 v[14:15], v245 offset:4096\n\t"
    "v_mfma_f32_16x16x32_bf16 v[180:183], v[28:31], v[96:99], v[180:183]\n\t"
    "ds_read_b64_tr_b16 v[24:25], v246 offset:0\n\t"
    "v_mfma_f32_16x16x32_bf16 v[188:191], v[28:31], v[100:103], v[188:191]\n\t"
    "ds_read_b64_tr_b16 v[26:27], v246 offset:4096\n\t"
    "v_mfma_f32_16x16x32_bf16 v[224:227], v[40:43], v[112:115], v[224:227]\n\t"
    "ds_read_b64_tr_b16 v[28:29], v247 offset:0\n\t"
    "s_add_u32 s63, s74, 151552\n\t"
    "buffer_load_dwordx4 v[140:143], v252, s[40:43], s63 offen offset:1024 sc1\n\t"
    "v_mfma_f32_16x16x32_bf16 v[232:235], v[40:43], v[116:119], v[232:235]\n\t"
    "ds_read_b64_tr_b16 v[30:31], v247 offset:4096\n\t"
    "s_add_u32 m0, s61, 32768\n\t"
    "s_add_u32 s63, s75, 131072\n\t"
    "buffer_load_dwordx4 v255, s[36:39], s63 offen lds\n\t"
    "s_lshl_b32 s65, s66, 12\n\t"
    "v_add_u32 v250, s65, v252\n\t"
    "v_mfma_f32_16x16x32_bf16 v[228:231], v[44:47], v[112:115], v[228:231]\n\t"
    "ds_read_b64_tr_b16 v[40:41], v244 offset:8192\n\t"
    "v_mfma_f32_16x16x32_bf16 v[236:239], v[44:47], v[116:119], v[236:239]\n\t"
    "ds_read_b64_tr_b16 v[42:43], v244 offset:12288\n\t"
    "v_mfma_f32_16x16x32_bf16 v[176:179], v[56:59], v[112:115], v[176:179]\n\t"
    "ds_read_b64_tr_b16 v[44:45], v245 offset:8192\n\t"
    "s_mov_b64 s[44:45], %[out]\n\t"
    "s_and_b32 s45, s45, 0xffff\n\t"
    "s_mov_b32 s46, 0\n\t"
    "s_mov_b32 s47, 0x20000\n\t"
    "v_mfma_f32_16x16x32_bf16 v[184:187], v[56:59], v[116:119], v[184:187]\n\t"
    "ds_read_b64_tr_b16 v[46:47], v245 offset:12288\n\t"
    "s_add_u32 m0, s61, 33792\n\t"
    "s_add_u32 s63, s75, 196608\n\t"
    "buffer_load_dwordx4 v255, s[36:39], s63 offen lds\n\t"
    "v_mfma_f32_16x16x32_bf16 v[180:183], v[60:63], v[112:115], v[180:183]\n\t"
    "ds_read_b64_tr_b16 v[56:57], v246 offset:8192\n\t"
    "s_add_u32 s63, s74, 151552\n\t"
    "buffer_load_dwordx4 v[128:131], v252, s[40:43], s63 offen offset:2048 sc1\n\t"
    "v_mfma_f32_16x16x32_bf16 v[188:191], v[60:63], v[116:119], v[188:191]\n\t"
    "ds_read_b64_tr_b16 v[58:59], v246 offset:12288\n\t"
    "s_waitcnt vmcnt(24) lgkmcnt(10)\n\t"
    "v_mfma_f32_16x16x32_bf16 v[224:227], v[8:11], v[0:3], v[224:227]\n\t"
    "ds_read_b64_tr_b16 v[60:61], v247 offset:8192\n\t"
    "s_lshr_b32 s46, %[onrec], 4\n\t"
    "s_waitcnt vmcnt(23)\n\t"
    "v_mfma_f32_16x16x32_bf16 v[232:235], v[8:11], v[4:7], v[232:235]\n\t"
    "ds_read_b64_tr_b16 v[62:63], v247 offset:12288\n\t"
    "s_add_u32 m0, s61, 49152\n\t"
    "s_add_u32 s63, s75, 393216\n\t"
    "buffer_load_dwordx4 v255, s[36:39], s63 offen lds\n\t"
    "v_mfma_f32_16x16x32_bf16 v[228:231], v[12:15], v[0:3], v[228:231]\n\t"
    "ds_read_b64_tr_b16 v[8:9], v244 offset:1024\n\t"
    "s_setprio 2\n\t"
    "v_mfma_f32_16x16x32_bf16 v[236:239], v[12:15], v[4:7], v[236:239]\n\t"
    "ds_read_b64_tr_b16 v[10:11], v244 offset:5120\n\t"
    "s_waitcnt lgkmcnt(10)\n\t"
    "v_mfma_f32_16x16x32_bf16 v[176:179], v[24:27], v[0:3], v[176:179]\n\t"
    "ds_read_b64_tr_b16 v[12:13], v245 offset:1024\n\t"
    "s_add_u32 s63, s74, 151552\n\t"
    "buffer_load_dwordx4 v[132:135], v252, s[40:43], s63 offen offset:3072 sc1\n\t"
    "v_mfma_f32_16x16x32_bf16 v[184:187], v[24:27], v[4:7], v[184:187]\n\t"
    "ds_read_b64_tr_b16 v[14:15], v245 offset:5120\n\t"
    "s_add_u32 m0, s61, 50176\n\t"
    "s_add_u32 s63, s75, 458752\n\t"
    "buffer_load_dwordx4 v255, s[36:39], s63 offen lds\n\t"
    "v_mfma_f32_16x16x32_bf16 v[180:183], v[28:31], v[0:3], v[180:183]\n\t"
    "ds_read_b64_tr_b16 v[24:25], v246 offset:1024\n\t"
    "v_mfma_f32_16x16x32_bf16 v[188:191], v[28:31], v[4:7], v[188:191]\n\t"
    "ds_read_b64_tr_b16 v[26:27], v246 offset:5120\n\t"
    "s_waitcnt vmcnt(25) lgkmcnt(10)\n\t"
    "v_mfma_f32_16x16x32_bf16 v[224:227], v[40:43], v[16:19], v[224:227]\n\t"
    "ds_read_b64_tr_b16 v[28:29], v247 offset:1024\n\t"
    "s_waitcnt vmcnt(23)\n\t"
    "v_mfma_f32_16x16x32_bf16 v[232:235], v[40:43], v[20:23], v[232:235]\n\t"
    "ds_read_b64_tr_b16 v[30:31], v247 offset:5120\n\t"
    "v_mfma_f32_16x16x32_bf16 v[228:231], v[44:47], v[16:19], v[228:231]\n\t"
    "ds_read_b64_tr_b16 v[40:41], v244 offset:9216\n\t"
    "s_setprio 1\n\t"
    "v_mfma_f32_16x16x32_bf16 v[236:239], v[44:47], v[20:23], v[236:239]\n\t"
    "ds_read_b64_tr_b16 v[42:43], v244 offset:13312\n\t"
    "s_add_u32 s63, s74, 217088\n\t"
    "buffer_load_dwordx4 v[64:67], v252, s[40:43], s63 offen sc1\n\t"
    "s_waitcnt lgkmcnt(10)\n\t"
    "v_mfma_f32_16x16x32_bf16 v[176:179], v[56:59], v[16:19], v[176:179]\n\t"
    "ds_read_b64_tr_b16 v[44:45], v245 offset:9216\n\t"
    "v_mfma_f32_16x16x32_bf16 v[184:187], v[56:59], v[20:23], v[184:187]\n\t"
    "ds_read_b64_tr_b16 v[46:47], v245 offset:13312\n\t"
    "v_mfma_f32_16x16x32_bf16 v[180:183], v[60:63], v[16:19], v[180:183]\n\t"
    "ds_read_b64_tr_b16 v[56:57], v246 offset:9216\n\t"
    "v_mfma_f32_16x16x32_bf16 v[188:191], v[60:63], v[20:23], v[188:191]\n\t"
    "ds_read_b64_tr_b16 v[58:59], v246 offset:13312\n\t"
    "s_waitcnt vmcnt(21) lgkmcnt(10)\n\t"
    "v_mfma_f32_16x16x32_bf16 v[224:227], v[8:11], v[32:35], v[224:227]\n\t"
    "ds_read_b64_tr_b16 v[60:61], v247 offset:9216\n\t"
    "s_setprio 0\n\t"
    "s_waitcnt vmcnt(19)\n\t"
    "v_mfma_f32_16x16x32_bf16 v[232:235], v[8:11], v[36:39], v[232:235]\n\t"
    "ds_read_b64_tr_b16 v[62:63], v247 offset:13312\n\t"
    "s_add_u32 s63, s74, 217088\n\t"
    "buffer_load_dwordx4 v[68:71], v252, s[40:43], s63 offen offset:1024 sc1\n\t"
    "v_mfma_f32_16x16x32_bf16 v[228:231], v[12:15], v[32:35], v[228:231]\n\t"
    "v_mfma_f32_16x16x32_bf16 v[236:239], v[12:15], v[36:39], v[236:239]\n\t"
    "s_waitcnt lgkmcnt(8)\n\t"
    "v_mfma_f32_16x16x32_bf16 v[176:179], v[24:27], v[32:35], v[176:179]\n\t"
    "v_mfma_f32_16x16x32_bf16 v[184:187], v[24:27], v[36:39], v[184:187]\n\t"
    "s_waitcnt lgkmcnt(0)\n\t"
    "s_barrier\n\t"
    "s_setprio 3\n\t"
    "ds_read_b64_tr_b16 v[8:9], v240 offset:0\n\t"
    "ds_read_b64_tr_b16 v[10:11], v240 offset:4096\n\t"
    "ds_read_b64_tr_b16 v[12:13], v241 offset:0\n\t"
    "ds_read_b64_tr_b16 v[14:15], v241 offset:4096\n\t"
    "v_mfma_f32_16x16x32_bf16 v[180:183], v[28:31], v[32:35], v[180:183]\n\t"
    "ds_read_b64_tr_b16 v[24:25], v242 offset:0\n\t"
    "s_add_u32 s63, s74, 217088\n\t"
    "buffer_load_dwordx4 v[152:155], v252, s[40:43], s63 offen offset:2048 sc1\n\t"
    "v_mfma_f32_16x16x32_bf16 v[188:191], v[28:31], v[36:39], v[188:191]\n\t"
    "ds_read_b64_tr_b16 v[26:27], v242 offset:4096\n\t"
    "s_waitcnt vmcnt(20)\n\t"
    "v_mfma_f32_16x16x32_bf16 v[224:227], v[40:43], v[48:51], v[224:227]\n\t"
    "ds_read_b64_tr_b16 v[28:29], v243 offset:0\n\t"
    "s_waitcnt vmcnt(19)\n\t"
    "v_mfma_f32_16x16x32_bf16 v[232:235], v[40:43], v[52:55], v[232:235]\n\t"
    "ds_read_b64_tr_b16 v[30:31], v243 offset:4096\n\t"
    "s_add_u32 m0, s61, 98304\n\t"
    "s_add_u32 s63, s75, 1179648\n\t"
    "buffer_load_dwordx4 v255, s[36:39], s63 offen lds\n\t"
    "v_mfma_f32_16x16x32_bf16 v[228:231], v[44:47], v[48:51], v[228:231]\n\t"
    "ds_read_b64_tr_b16 v[40:41], v240 offset:8192\n\t"
    "v_mfma_f32_16x16x32_bf16 v[236:239], v[44:47], v[52:55], v[236:239]\n\t"
    "ds_read_b64_tr_b16 v[42:43], v240 offset:12288\n\t"
    "v_mfma_f32_16x16x32_bf16 v[176:179], v[56:59], v[48:51], v[176:179]\n\t"
    "ds_read_b64_tr_b16 v[44:45], v241 offset:8192\n\t"
    "s_add_u32 s63, s74, 217088\n\t"
    "buffer_load_dwordx4 v[156:159], v252, s[40:43], s63 offen offset:3072 sc1\n\t"
    "v_mfma_f32_16x16x32_bf16 v[184:187], v[56:59], v[52:55], v[184:187]\n\t"
    "ds_read_b64_tr_b16 v[46:47], v241 offset:12288\n\t"
    "s_add_u32 m0, s61, 99328\n\t"
    "s_add_u32 s63, s75, 1245184\n\t"
    "buffer_load_dwordx4 v255, s[36:39], s63 offen lds\n\t"
    "v_mfma_f32_16x16x32_bf16 v[180:183], v[60:63], v[48:51], v[180:183]\n\t"
    "ds_read_b64_tr_b16 v[56:57], v242 offset:8192\n\t"
    "v_mfma_f32_16x16x32_bf16 v[188:191], v[60:63], v[52:55], v[188:191]\n\t"
    "ds_read_b64_tr_b16 v[58:59], v242 offset:12288\n\t"
    "s_waitcnt vmcnt(20) lgkmcnt(10)\n\t"
    "v_mfma_f32_16x16x32_bf16 v[224:227], v[8:11], v[72:75], v[224:227]\n\t"
    "ds_read_b64_tr_b16 v[60:61], v243 offset:8192\n\t"
    "s_waitcnt vmcnt(18)\n\t"
    "v_mfma_f32_16x16x32_bf16 v[232:235], v[8:11], v[76:79], v[232:235]\n\t"
    "ds_read_b64_tr_b16 v[62:63], v243 offset:12288\n\t"
    "s_add_u32 m0, s61, 114688\n\t"
    "PAD11_%=: s_cbranch_execz PADRET_%=\n\t"
    "s_add_u32 s63, s75, 1441792\n\t"
    "buffer_load_dwordx4 v255, s[36:39], s63 offen lds\n\t"
    "v_mfma_f32_16x16x32_bf16 v[228:231], v[12:15], v[72:75], v[228:231]\n\t"
    "ds_read_b64_tr_b16 v[8:9], v240 offset:1024\n\t"
    "s_setprio 2\n\t"
    "v_mfma_f32_16x16x32_bf16 v[236:239], v[12:15], v[76:79], v[236:239]\n\t"
    "ds_read_b64_tr_b16 v[10:11], v240 offset:5120\n\t"
    "s_add_u32 s63, s74, 221184\n\t"
    "buffer_load_dwordx4 v[80:83], v252, s[40:43], s63 offen sc1\n\t"
    "s_waitcnt lgkmcnt(10)\n\t"
    "v_mfma_f32_16x16x32_bf16 v[176:179], v[24:27], v[72:75], v[176:179]\n\t"
    "ds_read_b64_tr_b16 v[12:13], v241 offset:1024\n\t"
    "v_mfma_f32_16x16x32_bf16 v[184:187], v[24:27], v[76:79], v[184:187]\n\t"
    "ds_read_b64_tr_b16 v[14:15], v241 offset:5120\n\t"
    "s_add_u32 m0, s61, 115712\n\t"
    "s_add_u32 s63, s75, 1507328\n\t"
    "buffer_load_dwordx4 v255, s[36:39], s63 offen lds\n\t"
    "v_mfma_f32_16x16x32_bf16 v[180:183], v[28:31], v[72:75], v[180:183]\n\t"
    "ds_read_b64_tr_b16 v[24:25], v242 offset:1024\n\t"
    "v_mfma_f32_16x16x32_bf16 v[188:191], v[28:31], v[76:79], v[188:191]\n\t"
    "ds_read_b64_tr_b16 v[26:27], v242 offset:5120\n\t"
    "s_waitcnt vmcnt(18) lgkmcnt(10)\n\t"
    "v_mfma_f32_16x16x32_bf16 v[224:227], v[40:43], v[88:91], v[224:227]\n\t"
    "ds_read_b64_tr_b16 v[28:29], v243 offset:1024\n\t"
    "s_add_u32 s63, s74, 221184\n\t"
    "buffer_load_dwordx4 v[84:87], v252, s[40:43], s63 offen offset:1024 sc1\n\t"
    "s_waitcnt vmcnt(18)\n\t"
    "v_mfma_f32_16x16x32_bf16 v[232:235], v[40:43], v[92:95], v[232:235]\n\t"
    "ds_read_b64_tr_b16 v[30:31], v243 offset:5120\n\t"
    "v_mfma_f32_16x16x32_bf16 v[228:231], v[44:47], v[88:91], v[228:231]\n\t"
    "ds_read_b64_tr_b16 v[40:41], v240 offset:9216\n\t"
    "s_setprio 1\n\t"
    "v_mfma_f32_16x16x32_bf16 v[236:239], v[44:47], v[92:95], v[236:239]\n\t"
    "ds_read_b64_tr_b16 v[42:43], v240 offset:13312\n\t"
    "s_waitcnt lgkmcnt(10)\n\t"
    "v_mfma_f32_16x16x32_bf16 v[176:179], v[56:59], v[88:91], v[176:179]\n\t"
    "ds_read_b64_tr_b16 v[44:45], v241 offset:9216\n\t"
    "v_mfma_f32_16x16x32_bf16 v[184:187], v[56:59], v[92:95], v[184:187]\n\t"
    "ds_read_b64_tr_b16 v[46:47], v241 offset:13312\n\t"
    "v_mfma_f32_16x16x32_bf16 v[180:183], v[60:63], v[88:91], v[180:183]\n\t"
    "ds_read_b64_tr_b16 v[56:57], v242 offset:9216\n\t"
    "s_add_u32 s63, s74, 221184\n\t"
    "buffer_load_dwordx4 v[144:147], v252, s[40:43], s63 offen offset:2048 sc1\n\t"
    "v_mfma_f32_16x16x32_bf16 v[188:191], v[60:63], v[92:95], v[188:191]\n\t"
    "ds_read_b64_tr_b16 v[58:59], v242 offset:13312\n\t"
    "s_waitcnt vmcnt(18) lgkmcnt(10)\n\t"
    "v_mfma_f32_16x16x32_bf16 v[224:227], v[8:11], v[136:139], v[224:227]\n\t"
    "ds_read_b64_tr_b16 v[60:61], v243 offset:9216\n\t"
    "s_setprio 0\n\t"
    "s_waitcnt vmcnt(17)\n\t"
    "v_mfma_f32_16x16x32_bf16 v[232:235], v[8:11], v[140:143], v[232:235]\n\t"
    "ds_read_b64_tr_b16 v[62:63], v243 offset:13312\n\t"
    "v_mfma_f32_16x16x32_bf16 v[228:231], v[12:15], v[136:139], v[228:231]\n\t"
    "v_mfma_f32_16x16x32_bf16 v[236:239], v[12:15], v[140:143], v[236:239]\n\t"
    "s_waitcnt lgkmcnt(8)\n\t"
    "v_mfma_f32_16x16x32_bf16 v[176:179], v[24:27], v[136:139], v[176:179]\n\t"
    "s_add_u32 s63, s74, 221184\n\t"
    "buffer_load_dwordx4 v[148:151], v252, s[40:43], s63 offen offset:3072 sc1\n\t"
    "v_mfma_f32_16x16x32_bf16 v[184:187], v[24:27], v[140:143], v[184:187]\n\t"
    "s_waitcnt lgkmcnt(0)\n\t"
    "s_barrier\n\t"
    "s_setprio 3\n\t"
    "ds_read_b64_tr_b16 v[8:9], v104 offset:0\n\t"
    "ds_read_b64_tr_b16 v[10:11], v104 offset:4096\n\t"
    "ds_read_b64_tr_b16 v[12:13], v105 offset:0\n\t"
    "ds_read_b64_tr_b16 v[14:15], v105 offset:4096\n\t"
    "v_mfma_f32_16x16x32_bf16 v[180:183], v[28:31], v[136:139], v[180:183]\n\t"
    "ds_read_b64_tr_b16 v[24:25], v106 offset:0\n\t"
    "v_mfma_f32_16x16x32_bf16 v[188:191], v[28:31], v[140:143], v[188:191]\n\t"
    "ds_read_b64_tr_b16 v[26:27], v106 offset:4096\n\t"
    "s_waitcnt vmcnt(15)\n\t"
    "v_mfma_f32_16x16x32_bf16 v[224:227], v[40:43], v[128:131], v[224:227]\n\t"
    "ds_read_b64_tr_b16 v[28:29], v107 offset:0\n\t"
    "s_waitcnt vmcnt(13)\n\t"
    "v_mfma_f32_16x16x32_bf16 v[232:235], v[40:43], v[132:135], v[232:235]\n\t"
    "ds_read_b64_tr_b16 v[30:31], v107 offset:4096\n\t"
    "s_add_u32 m0, s61, 65536\n\t"
    "s_add_u32 s63, s75, 2228224\n\t"
    "buffer_load_dwordx4 v255, s[36:39], s63 offen lds\n\t"
    "v_mfma_f32_16x16x32_bf16 v[228:231], v[44:47], v[128:131], v[228:231]\n\t"
    "ds_read_b64_tr_b16 v[40:41], v104 offset:8192\n\t"
    "v_mfma_f32_16x16x32_bf16 v[236:239], v[44:47], v[132:135], v[236:239]\n\t"
    "ds_read_b64_tr_b16 v[42:43], v104 offset:12288\n\t"
    "v_mfma_f32_16x16x32_bf16 v[176:179], v[56:59], v[128:131], v[176:179]\n\t"
    "ds_read_b64_tr_b16 v[44:45], v105 offset:8192\n\t"
    "v_mfma_f32_16x16x32_bf16 v[184:187], v[56:59], v[132:135], v[184:187]\n\t"
    "ds_read_b64_tr_b16 v[46:47], v105 offset:12288\n\t"
    "s_add_u32 m0, s61, 66560\n\t"
    "s_add_u32 s63, s75, 2293760\n\t"
    "buffer_load_dwordx4 v255, s[36:39], s63 offen lds\n\t"
    "v_mfma_f32_16x16x32_bf16 v[180:183], v[60:63], v[128:131], v[180:183]\n\t"
    "ds_read_b64_tr_b16 v[56:57], v106 offset:8192\n\t"
    "v_mfma_f32_16x16x32_bf16 v[188:191], v[60:63], v[132:135], v[188:191]\n\t"
    "ds_read_b64_tr_b16 v[58:59], v106 offset:12288\n\t"
    "s_waitcnt vmcnt(13) lgkmcnt(10)\n\t"
    "v_mfma_f32_16x16x32_bf16 v[224:227], v[8:11], v[64:67], v[224:227]\n\t"
    "ds_read_b64_tr_b16 v[60:61], v107 offset:8192\n\t"
    "s_mov_b64 s[48:49], %[part]\n\t"
    "s_and_b32 s49, s49, 0xffff\n\t"
    "s_mov_b32 s50, 131072\n\t"
    "s_mov_b32 s51, 0x20000\n\t"
    "s_waitcnt vmcnt(12)\n\t"
    "v_mfma_f32_16x16x32_bf16 v[232:235], v[8:11], v[68:71], v[232:235]\n\t"
    "ds_read_b64_tr_b16 v[62:63], v107 offset:12288\n\t"
    "s_add_u32 m0, s61, 81920\n\t"
    "s_add_u32 s63, s75, 2490368\n\t"
    "buffer_load_dwordx4 v255, s[36:39], s63 offen lds\n\t"
    "v_mfma_f32_16x16x32_bf16 v[228:231], v[12:15], v[64:67], v[228:231]\n\t"
    "ds_read_b64_tr_b16 v[8:9], v104 offset:1024\n\t"
    "s_setprio 2\n\t"
    "v_mfma_f32_16x16x32_bf16 v[236:239], v[12:15], v[68:71], v[236:239]\n\t"
    "ds_read_b64_tr_b16 v[10:11], v104 offset:5120\n\t"
    "s_waitcnt lgkmcnt(10)\n\t"
    "v_mfma_f32_16x16x32_bf16 v[176:179], v[24:27], v[64:67], v[176:179]\n\t"
    "ds_read_b64_tr_b16 v[12:13], v105 offset:1024\n\t"
    "v_mov_b32 v124, 0\n\t"
    "v_mfma_f32_16x16x32_bf16 v[184:187], v[24:27], v[68:71], v[184:187]\n\t"
    "ds_read_b64_tr_b16 v[14:15], v105 offset:5120\n\t"
    "s_add_u32 m0, s61, 82944\n\t"
    "s_add_u32 s63, s75, 2555904\n\t"
    "buffer_load_dwordx4 v255, s[36:39], s63 offen lds\n\t"
    "v_mfma_f32_16x16x32_bf16 v[180:183], v[28:31], v[64:67], v[180:183]\n\t"
    "ds_read_b64_tr_b16 v[24:25], v106 offset:1024\n\t"
    "v_mfma_f32_16x16x32_bf16 v[188:191], v[28:31], v[68:71], v[188:191]\n\t"
    "ds_read_b64_tr_b16 v[26:27], v106 offset:5120\n\t"
    "s_waitcnt vmcnt(13) lgkmcnt(10)\n\t"
    "v_mfma_f32_16x16x32_bf16 v[224:227], v[40:43], v[152:155], v[224:227]\n\t"
    "ds_read_b64_tr_b16 v[28:29], v107 offset:1024\n\t"
    "s_waitcnt vmcnt(11)\n\t"
    "v_mfma_f32_16x16x32_bf16 v[232:235], v[40:43], v[156:159], v[232:235]\n\t"
    "ds_read_b64_tr_b16 v[30:31], v107 offset:5120\n\t"
    "v_mfma_f32_16x16x32_bf16 v[228:231], v[44:47], v[152:155], v[228:231]\n\t"
    "ds_read_b64_tr_b16 v[40:41], v104 offset:9216\n\t"
    "s_setprio 1\n\t"
    "v_mfma_f32_16x16x32_bf16 v[236:239], v[44:47], v[156:159], v[236:239]\n\t"
    "ds_read_b64_tr_b16 v[42:43], v104 offset:13312\n\t"
    "v_bfe_u32 v123, v252, 4, 1\n\t"
    "s_waitcnt lgkmcnt(10)\n\t"
    "v_mfma_f32_16x16x32_bf16 v[176:179], v[56:59], v[152:155], v[176:179]\n\t"
    "ds_read_b64_tr_b16 v[44:45], v105 offset:9216\n\t"
    "v_mfma_f32_16x16x32_bf16 v[184:187], v[56:59], v[156:159], v[184:187]\n\t"
    "ds_read_b64_tr_b16 v[46:47], v105 offset:13312\n\t"
    "v_mfma_f32_16x16x32_bf16 v[180:183], v[60:63], v[152:155], v[180:183]\n\t"
    "ds_read_b64_tr_b16 v[56:57], v106 offset:9216\n\t"
    "v_mfma_f32_16x16x32_bf16 v[188:191], v[60:63], v[156:159], v[188:191]\n\t"
    "ds_read_b64_tr_b16 v[58:59], v106 offset:13312\n\t"
    "s_waitcnt vmcnt(8) lgkmcnt(10)\n\t"
    "v_mfma_f32_16x16x32_bf16 v[224:227], v[8:11], v[80:83], v[224:227]\n\t"
    "ds_read_b64_tr_b16 v[60:61], v107 offset:9216\n\t"
    "s_setprio 0\n\t"
    "s_waitcnt vmcnt(6)\n\t"
    "v_mfma_f32_16x16x32_bf16 v[232:235], v[8:11], v[84:87], v[232:235]\n\t"
    "ds_read_b64_tr_b16 v[62:63], v107 offset:13312\n\t"
    "v_lshlrev_b32 v123, 3, v123\n\t"
    "v_mfma_f32_16x16x32_bf16 v[228:231], v[12:15], v[80:83], v[228:231]\n\t"
    "v_mfma_f32_16x16x32_bf16 v[236:239], v[12:15], v[84:87], v[236:239]\n\t"
    "s_waitcnt lgkmcnt(8)\n\t"
    "v_mfma_f32_16x16x32_bf16 v[176:179], v[24:27], v[80:83], v[176:179]\n\t"
    "v_add_u32 v120, 131072, v249\n\t"
    "v_mfma_f32_16x16x32_bf16 v[184:187], v[24:27], v[84:87], v[184:187]\n\t"
    "s_waitcnt lgkmcnt(0)\n\t"
    "s_barrier\n\t"
    "s_setprio 3\n\t"
    "ds_read_b64_tr_b16 v[8:9], v240 offset:32768\n\t"
    "ds_read_b64_tr_b16 v[10:11], v240 offset:36864\n\t"
    "ds_read_b64_tr_b16 v[12:13], v241 offset:32768\n\t"
    "ds_read_b64_tr_b16 v[14:15], v241 offset:36864\n\t"
    "v_mfma_f32_16x16x32_bf16 v[180:183], v[28:31], v[80:83], v[180:183]\n\t"
    "ds_read_b64_tr_b16 v[24:25], v242 offset:32768\n\t"
    "v_mfma_f32_16x16x32_bf16 v[188:191], v[28:31], v[84:87], v[188:191]\n\t"
    "ds_read_b64_tr_b16 v[26:27], v242 offset:36864\n\t"
    "s_waitcnt vmcnt(5)\n\t"
    "v_mfma_f32_16x16x32_bf16 v[224:227], v[40:43], v[144:147], v[224:227]\n\t"
    "ds_read_b64_tr_b16 v[28:29], v243 offset:32768\n\t"
    "s_waitcnt vmcnt(4)\n\t"
    "v_mfma_f32_16x16x32_bf16 v[232:235], v[40:43], v[148:151], v[232:235]\n\t"
    "ds_read_b64_tr_b16 v[30:31], v243 offset:36864\n\t"
    "s_add_u32 m0, s61, 0\n\t"
    "s_add_u32 s63, s75, 3276800\n\t"
    "buffer_load_dwordx4 v255, s[36:39], s63 offen lds\n\t"
    "v_mfma_f32_16x16x32_bf16 v[228:231], v[44:47], v[144:147], v[228:231]\n\t"
    "ds_read_b64_tr_b16 v[40:41], v240 offset:40960\n\t"
    "v_add_u32 v121, 131072, v250\n\t"
    "v_mfma_f32_16x16x32_bf16 v[236:239], v[44:47], v[148:151], v[236:239]\n\t"
    "ds_read_b64_tr_b16 v[42:43], v240 offset:45056\n\t"
    "v_mfma_f32_16x16x32_bf16 v[176:179], v[56:59], v[144:147], v[176:179]\n\t"
    "ds_read_b64_tr_b16 v[44:45], v241 offset:40960\n\t"
    "v_mfma_f32_16x16x32_bf16 v[184:187], v[56:59], v[148:151], v[184:187]\n\t"
    "ds_read_b64_tr_b16 v[46:47], v241 offset:45056\n\t"
    "s_add_u32 m0, s61, 1024\n\t"
    "s_add_u32 s63, s75, 3342336\n\t"
    "buffer_load_dwordx4 v255, s[36:39], s63 offen lds\n\t"
    "v_mfma_f32_16x16x32_bf16 v[180:183], v[60:63], v[144:147], v[180:183]\n\t"
    "ds_read_b64_tr_b16 v[56:57], v242 offset:40960\n\t"
    "v_mfma_f32_16x16x32_bf16 v[188:191], v[60:63], v[148:151], v[188:191]\n\t"
    "ds_read_b64_tr_b16 v[58:59], v242 offset:45056\n\t"
    "s_waitcnt lgkmcnt(10)\n\t"
    "v_mfma_f32_16x16x32_bf16 v[192:195], v[8:11], v[168:171], v[192:195]\n\t"
    "ds_read_b64_tr_b16 v[60:61], v243 offset:40960\n\t"
    "v_mfma_f32_16x16x32_bf16 v[200:203], v[8:11], v[172:175], v[200:203]\n\t"
    "ds_read_b64_tr_b16 v[62:63], v243 offset:45056\n\t"
    "s_add_u32 m0, s61, 16384\n\t"
    "s_add_u32 s63, s75, 3538944\n\t"
    "buffer_load_dwordx4 v255, s[36:39], s63 offen lds\n\t"
    "v_mfma_f32_16x16x32_bf16 v[196:199], v[12:15], v[168:171], v[196:199]\n\t"
    "ds_read_b64_tr_b16 v[8:9], v240 offset:33792\n\t"
    "s_setprio 2\n\t"
    "v_mfma_f32_16x16x32_bf16 v[204:207], v[12:15], v[172:175], v[204:207]\n\t"
    "ds_read_b64_tr_b16 v[10:11], v240 offset:37888\n\t"
    "s_waitcnt lgkmcnt(10)\n\t"
    "v_mfma_f32_16x16x32_bf16 v[208:211], v[24:27], v[168:171], v[208:211]\n\t"
    "ds_read_b64_tr_b16 v[12:13], v241 offset:33792\n\t"
    "ds_write_b128 v120, v[176:179] offset:0\n\t"
    "v_mfma_f32_16x16x32_bf16 v[216:219], v[24:27], v[172:175], v[216:219]\n\t"
    "ds_read_b64_tr_b16 v[14:15], v241 offset:37888\n\t"
    "s_add_u32 m0, s61, 17408\n\t"
    "s_add_u32 s63, s75, 3604480\n\t"
    "buffer_load_dwordx4 v255, s[36:39], s63 offen lds\n\t"
    "v_mfma_f32_16x16x32_bf16 v[212:215], v[28:31], v[168:171], v[212:215]\n\t"
    "ds_read_b64_tr_b16 v[24:25], v242 offset:33792\n\t"
    "ds_write_b128 v120, v[180:183] offset:1024\n\t"
    "v_mfma_f32_16x16x32_bf16 v[220:223], v[28:31], v[172:175], v[220:223]\n\t"
    "ds_read_b64_tr_b16 v[26:27], v242 offset:37888\n\t"
    "s_waitcnt lgkmcnt(12)\n\t"
    "v_mfma_f32_16x16x32_bf16 v[192:195], v[40:43], v[160:163], v[192:195]\n\t"
    "ds_read_b64_tr_b16 v[28:29], v243 offset:33792\n\t"
    "ds_write_b128 v120, v[184:187] offset:2048\n\t"
    "v_mfma_f32_16x16x32_bf16 v[200:203], v[40:43], v[164:167], v[200:203]\n\t"
    "ds_read_b64_tr_b16 v[30:31], v243 offset:37888\n\t"
    "v_mfma_f32_16x16x32_bf16 v[196:199], v[44:47], v[160:163], v[196:199]\n\t"
    "ds_read_b64_tr_b16 v[40:41], v240 offset:41984\n\t"
    "s_setprio 1\n\t"
    "v_mfma_f32_16x16x32_bf16 v[204:207], v[44:47], v[164:167], v[204:207]\n\t"
    "ds_read_b64_tr_b16 v[42:43], v240 offset:46080\n\t"
    "ds_write_b128 v120, v[188:191] offset:3072\n\t"
    "s_waitcnt lgkmcnt(14)\n\t"
    "v_mfma_f32_16x16x32_bf16 v[208:211], v[56:59], v[160:163], v[208:211]\n\t"
    "ds_read_b64_tr_b16 v[44:45], v241 offset:41984\n\t"
    "v_mfma_f32_16x16x32_bf16 v[216:219], v[56:59], v[164:167], v[216:219]\n\t"
    "ds_read_b64_tr_b16 v[46:47], v241 offset:46080\n\t"
    "v_mfma_f32_16x16x32_bf16 v[212:215], v[60:63], v[160:163], v[212:215]\n\t"
    "ds_read_b64_tr_b16 v[56:57], v242 offset:41984\n\t"
    "v_mfma_f32_16x16x32_bf16 v[220:223], v[60:63], v[164:167], v[220:223]\n\t"
    "ds_read_b64_tr_b16 v[58:59], v242 offset:46080\n\t"
    "s_waitcnt lgkmcnt(13)\n\t"
    "v_mfma_f32_16x16x32_bf16 v[192:195], v[8:11], v[96:99], v[192:195]\n\t"
    "ds_read_b64_tr_b16 v[60:61], v243 offset:41984\n\t"
    "s_setprio 0\n\t"
    "v_mfma_f32_16x16x32_bf16 v[200:203], v[8:11], v[100:103], v[200:203]\n\t"
    "ds_read_b64_tr_b16 v[62:63], v243 offset:46080\n\t"
    "v_mfma_f32_16x16x32_bf16 v[196:199], v[12:15], v[96:99], v[196:199]\n\t"
    "v_mfma_f32_16x16x32_bf16 v[204:207], v[12:15], v[100:103], v[204:207]\n\t"
    "s_waitcnt lgkmcnt(9)\n\t"
    "v_mfma_f32_16x16x32_bf16 v[208:211], v[24:27], v[96:99], v[208:211]\n\t"
    "v_mfma_f32_16x16x32_bf16 v[216:219], v[24:27], v[100:103], v[216:219]\n\t"
    "s_waitcnt lgkmcnt(0)\n\t"
    "s_barrier\n\t"
    "s_setprio 3\n\t"
    "ds_read_b64_tr_b16 v[8:9], v244 offset:32768\n\t"
    "ds_read_b64_tr_b16 v[10:11], v244 offset:36864\n\t"
    "ds_read_b64_tr_b16 v[12:13], v245 offset:32768\n\t"
    "ds_read_b64_tr_b16 v[14:15], v245 offset:36864\n\t"
    "v_mfma_f32_16x16x32_bf16 v[212:215], v[28:31], v[96:99], v[212:215]\n\t"
    "ds_read_b64_tr_b16 v[24:25], v246 offset:32768\n\t"
    "v_mfma_f32_16x16x32_bf16 v[220:223], v[28:31], v[100:103], v[220:223]\n\t"
    "ds_read_b64_tr_b16 v[26:27], v246 offset:36864\n\t"
    "v_mfma_f32_16x16x32_bf16 v[192:195], v[40:43], v[112:115], v[192:195]\n\t"
    "ds_read_b64_tr_b16 v[28:29], v247 offset:32768\n\t"
    "v_mfma_f32_16x16x32_bf16 v[200:203], v[40:43], v[116:119], v[200:203]\n\t"
    "ds_read_b64_tr_b16 v[30:31], v247 offset:36864\n\t"
    "v_mfma_f32_16x16x32_bf16 v[196:199], v[44:47], v[112:115], v[196:199]\n\t"
    "ds_read_b64_tr_b16 v[40:41], v244 offset:40960\n\t"
    "v_mfma_f32_16x16x32_bf16 v[204:207], v[44:47], v[116:119], v[204:207]\n\t"
    "ds_read_b64_tr_b16 v[42:43], v244 offset:45056\n\t"
    "v_mfma_f32_16x16x32_bf16 v[208:211], v[56:59], v[112:115], v[208:211]\n\t"
    "ds_read_b64_tr_b16 v[44:45], v245 offset:40960\n\t"
    "ds_read_b128 v[176:179], v121 offset:0\n\t"
    "v_mfma_f32_16x16x32_bf16 v[216:219], v[56:59], v[116:119], v[216:219]\n\t"
    "ds_read_b64_tr_b16 v[46:47], v245 offset:45056\n\t"
    "ds_read_b128 v[180:183], v121 offset:1024\n\t"
    "v_mfma_f32_16x16x32_bf16 v[212:215], v[60:63], v[112:115], v[212:215]\n\t"
    "ds_read_b64_tr_b16 v[56:57], v246 offset:40960\n\t"
    "ds_read_b128 v[184:187], v121 offset:2048\n\t"
    "v_mfma_f32_16x16x32_bf16 v[220:223], v[60:63], v[116:119], v[220:223]\n\t"
    "ds_read_b64_tr_b16 v[58:59], v246 offset:45056\n\t"
    "ds_read_b128 v[188:191], v121 offset:3072\n\t"
    "s_waitcnt lgkmcnt(14)\n\t"
    "v_mfma_f32_16x16x32_bf16 v[192:195], v[8:11], v[0:3], v[192:195]\n\t"
    "ds_read_b64_tr_b16 v[60:61], v247 offset:40960\n\t"
    "v_mfma_f32_16x16x32_bf16 v[200:203], v[8:11], v[4:7], v[200:203]\n\t"
    "ds_read_b64_tr_b16 v[62:63], v247 offset:45056\n\t"
    "v_mfma_f32_16x16x32_bf16 v[196:199], v[12:15], v[0:3], v[196:199]\n\t"
    "ds_read_b64_tr_b16 v[8:9], v244 offset:33792\n\t"
    "s_setprio 2\n\t"
    "s_waitcnt lgkmcnt(9)\n\t"
    "v_add_f32 v224, v224, v176\n\t"
    "v_add_f32 v225, v225, v177\n\t"
    "v_mfma_f32_16x16x32_bf16 v[204:207], v[12:15], v[4:7], v[204:207]\n\t"
    "ds_read_b64_tr_b16 v[10:11], v244 offset:37888\n\t"
    "v_add_f32 v226, v226, v178\n\t"
    "v_add_f32 v227, v227, v179\n\t"
    "v_mfma_f32_16x16x32_bf16 v[208:211], v[24:27], v[0:3], v[208:211]\n\t"
    "ds_read_b64_tr_b16 v[12:13], v245 offset:33792\n\t"
    "s_waitcnt lgkmcnt(9)\n\t"
    "v_add_f32 v228, v228, v180\n\t"
    "v_add_f32 v229, v229, v181\n\t"
    "v_mfma_f32_16x16x32_bf16 v[216:219], v[24:27], v[4:7], v[216:219]\n\t"
    "ds_read_b64_tr_b16 v[14:15], v245 offset:37888\n\t"
    "v_add_f32 v230, v230, v182\n\t"
    "v_add_f32 v231, v231, v183\n\t"
    "v_mfma_f32_16x16x32_bf16 v[212:215], v[28:31], v[0:3], v[212:215]\n\t"
    "ds_read_b64_tr_b16 v[24:25], v246 offset:33792\n\t"
    "s_waitcnt lgkmcnt(9)\n\t"
    "v_add_f32 v232, v232, v184\n\t"
    "v_add_f32 v233, v233, v185\n\t"
    "v_mfma_f32_16x16x32_bf16 v[220:223], v[28:31], v[4:7], v[220:223]\n\t"
    "ds_read_b64_tr_b16 v[26:27], v246 offset:37888\n\t"
    "v_add_f32 v234, v234, v186\n\t"
    "v_add_f32 v235, v235, v187\n\t"
    "v_mfma_f32_16x16x32_bf16 v[192:195], v[40:43], v[16:19], v[192:195]\n\t"
    "ds_read_b64_tr_b16 v[28:29], v247 offset:33792\n\t"
    "s_waitcnt lgkmcnt(9)\n\t"
    "v_add_f32 v236, v236, v188\n\t"
    "v_add_f32 v237, v237, v189\n\t"
    "v_mfma_f32_16x16x32_bf16 v[200:203], v[40:43], v[20:23], v[200:203]\n\t"
    "ds_read_b64_tr_b16 v[30:31], v247 offset:37888\n\t"
    "v_add_f32 v238, v238, v190\n\t"
    "v_add_f32 v239, v239, v191\n\t"
    "v_mfma_f32_16x16x32_bf16 v[196:199], v[44:47], v[16:19], v[196:199]\n\t"
    "ds_read_b64_tr_b16 v[40:41], v244 offset:41984\n\t"
    "s_setprio 1\n\t"
    "v_mfma_f32_16x16x32_bf16 v[204:207], v[44:47], v[20:23], v[204:207]\n\t"
    "ds_read_b64_tr_b16 v[42:43], v244 offset:46080\n\t"
    "s_lshl_b32 s63, s77, 9\n\t"
    "s_xor_b32 s63, s63, 65536\n\t"
    "buffer_store_dwordx4 v[224:227], v252, s[48:51], s63 offen sc1\n\t"
    "s_waitcnt lgkmcnt(10)\n\t"
    "v_mfma_f32_16x16x32_bf16 v[208:211], v[56:59], v[16:19], v[208:211]\n\t"
    "ds_read_b64_tr_b16 v[44:45], v245 offset:41984\n\t"
    "s_lshl_b32 s63, s77, 9\n\t"
    "s_xor_b32 s63, s63, 65536\n\t"
    "buffer_store_dwordx4 v[228:231], v252, s[48:51], s63 offen offset:1024 sc1\n\t"
    "v_mfma_f32_16x16x32_bf16 v[216:219], v[56:59], v[20:23], v[216:219]\n\t"
    "ds_read_b64_tr_b16 v[46:47], v245 offset:46080\n\t"
    "v_mfma_f32_16x16x32_bf16 v[212:215], v[60:63], v[16:19], v[212:215]\n\t"
    "ds_read_b64_tr_b16 v[56:57], v246 offset:41984\n\t"
    "s_lshl_b32 s63, s77, 9\n\t"
    "s_xor_b32 s63, s63, 65536\n\t"
    "buffer_store_dwordx4 v[232:235], v252, s[48:51], s63 offen offset:2048 sc1\n\t"
    "v_mfma_f32_16x16x32_bf16 v[220:223], v[60:63], v[20:23], v[220:223]\n\t"
    "ds_read_b64_tr_b16 v[58:59], v246 offset:46080\n\t"
    "s_waitcnt lgkmcnt(10)\n\t"
    "v_mfma_f32_16x16x32_bf16 v[192:195], v[8:11], v[32:35], v[192:195]\n\t"
    "ds_read_b64_tr_b16 v[60:61], v247 offset:41984\n\t"
    "s_setprio 0\n\t"
    "v_mfma_f32_16x16x32_bf16 v[200:203], v[8:11], v[36:39], v[200:203]\n\t"
    "ds_read_b64_tr_b16 v[62:63], v247 offset:46080\n\t"
    "s_lshl_b32 s63, s77, 9\n\t"
    "s_xor_b32 s63, s63, 65536\n\t"
    "buffer_store_dwordx4 v[236:239], v252, s[48:51], s63 offen offset:3072 sc1\n\t"
    "v_mfma_f32_16x16x32_bf16 v[196:199], v[12:15], v[32:35], v[196:199]\n\t"
    "v_mfma_f32_16x16x32_bf16 v[204:207], v[12:15], v[36:39], v[204:207]\n\t"
    "s_waitcnt lgkmcnt(8)\n\t"
    "v_mfma_f32_16x16x32_bf16 v[208:211], v[24:27], v[32:35], v[208:211]\n\t"
    "v_mfma_f32_16x16x32_bf16 v[216:219], v[24:27], v[36:39], v[216:219]\n\t"
    "s_waitcnt vmcnt(8) lgkmcnt(0)\n\t"
    "s_barrier\n\t"
    "s_setprio 3\n\t"
    "ds_read_b64_tr_b16 v[8:9], v244 offset:0\n\t"
    "ds_read_b64_tr_b16 v[10:11], v244 offset:4096\n\t"
    "ds_read_b64_tr_b16 v[12:13], v245 offset:0\n\t"
    "ds_read_b64_tr_b16 v[14:15], v245 offset:4096\n\t"
    "v_mfma_f32_16x16x32_bf16 v[212:215], v[28:31], v[32:35], v[212:215]\n\t"
    "ds_read_b64_tr_b16 v[24:25], v246 offset:0\n\t"
    "v_mfma_f32_16x16x32_bf16 v[220:223], v[28:31], v[36:39], v[220:223]\n\t"
    "ds_read_b64_tr_b16 v[26:27], v246 offset:4096\n\t"
    "v_mfma_f32_16x16x32_bf16 v[192:195], v[40:43], v[48:51], v[192:195]\n\t"
    "ds_read_b64_tr_b16 v[28:29], v247 offset:0\n\t"
    "v_mfma_f32_16x16x32_bf16 v[200:203], v[40:43], v[52:55], v[200:203]\n\t"
    "ds_read_b64_tr_b16 v[30:31], v247 offset:4096\n\t"
    "v_mfma_f32_16x16x32_bf16 v[196:199], v[44:47], v[48:51], v[196:199]\n\t"
    "ds_read_b64_tr_b16 v[40:41], v244 offset:8192\n\t"
    "v_mfma_f32_16x16x32_bf16 v[204:207], v[44:47], v[52:55], v[204:207]\n\t"
    "ds_read_b64_tr_b16 v[42:43], v244 offset:12288\n\t"
    "v_mfma_f32_16x16x32_bf16 v[208:211], v[56:59], v[48:51], v[208:211]\n\t"
    "ds_read_b64_tr_b16 v[44:45], v245 offset:8192\n\t"
    "v_mfma_f32_16x16x32_bf16 v[216:219], v[56:59], v[52:55], v[216:219]\n\t"
    "ds_read_b64_tr_b16 v[46:47], v245 offset:12288\n\t"
    "v_mfma_f32_16x16x32_bf16 v[212:215], v[60:63], v[48:51], v[212:215]\n\t"
    "ds_read_b64_tr_b16 v[56:57], v246 offset:8192\n\t"
    "v_mfma_f32_16x16x32_bf16 v[220:223], v[60:63], v[52:55], v[220:223]\n\t"
    "ds_read_b64_tr_b16 v[58:59], v246 offset:12288\n\t"
    "s_waitcnt lgkmcnt(10)\n\t"
    "v_mfma_f32_16x16x32_bf16 v[192:195], v[8:11], v[72:75], v[192:195]\n\t"
    "ds_read_b64_tr_b16 v[60:61], v247 offset:8192\n\t"
    "v_mfma_f32_16x16x32_bf16 v[200:203], v[8:11], v[76:79], v[200:203]\n\t"
    "ds_read_b64_tr_b16 v[62:63], v247 offset:12288\n\t"
    "v_mfma_f32_16x16x32_bf16 v[196:199], v[12:15], v[72:75], v[196:199]\n\t"
    "ds_read_b64_tr_b16 v[8:9], v244 offset:1024\n\t"
    "s_setprio 2\n\t"
    "v_mfma_f32_16x16x32_bf16 v[204:207], v[12:15], v[76:79], v[204:207]\n\t"
    "ds_read_b64_tr_b16 v[10:11], v244 offset:5120\n\t"
    "s_waitcnt lgkmcnt(10)\n\t"
    "v_mfma_f32_16x16x32_bf16 v[208:211], v[24:27], v[72:75], v[208:211]\n\t"
    "ds_read_b64_tr_b16 v[12:13], v245 offset:1024\n\t"
    "v_mfma_f32_16x16x32_bf16 v[216:219], v[24:27], v[76:79], v[216:219]\n\t"
    "ds_read_b64_tr_b16 v[14:15], v245 offset:5120\n\t"
    "v_mfma_f32_16x16x32_bf16 v[212:215], v[28:31], v[72:75], v[212:215]\n\t"
    "ds_read_b64_tr_b16 v[24:25], v246 offset:1024\n\t"
    "v_mfma_f32_16x16x32_bf16 v[220:223], v[28:31], v[76:79], v[220:223]\n\t"
    "ds_read_b64_tr_b16 v[26:27], v246 offset:5120\n\t"
    "s_waitcnt lgkmcnt(10)\n\t"
    "v_mfma_f32_16x16x32_bf16 v[192:195], v[40:43], v[88:91], v[192:195]\n\t"
    "ds_read_b64_tr_b16 v[28:29], v247 offset:1024\n\t"
    "s_waitcnt vmcnt(0)\n\t"
    "s_xor_b32 s63, %[wgi], 128\n\t"
    "buffer_store_dwordx2 v[126:127], v123, s[56:59], s63 offen sc1\n\t"
    "v_mfma_f32_16x16x32_bf16 v[200:203], v[40:43], v[92:95], v[200:203]\n\t"
    "ds_read_b64_tr_b16 v[30:31], v247 offset:5120\n\t"
    "v_mfma_f32_16x16x32_bf16 v[196:199], v[44:47], v[88:91], v[196:199]\n\t"
    "ds_read_b64_tr_b16 v[40:41], v244 offset:9216\n\t"
    "s_setprio 1\n\t"
    "v_mfma_f32_16x16x32_bf16 v[204:207], v[44:47], v[92:95], v[204:207]\n\t"
    "ds_read_b64_tr_b16 v[42:43], v244 offset:13312\n\t"
    "s_waitcnt lgkmcnt(10)\n\t"
    "v_mfma_f32_16x16x32_bf16 v[208:211], v[56:59], v[88:91], v[208:211]\n\t"
    "ds_read_b64_tr_b16 v[44:45], v245 offset:9216\n\t"
    "v_mfma_f32_16x16x32_bf16 v[216:219], v[56:59], v[92:95], v[216:219]\n\t"
    "ds_read_b64_tr_b16 v[46:47], v245 offset:13312\n\t"
    "v_mfma_f32_16x16x32_bf16 v[212:215], v[60:63], v[88:91], v[212:215]\n\t"
    "ds_read_b64_tr_b16 v[56:57], v246 offset:9216\n\t"
    "v_mfma_f32_16x16x32_bf16 v[220:223], v[60:63], v[92:95], v[220:223]\n\t"
    "ds_read_b64_tr_b16 v[58:59], v246 offset:13312\n\t"
    "s_waitcnt lgkmcnt(10)\n\t"
    "v_mfma_f32_16x16x32_bf16 v[192:195], v[8:11], v[136:139], v[192:195]\n\t"
    "ds_read_b64_tr_b16 v[60:61], v247 offset:9216\n\t"
    "s_setprio 0\n\t"
    "v_mfma_f32_16x16x32_bf16 v[200:203], v[8:11], v[140:143], v[200:203]\n\t"
    "ds_read_b64_tr_b16 v[62:63], v247 offset:13312\n\t"
    "v_mfma_f32_16x16x32_bf16 v[196:199], v[12:15], v[136:139], v[196:199]\n\t"
    "v_mfma_f32_16x16x32_bf16 v[204:207], v[12:15], v[140:143], v[204:207]\n\t"
    "s_waitcnt lgkmcnt(8)\n\t"
    "v_mfma_f32_16x16x32_bf16 v[208:211], v[24:27], v[136:139], v[208:211]\n\t"
    "v_mfma_f32_16x16x32_bf16 v[216:219], v[24:27], v[140:143], v[216:219]\n\t"
    "s_waitcnt lgkmcnt(0)\n\t"
    "s_barrier\n\t"
    "s_setprio 3\n\t"
    "ds_read_b64_tr_b16 v[8:9], v240 offset:0\n\t"
    "ds_read_b64_tr_b16 v[10:11], v240 offset:4096\n\t"
    "ds_read_b64_tr_b16 v[12:13], v241 offset:0\n\t"
    "ds_read_b64_tr_b16 v[14:15], v241 offset:4096\n\t"
    "v_mfma_f32_16x16x32_bf16 v[212:215], v[28:31], v[136:139], v[212:215]\n\t"
    "ds_read_b64_tr_b16 v[24:25], v242 offset:0\n\t"
    "v_mfma_f32_16x16x32_bf16 v[220:223], v[28:31], v[140:143], v[220:223]\n\t"
    "ds_read_b64_tr_b16 v[26:27], v242 offset:4096\n\t"
    "v_mfma_f32_16x16x32_bf16 v[192:195], v[40:43], v[128:131], v[192:195]\n\t"
    "ds_read_b64_tr_b16 v[28:29], v243 offset:0\n\t"
    "v_mfma_f32_16x16x32_bf16 v[200:203], v[40:43], v[132:135], v[200:203]\n\t"
    "ds_read_b64_tr_b16 v[30:31], v243 offset:4096\n\t"
    "v_mfma_f32_16x16x32_bf16 v[196:199], v[44:47], v[128:131], v[196:199]\n\t"
    "ds_read_b64_tr_b16 v[40:41], v240 offset:8192\n\t"
    "v_mfma_f32_16x16x32_bf16 v[204:207], v[44:47], v[132:135], v[204:207]\n\t"
    "ds_read_b64_tr_b16 v[42:43], v240 offset:12288\n\t"
    "v_mfma_f32_16x16x32_bf16 v[208:211], v[56:59], v[128:131], v[208:211]\n\t"
    "ds_read_b64_tr_b16 v[44:45], v241 offset:8192\n\t"
    "v_mfma_f32_16x16x32_bf16 v[216:219], v[56:59], v[132:135], v[216:219]\n\t"
    "ds_read_b64_tr_b16 v[46:47], v241 offset:12288\n\t"
    "v_mfma_f32_16x16x32_bf16 v[212:215], v[60:63], v[128:131], v[212:215]\n\t"
    "ds_read_b64_tr_b16 v[56:57], v242 offset:8192\n\t"
    "v_mfma_f32_16x16x32_bf16 v[220:223], v[60:63], v[132:135], v[220:223]\n\t"
    "ds_read_b64_tr_b16 v[58:59], v242 offset:12288\n\t"
    "s_waitcnt lgkmcnt(10)\n\t"
    "v_mfma_f32_16x16x32_bf16 v[192:195], v[8:11], v[64:67], v[192:195]\n\t"
    "ds_read_b64_tr_b16 v[60:61], v243 offset:8192\n\t"
    "v_mfma_f32_16x16x32_bf16 v[200:203], v[8:11], v[68:71], v[200:203]\n\t"
    "ds_read_b64_tr_b16 v[62:63], v243 offset:12288\n\t"
    "v_mfma_f32_16x16x32_bf16 v[196:199], v[12:15], v[64:67], v[196:199]\n\t"
    "ds_read_b64_tr_b16 v[8:9], v240 offset:1024\n\t"
    "s_setprio 2\n\t"
    "v_mfma_f32_16x16x32_bf16 v[204:207], v[12:15], v[68:71], v[204:207]\n\t"
    "ds_read_b64_tr_b16 v[10:11], v240 offset:5120\n\t"
    "s_waitcnt lgkmcnt(10)\n\t"
    "v_mfma_f32_16x16x32_bf16 v[208:211], v[24:27], v[64:67], v[208:211]\n\t"
    "ds_read_b64_tr_b16 v[12:13], v241 offset:1024\n\t"
    "s_mov_b32 s63, %[wgi]\n\t"
    "buffer_load_dwordx4 v[108:111], v124, s[56:59], s63 offen sc1\n\t"
    "v_mfma_f32_16x16x32_bf16 v[216:219], v[24:27], v[68:71], v[216:219]\n\t"
    "ds_read_b64_tr_b16 v[14:15], v241 offset:5120\n\t"
    "v_mfma_f32_16x16x32_bf16 v[212:215], v[28:31], v[64:67], v[212:215]\n\t"
    "ds_read_b64_tr_b16 v[24:25], v242 offset:1024\n\t"
    "v_mfma_f32_16x16x32_bf16 v[220:223], v[28:31], v[68:71], v[220:223]\n\t"
    "ds_read_b64_tr_b16 v[26:27], v242 offset:5120\n\t"
    "s_waitcnt lgkmcnt(10)\n\t"
    "v_mfma_f32_16x16x32_bf16 v[192:195], v[40:43], v[152:155], v[192:195]\n\t"
    "ds_read_b64_tr_b16 v[28:29], v243 offset:1024\n\t"
    "v_mfma_f32_16x16x32_bf16 v[200:203], v[40:43], v[156:159], v[200:203]\n\t"
    "ds_read_b64_tr_b16 v[30:31], v243 offset:5120\n\t"
    "v_mfma_f32_16x16x32_bf16 v[196:199], v[44:47], v[152:155], v[196:199]\n\t"
    "ds_read_b64_tr_b16 v[40:41], v240 offset:9216\n\t"
    "s_setprio 1\n\t"
    "v_mfma_f32_16x16x32_bf16 v[204:207], v[44:47], v[156:159], v[204:207]\n\t"
    "ds_read_b64_tr_b16 v[42:43], v240 offset:13312\n\t"
    "s_waitcnt lgkmcnt(10)\n\t"
    "v_mfma_f32_16x16x32_bf16 v[208:211], v[56:59], v[152:155], v[208:211]\n\t"
    "ds_read_b64_tr_b16 v[44:45], v241 offset:9216\n\t"
    "v_mfma_f32_16x16x32_bf16 v[216:219], v[56:59], v[156:159], v[216:219]\n\t"
    "ds_read_b64_tr_b16 v[46:47], v241 offset:13312\n\t"
    "v_mfma_f32_16x16x32_bf16 v[212:215], v[60:63], v[152:155], v[212:215]\n\t"
    "ds_read_b64_tr_b16 v[56:57], v242 offset:9216\n\t"
    "v_mfma_f32_16x16x32_bf16 v[220:223], v[60:63], v[156:159], v[220:223]\n\t"
    "ds_read_b64_tr_b16 v[58:59], v242 offset:13312\n\t"
    "s_waitcnt lgkmcnt(10)\n\t"
    "v_mfma_f32_16x16x32_bf16 v[192:195], v[8:11], v[80:83], v[192:195]\n\t"
    "ds_read_b64_tr_b16 v[60:61], v243 offset:9216\n\t"
    "s_setprio 0\n\t"
    "v_mfma_f32_16x16x32_bf16 v[200:203], v[8:11], v[84:87], v[200:203]\n\t"
    "ds_read_b64_tr_b16 v[62:63], v243 offset:13312\n\t"
    "v_mfma_f32_16x16x32_bf16 v[196:199], v[12:15], v[80:83], v[196:199]\n\t"
    "v_mfma_f32_16x16x32_bf16 v[204:207], v[12:15], v[84:87], v[204:207]\n\t"
    "s_waitcnt lgkmcnt(8)\n\t"
    "v_mfma_f32_16x16x32_bf16 v[208:211], v[24:27], v[80:83], v[208:211]\n\t"
    "v_mfma_f32_16x16x32_bf16 v[216:219], v[24:27], v[84:87], v[216:219]\n\t"
    "v_mfma_f32_16x16x32_bf16 v[212:215], v[28:31], v[80:83], v[212:215]\n\t"
    "v_mfma_f32_16x16x32_bf16 v[220:223], v[28:31], v[84:87], v[220:223]\n\t"
    "s_waitcnt lgkmcnt(4)\n\t"
    "v_mfma_f32_16x16x32_bf16 v[192:195], v[40:43], v[144:147], v[192:195]\n\t"
    "v_mfma_f32_16x16x32_bf16 v[200:203], v[40:43], v[148:151], v[200:203]\n\t"
    "v_mfma_f32_16x16x32_bf16 v[196:199], v[44:47], v[144:147], v[196:199]\n\t"
    "v_mfma_f32_16x16x32_bf16 v[204:207], v[44:47], v[148:151], v[204:207]\n\t"
    "s_waitcnt lgkmcnt(0)\n\t"
    "v_mfma_f32_16x16x32_bf16 v[208:211], v[56:59], v[144:147], v[208:211]\n\t"
    "s_waitcnt vmcnt(0)\n\t"
    "v_xor_b32 v108, v108, v126\n\t"
    "v_xor_b32 v109, v109, v127\n\t"
    "v_xor_b32 v110, v110, v126\n\t"
    "v_xor_b32 v111, v111, v127\n\t"
    "v_or3_b32 v108, v108, v109, v110\n\t"
    "v_or_b32 v108, v108, v111\n\t"
    "v_cmp_ne_u32 vcc, 0, v108\n\t"
    "s_nop 4\n\t"
    "s_cbranch_vccz PGO2_%=\n\t"
    "s_mov_b32 s70, 0x200000\n\t"
    "PSP2_%=:\n\t"
    "s_sleep 4\n\t"
    "s_mov_b32 s63, %[wgi]\n\t"
    "buffer_load_dwordx4 v[108:111], v124, s[56:59], s63 offen sc1\n\t"
    "s_waitcnt vmcnt(0)\n\t"
    "v_xor_b32 v108, v108, v126\n\t"
    "v_xor_b32 v109, v109, v127\n\t"
    "v_xor_b32 v110, v110, v126\n\t"
    "v_xor_b32 v111, v111, v127\n\t"
    "v_or3_b32 v108, v108, v109, v110\n\t"
    "v_or_b32 v108, v108, v111\n\t"
    "v_cmp_ne_u32 vcc, 0, v108\n\t"
    "s_sub_u32 s70, s70, 1\n\t"
    "s_cbranch_scc1 PGO2_%=\n\t"
    "s_nop 4\n\t"
    "s_cbranch_vccnz PSP2_%=\n\t"
    "PGO2_%=:\n\t"
    "s_nop 1\n\t"
    "s_lshl_b32 s63, s77, 9\n\t"
    "buffer_load_dwordx4 v[224:227], v252, s[48:51], s63 offen sc1\n\t"
    "s_lshl_b32 s63, s77, 9\n\t"
    "buffer_load_dwordx4 v[228:231], v252, s[48:51], s63 offen offset:1024 sc1\n\t"
    "v_mfma_f32_16x16x32_bf16 v[216:219], v[56:59], v[148:151], v[216:219]\n\t"
    "s_lshl_b32 s63, s77, 9\n\t"
    "buffer_load_dwordx4 v[232:235], v252, s[48:51], s63 offen offset:2048 sc1\n\t"
    "s_lshl_b32 s63, s77, 9\n\t"
    "buffer_load_dwordx4 v[236:239], v252, s[48:51], s63 offen offset:3072 sc1\n\t"
    "v_mfma_f32_16x16x32_bf16 v[212:215], v[60:63], v[144:147], v[212:215]\n\t"
    "v_mfma_f32_16x16x32_bf16 v[220:223], v[60:63], v[148:151], v[220:223]\n\t"
    "s_setprio 0\n\t"
    "ds_write_b128 v249, v[208:211] offset:32768\n\t"
    "s_nop 4\n\t"
    "ds_write_b128 v249, v[212:215] offset:33792\n\t"
    "ds_write_b128 v249, v[216:219] offset:34816\n\t"
    "ds_write_b128 v249, v[220:223] offset:35840\n\t"
    "s_waitcnt lgkmcnt(0)\n\t"
    "s_barrier\n\t"
    "ds_read_b128 v[208:211], v250 offset:32768\n\t"
    "ds_read_b128 v[212:215], v250 offset:33792\n\t"
    "ds_read_b128 v[216:219], v250 offset:34816\n\t"
    "PAD12_%=: s_cbranch_execz PADRET_%=\n\t"
    "ds_read_b128 v[220:223], v250 offset:35840\n\t"
    "s_waitcnt lgkmcnt(3)\n\t"
    "v_add_f32 v192, v192, v208\n\t"
    "v_add_f32 v193, v193, v209\n\t"
    "v_add_f32 v194, v194, v210\n\t"
    "v_add_f32 v195, v195, v211\n\t"
    "s_waitcnt lgkmcnt(2)\n\t"
    "v_add_f32 v196, v196, v212\n\t"
    "v_add_f32 v197, v197, v213\n\t"
    "v_add_f32 v198, v198, v214\n\t"
    "v_add_f32 v199, v199, v215\n\t"
    "s_waitcnt lgkmcnt(1)\n\t"
    "v_add_f32 v200, v200, v216\n\t"
    "v_add_f32 v201, v201, v217\n\t"
    "v_add_f32 v202, v202, v218\n\t"
    "v_add_f32 v203, v203, v219\n\t"
    "s_waitcnt lgkmcnt(0)\n\t"
    "v_add_f32 v204, v204, v220\n\t"
    "v_add_f32 v205, v205, v221\n\t"
    "v_add_f32 v206, v206, v222\n\t"
    "v_add_f32 v207, v207, v223\n\t"
    "s_waitcnt vmcnt(3)\n\t"
    "v_add_f32 v192, v192, v224\n\t"
    "v_add_f32 v193, v193, v225\n\t"
    "v_add_f32 v194, v194, v226\n\t"
    "v_add_f32 v195, v195, v227\n\t"
    "s_waitcnt vmcnt(2)\n\t"
    "v_add_f32 v196, v196, v228\n\t"
    "v_add_f32 v197, v197, v229\n\t"
    "v_add_f32 v198, v198, v230\n\t"
    "v_add_f32 v199, v199, v231\n\t"
    "s_waitcnt vmcnt(1)\n\t"
    "v_add_f32 v200, v200, v232\n\t"
    "v_add_f32 v201, v201, v233\n\t"
    "v_add_f32 v202, v202, v234\n\t"
    "v_add_f32 v203, v203, v235\n\t"
    "s_waitcnt vmcnt(0)\n\t"
    "v_add_f32 v204, v204, v236\n\t"
    "v_add_f32 v205, v205, v237\n\t"
    "v_add_f32 v206, v206, v238\n\t"
    "v_add_f32 v207, v207, v239\n\t"
    "v_cvt_pk_bf16_f32 v192, v192, v193\n\t"
    "v_cvt_pk_bf16_f32 v193, v194, v195\n\t"
    "v_cvt_pk_bf16_f32 v194, v196, v197\n\t"
    "v_cvt_pk_bf16_f32 v195, v198, v199\n\t"
    "v_cvt_pk_bf16_f32 v196, v200, v201\n\t"
    "v_cvt_pk_bf16_f32 v197, v202, v203\n\t"
    "v_cvt_pk_bf16_f32 v198, v204, v205\n\t"
    "v_cvt_pk_bf16_f32 v199, v206, v207\n\t"
    "v_permlane32_swap_b32 v192, v194\n\t"
    "v_permlane32_swap_b32 v193, v195\n\t"
    "v_permlane32_swap_b32 v196, v198\n\t"
    "v_permlane32_swap_b32 v197, v199\n\t"
    "v_permlane16_swap_b32 v192, v194\n\t"
    "v_permlane16_swap_b32 v193, v195\n\t"
    "v_permlane16_swap_b32 v196, v198\n\t"
    "v_permlane16_swap_b32 v197, v199\n\t"
    "buffer_store_dwordx4 v[192:195], v248, s[44:47], 0 offen\n\t"
    "s_movk_i32 s63, 0x4000\n\t"
    "buffer_store_dwordx4 v[196:199], v248, s[44:47], s63 offen\n\t"
    "s_branch KOUT_%=\n\t"
    "KEND_%=:\n\t"
    "s_waitcnt vmcnt(0) lgkmcnt(0)\n\t"
    "s_nop 15\n\t"
    "KOUT_%=:\n\t"
    "s_mov_b32 %[flag], s64\n\t"
    "s_mov_b64 %[tagout], s[72:73]\n\t"
    : [flag]"=s"(flag), [tagout]"=s"(tag), "+{s[36:37]}"(xp), "+{s[40:41]}"(wp), "+{s[44:45]}"(kp), "+{s[48:49]}"(vp), "+{s[52:53]}"(op), "+{s[56:57]}"(fl1)
    : [w]"s"(w), [rowlo]"s"(rowlo),
      [wo]"s"(wop), [ofr]"s"(ofr), [out]"s"(outp), [myfl]"s"(myfl), [pollfl]"s"(pollfl), [onrec]"s"(onrec), [bump]"s"(bumpo), [wopf]"s"(wopf), [part]"s"(pp), [wgi]"s"(wgi)
    : "v0", "v1", "v2", "v3", "v4", "v5", "v6", "v7", "v8", "v9", "v10", "v11", "v12", "v13", "v14", "v15", "v16", "v17", "v18", "v19", "v20", "v21", "v22", "v23", "v24", "v25", "v26", "v27", "v28", "v29", "v30", "v31", "v32", "v33", "v34", "v35", "v36", "v37", "v38", "v39", "v40", "v41", "v42", "v43", "v44", "v45", "v46", "v47", "v48", "v49", "v50", "v51", "v52", "v53", "v54", "v55", "v56", "v57", "v58", "v59", "v60", "v61", "v62", "v63", "v64", "v65", "v66", "v67", "v68", "v69", "v70", "v71", "v72", "v73", "v74", "v75", "v76", "v77", "v78", "v79", "v80", "v81", "v82", "v83", "v84", "v85", "v86", "v87", "v88", "v89", "v90", "v91", "v92", "v93", "v94", "v95", "v96", "v97", "v98", "v99", "v100", "v101", "v102", "v103", "v104", "v105", "v106", "v107", "v108", "v109", "v110", "v111", "v112", "v113", "v114", "v115", "v116", "v117", "v118", "v119", "v120", "v121", "v122", "v123", "v124", "v125", "v126", "v127", "v128", "v129", "v130", "v131", "v132", "v133", "v134", "v135", "v136", "v137", "v138", "v139", "v140", "v141", "v142", "v143", "v144", "v145", "v146", "v147", "v148", "v149", "v150", "v151", "v152", "v153", "v154", "v155", "v156", "v157", "v158", "v159", "v160", "v161", "v162", "v163", "v164", "v165", "v166", "v167", "v168", "v169", "v170", "v171", "v172", "v173", "v174", "v175", "v176", "v177", "v178", "v179", "v180", "v181", "v182", "v183", "v184", "v185", "v186", "v187", "v188", "v189", "v190", "v191", "v192", "v193", "v194", "v195", "v196", "v197", "v198", "v199", "v200", "v201", "v202", "v203", "v204", "v205", "v206", "v207", "v208", "v209", "v210", "v211", "v212", "v213", "v214", "v215", "v216", "v217", "v218", "v219", "v220", "v221", "v222", "v223", "v224", "v225", "v226", "v227", "v228", "v229", "v230", "v231", "v232", "v233", "v234", "v235", "v236", "v237", "v238", "v239", "v240", "v241", "v242", "v243", "v244", "v245", "v246", "v247", "v248", "v249", "v250", "v251", "v252", "v253", "v254", "v255", "s38", "s39", "s42", "s43", "s46", "s47", "s50", "s51", "s54", "s55", "s58", "s59", "s60", "s61", "s62", "s63", "s64", "s65", "s66", "s67", "s68", "s69", "s70", "s71", "s72", "s73", "s74", "s75", "s76", "s77", "s78", "s79", "s80", "s81", "s82", "s83", "vcc", "scc", "memory");
  if(flag){
    const int lane=__builtin_amdgcn_mbcnt_hi(~0u,__builtin_amdgcn_mbcnt_lo(~0u,0u));
    #pragma nounroll
    for(int it=(flag&1)?0:1;it<2;++it) slow_item(x,kc,vc,wq,ows,b,2*hp+it,G0+w,lane);
    __builtin_amdgcn_fence(__ATOMIC_RELEASE,"agent");
    asm volatile("s_waitcnt vmcnt(0)":::"memory");
    const int lr=32*w+16*(lane&1);
    if(lane<4 && lr>=rowlo && lr<rowhi)
      __hip_atomic_store((u64*)(flags+myfl+(lane>>1)*4096+(lane&1)*8), tag, __ATOMIC_RELAXED, __HIP_MEMORY_SCOPE_AGENT);
    asm volatile(
    "s_waitcnt vmcnt(0) lgkmcnt(0)\n\t"
    "s_mov_b32 s66, %[w]\n\t"
    "s_lshl_b32 s61, s66, 11\n\t"
    "s_and_b32 s57, s57, 0xffff\n\t"
    "s_mov_b32 s58, 131072\n\t"
    "s_mov_b32 s59, 0x20000\n\t"
    "s_mov_b64 s[72:73], %[tagin]\n\t"
    "v_mbcnt_lo_u32_b32 v252, -1, 0\n\t"
    "v_mbcnt_hi_u32_b32 v252, -1, v252\n\t"
    "v_lshlrev_b32 v252, 4, v252\n\t"
    "v_mbcnt_lo_u32_b32 v200, -1, 0\n\t"
    "v_mbcnt_hi_u32_b32 v200, -1, v200\n\t"
    "v_bfe_u32 v253, v200, 4, 1\n\t"
    "v_lshlrev_b32 v253, 2, v253\n\t"
    "v_bfe_u32 v254, v200, 5, 1\n\t"
    "v_lshl_add_u32 v253, v254, 1, v253\n\t"
    "v_and_b32 v254, 7, v200\n\t"
    "v_xor_b32 v253, v253, v254\n\t"
    "v_lshlrev_b32 v253, 4, v253\n\t"
    "v_lshrrev_b32 v254, 3, v200\n\t"
    "s_lshl_b32 s65, s66, 3\n\t"
    "v_add_u32 v254, s65, v254\n\t"
    "v_lshl_add_u32 v255, v254, 10, v253\n\t"
    "s_mov_b64 s[36:37], %[wo]\n\t"
    "s_and_b32 s37, s37, 0xffff\n\t"
    "s_mov_b32 s38, 4194304\n\t"
    "s_mov_b32 s39, 0x20000\n\t"
    "s_mov_b64 s[40:41], %[ofr]\n\t"
    "s_and_b32 s41, s41, 0xffff\n\t"
    "s_mov_b32 s42, %[onrec]\n\t"
    "s_mov_b32 s43, 0x20000\n\t"
    "s_bfe_u32 s65, %[wgi], 0x10007\n\t"
    "s_mul_i32 s74, s65, 34816\n\t"
    "s_lshl_b32 s77, s65, 7\n\t"
    "s_lshl_b32 s75, s65, 19\n\t"
    "s_xor_b32 s76, s77, 128\n\t"
    "s_add_u32 s76, s76, s75\n\t"
    "s_add_u32 s75, s75, s77\n\t"
    "v_mbcnt_lo_u32_b32 v192, -1, 0\n\t"
    "v_mbcnt_hi_u32_b32 v192, -1, v192\n\t"
    "v_bfe_u32 v253, v192, 1, 1\n\t"
    "v_bfe_u32 v254, v192, 3, 1\n\t"
    "v_lshl_add_u32 v253, v254, 2, v253\n\t"
    "v_bfe_u32 v254, v192, 4, 1\n\t"
    "v_lshl_add_u32 v253, v254, 1, v253\n\t"
    "s_and_b32 s65, s66, 4\n\t"
    "v_xor_b32 v253, s65, v253\n\t"
    "v_lshlrev_b32 v253, 4, v253\n\t"
    "v_bfe_u32 v254, v192, 2, 2\n\t"
    "v_lshlrev_b32 v254, 7, v254\n\t"
    "v_and_b32 v193, 1, v192\n\t"
    "v_lshl_add_u32 v254, v193, 3, v254\n\t"
    "v_bfe_u32 v193, v192, 5, 1\n\t"
    "v_lshl_add_u32 v254, v193, 11, v254\n\t"
    "v_bfe_u32 v193, v192, 4, 1\n\t"
    "v_lshl_add_u32 v254, v193, 9, v254\n\t"
    "s_and_b32 s65, s66, 4\n\t"
    "s_lshl_b32 s65, s65, 12\n\t"
    "v_add_u32 v254, s65, v254\n\t"
    "v_xor_b32 v240, 0, v253\n\t"
    "s_mov_b32 s65, 0\n\t"
    "v_add3_u32 v240, v240, v254, s65\n\t"
    "v_xor_b32 v241, 32, v253\n\t"
    "s_mov_b32 s65, 0\n\t"
    "v_add3_u32 v241, v241, v254, s65\n\t"
    "v_xor_b32 v242, 64, v253\n\t"
    "s_mov_b32 s65, 0\n\t"
    "v_add3_u32 v242, v242, v254, s65\n\t"
    "v_xor_b32 v243, 96, v253\n\t"
    "s_mov_b32 s65, 0\n\t"
    "v_add3_u32 v243, v243, v254, s65\n\t"
    "s_lshr_b32 s70, s77, 2\n\t"
    "s_cmp_eq_u32 %[bump], 1073741824\n\t"
    "s_cselect_b32 s65, 0, 32\n\t"
    "s_cselect_b32 s70, s70, 0\n\t"
    "v_and_b32 v206, 0x100, v252\n\t"
    "v_mul_u32_u24 v206, s65, v206\n\t"
    "s_or_b32 s65, s65, 0xd0\n\t"
    "v_and_b32 v205, s65, v252\n\t"
    "v_or3_b32 v205, v205, v206, s70\n\t"
    "v_mov_b32 v206, s72\n\t"
    "v_mov_b32 v207, s73\n\t"
    "s_add_u32 s63, %[pollfl], 0\n\t"
    "buffer_load_dwordx4 v[200:203], v205, s[56:59], s63 offen sc1\n\t"
    "s_waitcnt vmcnt(0)\n\t"
    "v_xor_b32 v200, v200, v206\n\t"
    "v_xor_b32 v201, v201, v207\n\t"
    "v_xor_b32 v202, v202, v206\n\t"
    "v_xor_b32 v203, v203, v207\n\t"
    "v_or3_b32 v200, v200, v201, v202\n\t"
    "v_or_b32 v200, v200, v203\n\t"
    "v_cmp_ne_u32 vcc, 0, v200\n\t"
    "s_nop 4\n\t"
    "s_cbranch_vccz PGO0_%=\n\t"
    "s_mov_b32 s70, 0x200000\n\t"
    "PSP0_%=:\n\t"
    "s_sleep 4\n\t"
    "s_add_u32 s63, %[pollfl], 0\n\t"
    "buffer_load_dwordx4 v[200:203], v205, s[56:59], s63 offen sc1\n\t"
    "s_waitcnt vmcnt(0)\n\t"
    "v_xor_b32 v200, v200, v206\n\t"
    "v_xor_b32 v201, v201, v207\n\t"
    "v_xor_b32 v202, v202, v206\n\t"
    "v_xor_b32 v203, v203, v207\n\t"
    "v_or3_b32 v200, v200, v201, v202\n\t"
    "v_or_b32 v200, v200, v203\n\t"
    "v_cmp_ne_u32 vcc, 0, v200\n\t"
    "s_sub_u32 s70, s70, 1\n\t"
    "s_cbranch_scc1 PGO0_%=\n\t"
    "s_nop 4\n\t"
    "s_cbranch_vccnz PSP0_%=\n\t"
    "PGO0_%=:\n\t"
    "s_nop 1\n\t"
    "v_mov_b32 v204, 0\n\t"
    "buffer_store_dwordx2 v[206:207], v204, s[56:59], %[bump] offen sc1\n\t"
    "s_add_u32 s63, s74, 0\n\t"
    "buffer_load_dwordx4 v[64:67], v252, s[40:43], s63 offen sc1\n\t"
    "s_add_u32 s63, s74, 0\n\t"
    "buffer_load_dwordx4 v[68:71], v252, s[40:43], s63 offen offset:1024 sc1\n\t"
    "s_add_u32 s63, s74, 0\n\t"
    "buffer_load_dwordx4 v[80:83], v252, s[40:43], s63 offen offset:2048 sc1\n\t"
    "s_add_u32 s63, s74, 0\n\t"
    "buffer_load_dwordx4 v[84:87], v252, s[40:43], s63 offen offset:3072 sc1\n\t"
    "s_add_u32 s63, s74, 4096\n\t"
    "buffer_load_dwordx4 v[96:99], v252, s[40:43], s63 offen sc1\n\t"
    "s_add_u32 s63, s74, 4096\n\t"
    "buffer_load_dwordx4 v[100:103], v252, s[40:43], s63 offen offset:1024 sc1\n\t"
    "s_add_u32 s63, s74, 4096\n\t"
    "buffer_load_dwordx4 v[112:115], v252, s[40:43], s63 offen offset:2048 sc1\n\t"
    "s_add_u32 s63, s74, 4096\n\t"
    "buffer_load_dwordx4 v[116:119], v252, s[40:43], s63 offen offset:3072 sc1\n\t"
    "s_add_u32 s63, s74, 69632\n\t"
    "buffer_load_dwordx4 v[0:3], v252, s[40:43], s63 offen sc1\n\t"
    "s_add_u32 s63, s74, 69632\n\t"
    "buffer_load_dwordx4 v[4:7], v252, s[40:43], s63 offen offset:1024 sc1\n\t"
    "s_add_u32 s63, s74, 69632\n\t"
    "buffer_load_dwordx4 v[16:19], v252, s[40:43], s63 offen offset:2048 sc1\n\t"
    "s_add_u32 s63, s74, 69632\n\t"
    "buffer_load_dwordx4 v[20:23], v252, s[40:43], s63 offen offset:3072 sc1\n\t"
    "s_add_u32 s63, s74, 73728\n\t"
    "buffer_load_dwordx4 v[32:35], v252, s[40:43], s63 offen sc1\n\t"
    "s_add_u32 s63, s74, 73728\n\t"
    "buffer_load_dwordx4 v[36:39], v252, s[40:43], s63 offen offset:1024 sc1\n\t"
    "s_add_u32 s63, s74, 73728\n\t"
    "buffer_load_dwordx4 v[48:51], v252, s[40:43], s63 offen offset:2048 sc1\n\t"
    "s_add_u32 s63, s74, 73728\n\t"
    "buffer_load_dwordx4 v[52:55], v252, s[40:43], s63 offen offset:3072 sc1\n\t"
    "s_add_u32 s63, s74, 139264\n\t"
    "buffer_load_dwordx4 v[136:139], v252, s[40:43], s63 offen sc1\n\t"
    "s_add_u32 s63, s74, 139264\n\t"
    "buffer_load_dwordx4 v[140:143], v252, s[40:43], s63 offen offset:1024 sc1\n\t"
    "s_add_u32 s63, s74, 139264\n\t"
    "buffer_load_dwordx4 v[152:155], v252, s[40:43], s63 offen offset:2048 sc1\n\t"
    "s_add_u32 s63, s74, 139264\n\t"
    "buffer_load_dwordx4 v[156:159], v252, s[40:43], s63 offen offset:3072 sc1\n\t"
    "s_add_u32 s63, s74, 143360\n\t"
    "buffer_load_dwordx4 v[168:171], v252, s[40:43], s63 offen sc1\n\t"
    "s_add_u32 s63, s74, 143360\n\t"
    "buffer_load_dwordx4 v[172:175], v252, s[40:43], s63 offen offset:1024 sc1\n\t"
    "s_add_u32 s63, s74, 143360\n\t"
    "buffer_load_dwordx4 v[128:131], v252, s[40:43], s63 offen offset:2048 sc1\n\t"
    "s_add_u32 s63, s74, 143360\n\t"
    "buffer_load_dwordx4 v[132:135], v252, s[40:43], s63 offen offset:3072 sc1\n\t"
    "s_add_u32 s63, s74, 208896\n\t"
    "buffer_load_dwordx4 v[144:147], v252, s[40:43], s63 offen sc1\n\t"
    "s_add_u32 s63, s74, 208896\n\t"
    "buffer_load_dwordx4 v[148:151], v252, s[40:43], s63 offen offset:1024 sc1\n\t"
    "s_add_u32 s63, s74, 208896\n\t"
    "buffer_load_dwordx4 v[160:163], v252, s[40:43], s63 offen offset:2048 sc1\n\t"
    "s_add_u32 s63, s74, 208896\n\t"
    "buffer_load_dwordx4 v[164:167], v252, s[40:43], s63 offen offset:3072 sc1\n\t"
    "s_barrier\n\t"
    "s_setprio 3\n\t"
    "ds_read_b64_tr_b16 v[8:9], v240 offset:0\n\t"
    "ds_read_b64_tr_b16 v[10:11], v240 offset:4096\n\t"
    "ds_read_b64_tr_b16 v[12:13], v241 offset:0\n\t"
    "ds_read_b64_tr_b16 v[14:15], v241 offset:4096\n\t"
    "ds_read_b64_tr_b16 v[24:25], v240 offset:8192\n\t"
    "ds_read_b64_tr_b16 v[26:27], v240 offset:12288\n\t"
    "ds_read_b64_tr_b16 v[28:29], v241 offset:8192\n\t"
    "ds_read_b64_tr_b16 v[30:31], v241 offset:12288\n\t"
    "ds_read_b64_tr_b16 v[40:41], v242 offset:0\n\t"
    "ds_read_b64_tr_b16 v[42:43], v242 offset:4096\n\t"
    "ds_read_b64_tr_b16 v[44:45], v243 offset:0\n\t"
    "ds_read_b64_tr_b16 v[46:47], v243 offset:4096\n\t"
    "ds_read_b64_tr_b16 v[56:57], v242 offset:8192\n\t"
    "ds_read_b64_tr_b16 v[58:59], v242 offset:12288\n\t"
    "s_waitcnt vmcnt(27) lgkmcnt(10)\n\t"
    "v_mfma_f32_16x16x32_bf16 v[192:195], v[8:11], v[64:67], 0\n\t"
    "ds_read_b64_tr_b16 v[60:61], v243 offset:8192\n\t"
    "s_waitcnt vmcnt(26)\n\t"
    "v_mfma_f32_16x16x32_bf16 v[200:203], v[8:11], v[68:71], 0\n\t"
    "ds_read_b64_tr_b16 v[62:63], v243 offset:12288\n\t"
    "v_mfma_f32_16x16x32_bf16 v[196:199], v[12:15], v[64:67], 0\n\t"
    "ds_read_b64_tr_b16 v[8:9], v240 offset:1024\n\t"
    "s_add_u32 s63, s74, 212992\n\t"
    "buffer_load_dwordx4 v[72:75], v252, s[40:43], s63 offen sc1\n\t"
    "v_mfma_f32_16x16x32_bf16 v[204:207], v[12:15], v[68:71], 0\n\t"
    "ds_read_b64_tr_b16 v[10:11], v240 offset:5120\n\t"
    "s_add_u32 m0, s61, 65536\n\t"
    "s_add_u32 s63, s75, 2097152\n\t"
    "buffer_load_dwordx4 v255, s[36:39], s63 offen lds\n\t"
    "s_waitcnt vmcnt(27) lgkmcnt(10)\n\t"
    "v_mfma_f32_16x16x32_bf16 v[192:195], v[24:27], v[80:83], v[192:195]\n\t"
    "ds_read_b64_tr_b16 v[12:13], v241 offset:1024\n\t"
    "s_waitcnt vmcnt(26)\n\t"
    "v_mfma_f32_16x16x32_bf16 v[200:203], v[24:27], v[84:87], v[200:203]\n\t"
    "ds_read_b64_tr_b16 v[14:15], v241 offset:5120\n\t"
    "v_mfma_f32_16x16x32_bf16 v[196:199], v[28:31], v[80:83], v[196:199]\n\t"
    "ds_read_b64_tr_b16 v[24:25], v240 offset:9216\n\t"
    "v_mfma_f32_16x16x32_bf16 v[204:207], v[28:31], v[84:87], v[204:207]\n\t"
    "ds_read_b64_tr_b16 v[26:27], v240 offset:13312\n\t"
    "s_add_u32 m0, s61, 66560\n\t"
    "s_add_u32 s63, s76, 2097152\n\t"
    "buffer_load_dwordx4 v255, s[36:39], s63 offen lds\n\t"
    "s_waitcnt lgkmcnt(10)\n\t"
    "v_mfma_f32_16x16x32_bf16 v[208:211], v[40:43], v[64:67], 0\n\t"
    "ds_read_b64_tr_b16 v[28:29], v241 offset:9216\n\t"
    "v_add_u32 v104, 131072, v240\n\t"
    "v_add_u32 v105, 131072, v241\n\t"
    "v_add_u32 v106, 131072, v242\n\t"
    "v_add_u32 v107, 131072, v243\n\t"
    "v_add_u32 v244, 65536, v240\n\t"
    "v_mfma_f32_16x16x32_bf16 v[216:219], v[40:43], v[68:71], 0\n\t"
    "ds_read_b64_tr_b16 v[30:31], v241 offset:13312\n\t"
    "v_add_u32 v245, 65536, v241\n\t"
    "v_add_u32 v246, 65536, v242\n\t"
    "v_add_u32 v247, 65536, v243\n\t"
    "s_add_u32 s63, s74, 212992\n\t"
    "buffer_load_dwordx4 v[76:79], v252, s[40:43], s63 offen offset:1024 sc1\n\t"
    "v_mfma_f32_16x16x32_bf16 v[212:215], v[44:47], v[64:67], 0\n\t"
    "ds_read_b64_tr_b16 v[40:41], v242 offset:1024\n\t"
    "v_mfma_f32_16x16x32_bf16 v[220:223], v[44:47], v[68:71], 0\n\t"
    "ds_read_b64_tr_b16 v[42:43], v242 offset:5120\n\t"
    "s_add_u32 m0, s61, 81920\n\t"
    "s_add_u32 s63, s75, 2359296\n\t"
    "buffer_load_dwordx4 v255, s[36:39], s63 offen lds\n\t"
    "s_waitcnt lgkmcnt(10)\n\t"
    "v_mfma_f32_16x16x32_bf16 v[208:211], v[56:59], v[80:83], v[208:211]\n\t"
    "ds_read_b64_tr_b16 v[44:45], v243 offset:1024\n\t"
    "s_setprio 2\n\t"
    "v_mfma_f32_16x16x32_bf16 v[216:219], v[56:59], v[84:87], v[216:219]\n\t"
    "ds_read_b64_tr_b16 v[46:47], v243 offset:5120\n\t"
    "v_mfma_f32_16x16x32_bf16 v[212:215], v[60:63], v[80:83], v[212:215]\n\t"
    "ds_read_b64_tr_b16 v[56:57], v242 offset:9216\n\t"
    "s_add_u32 s63, s74, 212992\n\t"
    "buffer_load_dwordx4 v[88:91], v252, s[40:43], s63 offen offset:2048 sc1\n\t"
    "v_mfma_f32_16x16x32_bf16 v[220:223], v[60:63], v[84:87], v[220:223]\n\t"
    "ds_read_b64_tr_b16 v[58:59], v242 offset:13312\n\t"
    "s_add_u32 m0, s61, 82944\n\t"
    "s_add_u32 s63, s76, 2359296\n\t"
    "buffer_load_dwordx4 v255, s[36:39], s63 offen lds\n\t"
    "s_waitcnt lgkmcnt(10)\n\t"
    "v_mfma_f32_16x16x32_bf16 v[224:227], v[8:11], v[64:67], 0\n\t"
    "ds_read_b64_tr_b16 v[60:61], v243 offset:9216\n\t"
    "v_mfma_f32_16x16x32_bf16 v[232:235], v[8:11], v[68:71], 0\n\t"
    "ds_read_b64_tr_b16 v[62:63], v243 offset:13312\n\t"
    "v_mfma_f32_16x16x32_bf16 v[228:231], v[12:15], v[64:67], 0\n\t"
    "v_mfma_f32_16x16x32_bf16 v[236:239], v[12:15], v[68:71], 0\n\t"
    "s_waitcnt lgkmcnt(8)\n\t"
    "v_mfma_f32_16x16x32_bf16 v[224:227], v[24:27], v[80:83], v[224:227]\n\t"
    "s_setprio 1\n\t"
    "s_add_u32 s63, s74, 212992\n\t"
    "buffer_load_dwordx4 v[92:95], v252, s[40:43], s63 offen offset:3072 sc1\n\t"
    "v_mfma_f32_16x16x32_bf16 v[232:235], v[24:27], v[84:87], v[232:235]\n\t"
    "s_waitcnt lgkmcnt(0)\n\t"
    "s_barrier\n\t"
    "s_setprio 3\n\t"
    "ds_read_b64_tr_b16 v[8:9], v104 offset:0\n\t"
    "ds_read_b64_tr_b16 v[10:11], v104 offset:4096\n\t"
    "ds_read_b64_tr_b16 v[12:13], v105 offset:0\n\t"
    "ds_read_b64_tr_b16 v[14:15], v105 offset:4096\n\t"
    "v_mfma_f32_16x16x32_bf16 v[228:231], v[28:31], v[80:83], v[228:231]\n\t"
    "ds_read_b64_tr_b16 v[24:25], v106 offset:0\n\t"
    "v_mfma_f32_16x16x32_bf16 v[236:239], v[28:31], v[84:87], v[236:239]\n\t"
    "ds_read_b64_tr_b16 v[26:27], v106 offset:4096\n\t"
    "v_mfma_f32_16x16x32_bf16 v[176:179], v[40:43], v[64:67], 0\n\t"
    "ds_read_b64_tr_b16 v[28:29], v107 offset:0\n\t"
    "v_mfma_f32_16x16x32_bf16 v[184:187], v[40:43], v[68:71], 0\n\t"
    "ds_read_b64_tr_b16 v[30:31], v107 offset:4096\n\t"
    "v_mfma_f32_16x16x32_bf16 v[180:183], v[44:47], v[64:67], 0\n\t"
    "ds_read_b64_tr_b16 v[40:41], v104 offset:1024\n\t"
    "s_setprio 0\n\t"
    "v_mfma_f32_16x16x32_bf16 v[188:191], v[44:47], v[68:71], 0\n\t"
    "ds_read_b64_tr_b16 v[42:43], v104 offset:5120\n\t"
    "v_mfma_f32_16x16x32_bf16 v[176:179], v[56:59], v[80:83], v[176:179]\n\t"
    "ds_read_b64_tr_b16 v[44:45], v105 offset:1024\n\t"
    "v_mfma_f32_16x16x32_bf16 v[184:187], v[56:59], v[84:87], v[184:187]\n\t"
    "ds_read_b64_tr_b16 v[46:47], v105 offset:5120\n\t"
    "v_mfma_f32_16x16x32_bf16 v[180:183], v[60:63], v[80:83], v[180:183]\n\t"
    "ds_read_b64_tr_b16 v[56:57], v106 offset:1024\n\t"
    "v_mfma_f32_16x16x32_bf16 v[188:191], v[60:63], v[84:87], v[188:191]\n\t"
    "ds_read_b64_tr_b16 v[58:59], v106 offset:5120\n\t"
    "s_waitcnt vmcnt(31) lgkmcnt(10)\n\t"
    "v_mfma_f32_16x16x32_bf16 v[192:195], v[8:11], v[96:99], v[192:195]\n\t"
    "ds_read_b64_tr_b16 v[60:61], v107 offset:1024\n\t"
    "s_waitcnt vmcnt(30)\n\t"
    "v_mfma_f32_16x16x32_bf16 v[200:203], v[8:11], v[100:103], v[200:203]\n\t"
    "ds_read_b64_tr_b16 v[62:63], v107 offset:5120\n\t"
    "v_mfma_f32_16x16x32_bf16 v[196:199], v[12:15], v[96:99], v[196:199]\n\t"
    "ds_read_b64_tr_b16 v[8:9], v104 offset:8192\n\t"
    "s_setprio 2\n\t"
    "v_mfma_f32_16x16x32_bf16 v[204:207], v[12:15], v[100:103], v[204:207]\n\t"
    "ds_read_b64_tr_b16 v[10:11], v104 offset:12288\n\t"
    "s_waitcnt lgkmcnt(10)\n\t"
    "v_mfma_f32_16x16x32_bf16 v[208:211], v[24:27], v[96:99], v[208:211]\n\t"
    "ds_read_b64_tr_b16 v[12:13], v105 offset:8192\n\t"
    "v_mfma_f32_16x16x32_bf16 v[216:219], v[24:27], v[100:103], v[216:219]\n\t"
    "ds_read_b64_tr_b16 v[14:15], v105 offset:12288\n\t"
    "v_mfma_f32_16x16x32_bf16 v[212:215], v[28:31], v[96:99], v[212:215]\n\t"
    "ds_read_b64_tr_b16 v[24:25], v106 offset:8192\n\t"
    "v_mfma_f32_16x16x32_bf16 v[220:223], v[28:31], v[100:103], v[220:223]\n\t"
    "ds_read_b64_tr_b16 v[26:27], v106 offset:12288\n\t"
    "s_waitcnt lgkmcnt(10)\n\t"
    "v_mfma_f32_16x16x32_bf16 v[224:227], v[40:43], v[96:99], v[224:227]\n\t"
    "ds_read_b64_tr_b16 v[28:29], v107 offset:8192\n\t"
    "v_mfma_f32_16x16x32_bf16 v[232:235], v[40:43], v[100:103], v[232:235]\n\t"
    "ds_read_b64_tr_b16 v[30:31], v107 offset:12288\n\t"
    "v_mfma_f32_16x16x32_bf16 v[228:231], v[44:47], v[96:99], v[228:231]\n\t"
    "ds_read_b64_tr_b16 v[40:41], v104 offset:9216\n\t"
    "s_setprio 1\n\t"
    "v_mfma_f32_16x16x32_bf16 v[236:239], v[44:47], v[100:103], v[236:239]\n\t"
    "ds_read_b64_tr_b16 v[42:43], v104 offset:13312\n\t"
    "s_waitcnt lgkmcnt(10)\n\t"
    "v_mfma_f32_16x16x32_bf16 v[176:179], v[56:59], v[96:99], v[176:179]\n\t"
    "ds_read_b64_tr_b16 v[44:45], v105 offset:9216\n\t"
    "v_mfma_f32_16x16x32_bf16 v[184:187], v[56:59], v[100:103], v[184:187]\n\t"
    "ds_read_b64_tr_b16 v[46:47], v105 offset:13312\n\t"
    "v_mfma_f32_16x16x32_bf16 v[180:183], v[60:63], v[96:99], v[180:183]\n\t"
    "ds_read_b64_tr_b16 v[56:57], v106 offset:9216\n\t"
    "v_mfma_f32_16x16x32_bf16 v[188:191], v[60:63], v[100:103], v[188:191]\n\t"
    "ds_read_b64_tr_b16 v[58:59], v106 offset:13312\n\t"
    "s_waitcnt vmcnt(29) lgkmcnt(10)\n\t"
    "v_mfma_f32_16x16x32_bf16 v[192:195], v[8:11], v[112:115], v[192:195]\n\t"
    "ds_read_b64_tr_b16 v[60:61], v107 offset:9216\n\t"
    "s_setprio 0\n\t"
    "s_waitcnt vmcnt(28)\n\t"
    "v_mfma_f32_16x16x32_bf16 v[200:203], v[8:11], v[116:119], v[200:203]\n\t"
    "ds_read_b64_tr_b16 v[62:63], v107 offset:13312\n\t"
    "v_mfma_f32_16x16x32_bf16 v[196:199], v[12:15], v[112:115], v[196:199]\n\t"
    "v_mfma_f32_16x16x32_bf16 v[204:207], v[12:15], v[116:119], v[204:207]\n\t"
    "s_waitcnt lgkmcnt(8)\n\t"
    "v_mfma_f32_16x16x32_bf16 v[208:211], v[24:27], v[112:115], v[208:211]\n\t"
    "v_mfma_f32_16x16x32_bf16 v[216:219], v[24:27], v[116:119], v[216:219]\n\t"
    "s_waitcnt lgkmcnt(0)\n\t"
    "s_barrier\n\t"
    "s_setprio 3\n\t"
    "ds_read_b64_tr_b16 v[8:9], v240 offset:32768\n\t"
    "ds_read_b64_tr_b16 v[10:11], v240 offset:36864\n\t"
    "ds_read_b64_tr_b16 v[12:13], v241 offset:32768\n\t"
    "ds_read_b64_tr_b16 v[14:15], v241 offset:36864\n\t"
    "v_mfma_f32_16x16x32_bf16 v[212:215], v[28:31], v[112:115], v[212:215]\n\t"
    "ds_read_b64_tr_b16 v[24:25], v242 offset:32768\n\t"
    "v_mfma_f32_16x16x32_bf16 v[220:223], v[28:31], v[116:119], v[220:223]\n\t"
    "ds_read_b64_tr_b16 v[26:27], v242 offset:36864\n\t"
    "v_mfma_f32_16x16x32_bf16 v[224:227], v[40:43], v[112:115], v[224:227]\n\t"
    "ds_read_b64_tr_b16 v[28:29], v243 offset:32768\n\t"
    "v_mfma_f32_16x16x32_bf16 v[232:235], v[40:43], v[116:119], v[232:235]\n\t"
    "ds_read_b64_tr_b16 v[30:31], v243 offset:36864\n\t"
    "s_add_u32 m0, s61, 0\n\t"
    "s_add_u32 s63, s75, 2162688\n\t"
    "buffer_load_dwordx4 v255, s[36:39], s63 offen lds\n\t"
    "v_mfma_f32_16x16x32_bf16 v[228:231], v[44:47], v[112:115], v[228:231]\n\t"
    "ds_read_b64_tr_b16 v[40:41], v240 offset:33792\n\t"
    "v_mfma_f32_16x16x32_bf16 v[236:239], v[44:47], v[116:119], v[236:239]\n\t"
    "ds_read_b64_tr_b16 v[42:43], v240 offset:37888\n\t"
    "v_mfma_f32_16x16x32_bf16 v[176:179], v[56:59], v[112:115], v[176:179]\n\t"
    "ds_read_b64_tr_b16 v[44:45], v241 offset:33792\n\t"
    "v_mfma_f32_16x16x32_bf16 v[184:187], v[56:59], v[116:119], v[184:187]\n\t"
    "ds_read_b64_tr_b16 v[46:47], v241 offset:37888\n\t"
    "s_add_u32 m0, s61, 1024\n\t"
    "s_add_u32 s63, s76, 2162688\n\t"
    "buffer_load_dwordx4 v255, s[36:39], s63 offen lds\n\t"
    "v_mfma_f32_16x16x32_bf16 v[180:183], v[60:63], v[112:115], v[180:183]\n\t"
    "ds_read_b64_tr_b16 v[56:57], v242 offset:33792\n\t"
    "v_mfma_f32_16x16x32_bf16 v[188:191], v[60:63], v[116:119], v[188:191]\n\t"
    "ds_read_b64_tr_b16 v[58:59], v242 offset:37888\n\t"
    "s_waitcnt vmcnt(29) lgkmcnt(10)\n\t"
    "v_mfma_f32_16x16x32_bf16 v[192:195], v[8:11], v[0:3], v[192:195]\n\t"
    "ds_read_b64_tr_b16 v[60:61], v243 offset:33792\n\t"
    "s_waitcnt vmcnt(28)\n\t"
    "v_mfma_f32_16x16x32_bf16 v[200:203], v[8:11], v[4:7], v[200:203]\n\t"
    "ds_read_b64_tr_b16 v[62:63], v243 offset:37888\n\t"
    "s_add_u32 m0, s61, 16384\n\t"
    "s_add_u32 s63, s75, 2424832\n\t"
    "buffer_load_dwordx4 v255, s[36:39], s63 offen lds\n\t"
    "v_mfma_f32_16x16x32_bf16 v[196:199], v[12:15], v[0:3], v[196:199]\n\t"
    "ds_read_b64_tr_b16 v[8:9], v240 offset:40960\n\t"
    "s_setprio 2\n\t"
    "v_mfma_f32_16x16x32_bf16 v[204:207], v[12:15], v[4:7], v[204:207]\n\t"
    "ds_read_b64_tr_b16 v[10:11], v240 offset:45056\n\t"
    "s_waitcnt lgkmcnt(10)\n\t"
    "v_mfma_f32_16x16x32_bf16 v[208:211], v[24:27], v[0:3], v[208:211]\n\t"
    "ds_read_b64_tr_b16 v[12:13], v241 offset:40960\n\t"
    "v_mfma_f32_16x16x32_bf16 v[216:219], v[24:27], v[4:7], v[216:219]\n\t"
    "ds_read_b64_tr_b16 v[14:15], v241 offset:45056\n\t"
    "s_add_u32 m0, s61, 17408\n\t"
    "s_add_u32 s63, s76, 2424832\n\t"
    "buffer_load_dwordx4 v255, s[36:39], s63 offen lds\n\t"
    "v_mfma_f32_16x16x32_bf16 v[212:215], v[28:31], v[0:3], v[212:215]\n\t"
    "ds_read_b64_tr_b16 v[24:25], v242 offset:40960\n\t"
    "v_mfma_f32_16x16x32_bf16 v[220:223], v[28:31], v[4:7], v[220:223]\n\t"
    "ds_read_b64_tr_b16 v[26:27], v242 offset:45056\n\t"
    "s_waitcnt lgkmcnt(10)\n\t"
    "v_mfma_f32_16x16x32_bf16 v[224:227], v[40:43], v[0:3], v[224:227]\n\t"
    "ds_read_b64_tr_b16 v[28:29], v243 offset:40960\n\t"
    "v_mfma_f32_16x16x32_bf16 v[232:235], v[40:43], v[4:7], v[232:235]\n\t"
    "ds_read_b64_tr_b16 v[30:31], v243 offset:45056\n\t"
    "v_mfma_f32_16x16x32_bf16 v[228:231], v[44:47], v[0:3], v[228:231]\n\t"
    "ds_read_b64_tr_b16 v[40:41], v240 offset:41984\n\t"
    "s_setprio 1\n\t"
    "v_mfma_f32_16x16x32_bf16 v[236:239], v[44:47], v[4:7], v[236:239]\n\t"
    "ds_read_b64_tr_b16 v[42:43], v240 offset:46080\n\t"
    "s_waitcnt lgkmcnt(10)\n\t"
    "v_mfma_f32_16x16x32_bf16 v[176:179], v[56:59], v[0:3], v[176:179]\n\t"
    "ds_read_b64_tr_b16 v[44:45], v241 offset:41984\n\t"
    "v_mfma_f32_16x16x32_bf16 v[184:187], v[56:59], v[4:7], v[184:187]\n\t"
    "ds_read_b64_tr_b16 v[46:47], v241 offset:46080\n\t"
    "v_mfma_f32_16x16x32_bf16 v[180:183], v[60:63], v[0:3], v[180:183]\n\t"
    "ds_read_b64_tr_b16 v[56:57], v242 offset:41984\n\t"
    "v_mfma_f32_16x16x32_bf16 v[188:191], v[60:63], v[4:7], v[188:191]\n\t"
    "ds_read_b64_tr_b16 v[58:59], v242 offset:46080\n\t"
    "s_waitcnt vmcnt(29) lgkmcnt(10)\n\t"
    "v_mfma_f32_16x16x32_bf16 v[192:195], v[8:11], v[16:19], v[192:195]\n\t"
    "ds_read_b64_tr_b16 v[60:61], v243 offset:41984\n\t"
    "s_setprio 0\n\t"
    "s_waitcnt vmcnt(28)\n\t"
    "v_mfma_f32_16x16x32_bf16 v[200:203], v[8:11], v[20:23], v[200:203]\n\t"
    "ds_read_b64_tr_b16 v[62:63], v243 offset:46080\n\t"
    "v_mfma_f32_16x16x32_bf16 v[196:199], v[12:15], v[16:19], v[196:199]\n\t"
    "v_mfma_f32_16x16x32_bf16 v[204:207], v[12:15], v[20:23], v[204:207]\n\t"
    "s_waitcnt lgkmcnt(8)\n\t"
    "v_mfma_f32_16x16x32_bf16 v[208:211], v[24:27], v[16:19], v[208:211]\n\t"
    "v_mfma_f32_16x16x32_bf16 v[216:219], v[24:27], v[20:23], v[216:219]\n\t"
    "s_waitcnt lgkmcnt(0)\n\t"
    "s_barrier\n\t"
    "s_setprio 3\n\t"
    "ds_read_b64_tr_b16 v[8:9], v244 offset:32768\n\t"
    "ds_read_b64_tr_b16 v[10:11], v244 offset:36864\n\t"
    "ds_read_b64_tr_b16 v[12:13], v245 offset:32768\n\t"
    "ds_read_b64_tr_b16 v[14:15], v245 offset:36864\n\t"
    "v_mfma_f32_16x16x32_bf16 v[212:215], v[28:31], v[16:19], v[212:215]\n\t"
    "ds_read_b64_tr_b16 v[24:25], v246 offset:32768\n\t"
    "v_mfma_f32_16x16x32_bf16 v[220:223], v[28:31], v[20:23], v[220:223]\n\t"
    "ds_read_b64_tr_b16 v[26:27], v246 offset:36864\n\t"
    "v_mfma_f32_16x16x32_bf16 v[224:227], v[40:43], v[16:19], v[224:227]\n\t"
    "ds_read_b64_tr_b16 v[28:29], v247 offset:32768\n\t"
    "v_mfma_f32_16x16x32_bf16 v[232:235], v[40:43], v[20:23], v[232:235]\n\t"
    "ds_read_b64_tr_b16 v[30:31], v247 offset:36864\n\t"
    "s_add_u32 m0, s61, 131072\n\t"
    "s_add_u32 s63, s75, 3145728\n\t"
    "buffer_load_dwordx4 v255, s[36:39], s63 offen lds\n\t"
    "v_mfma_f32_16x16x32_bf16 v[228:231], v[44:47], v[16:19], v[228:231]\n\t"
    "ds_read_b64_tr_b16 v[40:41], v244 offset:33792\n\t"
    "v_mfma_f32_16x16x32_bf16 v[236:239], v[44:47], v[20:23], v[236:239]\n\t"
    "ds_read_b64_tr_b16 v[42:43], v244 offset:37888\n\t"
    "v_mfma_f32_16x16x32_bf16 v[176:179], v[56:59], v[16:19], v[176:179]\n\t"
    "ds_read_b64_tr_b16 v[44:45], v245 offset:33792\n\t"
    "v_mfma_f32_16x16x32_bf16 v[184:187], v[56:59], v[20:23], v[184:187]\n\t"
    "ds_read_b64_tr_b16 v[46:47], v245 offset:37888\n\t"
    "s_add_u32 m0, s61, 132096\n\t"
    "s_add_u32 s63, s76, 3145728\n\t"
    "buffer_load_dwordx4 v255, s[36:39], s63 offen lds\n\t"
    "v_mfma_f32_16x16x32_bf16 v[180:183], v[60:63], v[16:19], v[180:183]\n\t"
    "ds_read_b64_tr_b16 v[56:57], v246 offset:33792\n\t"
    "v_mfma_f32_16x16x32_bf16 v[188:191], v[60:63], v[20:23], v[188:191]\n\t"
    "ds_read_b64_tr_b16 v[58:59], v246 offset:37888\n\t"
    "s_waitcnt vmcnt(29) lgkmcnt(10)\n\t"
    "v_mfma_f32_16x16x32_bf16 v[192:195], v[8:11], v[32:35], v[192:195]\n\t"
    "ds_read_b64_tr_b16 v[60:61], v247 offset:33792\n\t"
    "s_waitcnt vmcnt(28)\n\t"
    "v_mfma_f32_16x16x32_bf16 v[200:203], v[8:11], v[36:39], v[200:203]\n\t"
    "ds_read_b64_tr_b16 v[62:63], v247 offset:37888\n\t"
    "s_add_u32 m0, s61, 147456\n\t"
    "s_add_u32 s63, s75, 3407872\n\t"
    "buffer_load_dwordx4 v255, s[36:39], s63 offen lds\n\t"
    "v_mfma_f32_16x16x32_bf16 v[196:199], v[12:15], v[32:35], v[196:199]\n\t"
    "ds_read_b64_tr_b16 v[8:9], v244 offset:40960\n\t"
    "s_setprio 2\n\t"
    "v_mfma_f32_16x16x32_bf16 v[204:207], v[12:15], v[36:39], v[204:207]\n\t"
    "ds_read_b64_tr_b16 v[10:11], v244 offset:45056\n\t"
    "s_waitcnt lgkmcnt(10)\n\t"
    "v_mfma_f32_16x16x32_bf16 v[208:211], v[24:27], v[32:35], v[208:211]\n\t"
    "ds_read_b64_tr_b16 v[12:13], v245 offset:40960\n\t"
    "v_mfma_f32_16x16x32_bf16 v[216:219], v[24:27], v[36:39], v[216:219]\n\t"
    "ds_read_b64_tr_b16 v[14:15], v245 offset:45056\n\t"
    "s_add_u32 m0, s61, 148480\n\t"
    "s_add_u32 s63, s76, 3407872\n\t"
    "buffer_load_dwordx4 v255, s[36:39], s63 offen lds\n\t"
    "v_mfma_f32_16x16x32_bf16 v[212:215], v[28:31], v[32:35], v[212:215]\n\t"
    "ds_read_b64_tr_b16 v[24:25], v246 offset:40960\n\t"
    "v_mfma_f32_16x16x32_bf16 v[220:223], v[28:31], v[36:39], v[220:223]\n\t"
    "ds_read_b64_tr_b16 v[26:27], v246 offset:45056\n\t"
    "s_waitcnt lgkmcnt(10)\n\t"
    "v_mfma_f32_16x16x32_bf16 v[224:227], v[40:43], v[32:35], v[224:227]\n\t"
    "ds_read_b64_tr_b16 v[28:29], v247 offset:40960\n\t"
    "s_lshr_b32 s70, s77, 2\n\t"
    "s_cmp_eq_u32 %[bump], 1073741824\n\t"
    "s_cselect_b32 s65, 0, 32\n\t"
    "s_cselect_b32 s70, s70, 0\n\t"
    "v_and_b32 v126, 0x100, v252\n\t"
    "v_mul_u32_u24 v126, s65, v126\n\t"
    "s_or_b32 s65, s65, 0xd0\n\t"
    "v_and_b32 v125, s65, v252\n\t"
    "v_or3_b32 v125, v125, v126, s70\n\t"
    "v_mfma_f32_16x16x32_bf16 v[232:235], v[40:43], v[36:39], v[232:235]\n\t"
    "ds_read_b64_tr_b16 v[30:31], v247 offset:45056\n\t"
    "v_mfma_f32_16x16x32_bf16 v[228:231], v[44:47], v[32:35], v[228:231]\n\t"
    "ds_read_b64_tr_b16 v[40:41], v244 offset:41984\n\t"
    "s_setprio 1\n\t"
    "v_mfma_f32_16x16x32_bf16 v[236:239], v[44:47], v[36:39], v[236:239]\n\t"
    "ds_read_b64_tr_b16 v[42:43], v244 offset:46080\n\t"
    "s_waitcnt lgkmcnt(10)\n\t"
    "v_mfma_f32_16x16x32_bf16 v[176:179], v[56:59], v[32:35], v[176:179]\n\t"
    "ds_read_b64_tr_b16 v[44:45], v245 offset:41984\n\t"
    "v_mfma_f32_16x16x32_bf16 v[184:187], v[56:59], v[36:39], v[184:187]\n\t"
    "ds_read_b64_tr_b16 v[46:47], v245 offset:46080\n\t"
    "v_mfma_f32_16x16x32_bf16 v[180:183], v[60:63], v[32:35], v[180:183]\n\t"
    "ds_read_b64_tr_b16 v[56:57], v246 offset:41984\n\t"
    "v_mfma_f32_16x16x32_bf16 v[188:191], v[60:63], v[36:39], v[188:191]\n\t"
    "ds_read_b64_tr_b16 v[58:59], v246 offset:46080\n\t"
    "s_waitcnt vmcnt(29) lgkmcnt(10)\n\t"
    "v_mfma_f32_16x16x32_bf16 v[192:195], v[8:11], v[48:51], v[192:195]\n\t"
    "ds_read_b64_tr_b16 v[60:61], v247 offset:41984\n\t"
    "s_setprio 0\n\t"
    "s_waitcnt vmcnt(28)\n\t"
    "v_mfma_f32_16x16x32_bf16 v[200:203], v[8:11], v[52:55], v[200:203]\n\t"
    "ds_read_b64_tr_b16 v[62:63], v247 offset:46080\n\t"
    "v_mov_b32 v126, s72\n\t"
    "v_mov_b32 v127, s73\n\t"
    "v_mfma_f32_16x16x32_bf16 v[196:199], v[12:15], v[48:51], v[196:199]\n\t"
    "v_mfma_f32_16x16x32_bf16 v[204:207], v[12:15], v[52:55], v[204:207]\n\t"
    "s_waitcnt lgkmcnt(8)\n\t"
    "v_mfma_f32_16x16x32_bf16 v[208:211], v[24:27], v[48:51], v[208:211]\n\t"
    "v_mfma_f32_16x16x32_bf16 v[216:219], v[24:27], v[52:55], v[216:219]\n\t"
    "s_waitcnt vmcnt(9) lgkmcnt(0)\n\t"
    "s_barrier\n\t"
    "s_setprio 3\n\t"
    "ds_read_b64_tr_b16 v[8:9], v244 offset:0\n\t"
    "ds_read_b64_tr_b16 v[10:11], v244 offset:4096\n\t"
    "ds_read_b64_tr_b16 v[12:13], v245 offset:0\n\t"
    "ds_read_b64_tr_b16 v[14:15], v245 offset:4096\n\t"
    "v_mfma_f32_16x16x32_bf16 v[212:215], v[28:31], v[48:51], v[212:215]\n\t"
    "ds_read_b64_tr_b16 v[24:25], v246 offset:0\n\t"
    "v_mfma_f32_16x16x32_bf16 v[220:223], v[28:31], v[52:55], v[220:223]\n\t"
    "ds_read_b64_tr_b16 v[26:27], v246 offset:4096\n\t"
    "v_mfma_f32_16x16x32_bf16 v[224:227], v[40:43], v[48:51], v[224:227]\n\t"
    "ds_read_b64_tr_b16 v[28:29], v247 offset:0\n\t"
    "s_add_u32 s63, %[pollfl], 4096\n\t"
    "buffer_load_dwordx4 v[108:111], v125, s[56:59], s63 offen sc1\n\t"
    "v_mfma_f32_16x16x32_bf16 v[232:235], v[40:43], v[52:55], v[232:235]\n\t"
    "ds_read_b64_tr_b16 v[30:31], v247 offset:4096\n\t"
    "s_add_u32 m0, s61, 32768\n\t"
    "s_add_u32 s63, s75, 3211264\n\t"
    "buffer_load_dwordx4 v255, s[36:39], s63 offen lds\n\t"
    "v_mfma_f32_16x16x32_bf16 v[228:231], v[44:47], v[48:51], v[228:231]\n\t"
    "ds_read_b64_tr_b16 v[40:41], v244 offset:1024\n\t"
    "v_mfma_f32_16x16x32_bf16 v[236:239], v[44:47], v[52:55], v[236:239]\n\t"
    "ds_read_b64_tr_b16 v[42:43], v244 offset:5120\n\t"
    "v_mfma_f32_16x16x32_bf16 v[176:179], v[56:59], v[48:51], v[176:179]\n\t"
    "ds_read_b64_tr_b16 v[44:45], v245 offset:1024\n\t"
    "v_mfma_f32_16x16x32_bf16 v[184:187], v[56:59], v[52:55], v[184:187]\n\t"
    "ds_read_b64_tr_b16 v[46:47], v245 offset:5120\n\t"
    "s_add_u32 m0, s61, 33792\n\t"
    "s_add_u32 s63, s76, 3211264\n\t"
    "buffer_load_dwordx4 v255, s[36:39], s63 offen lds\n\t"
    "v_mfma_f32_16x16x32_bf16 v[180:183], v[60:63], v[48:51], v[180:183]\n\t"
    "ds_read_b64_tr_b16 v[56:57], v246 offset:1024\n\t"
    "v_mfma_f32_16x16x32_bf16 v[188:191], v[60:63], v[52:55], v[188:191]\n\t"
    "ds_read_b64_tr_b16 v[58:59], v246 offset:5120\n\t"
    "s_waitcnt lgkmcnt(10)\n\t"
    "v_mfma_f32_16x16x32_bf16 v[192:195], v[8:11], v[136:139], v[192:195]\n\t"
    "ds_read_b64_tr_b16 v[60:61], v247 offset:1024\n\t"
    "v_mfma_f32_16x16x32_bf16 v[200:203], v[8:11], v[140:143], v[200:203]\n\t"
    "ds_read_b64_tr_b16 v[62:63], v247 offset:5120\n\t"
    "s_add_u32 m0, s61, 49152\n\t"
    "s_add_u32 s63, s75, 3473408\n\t"
    "buffer_load_dwordx4 v255, s[36:39], s63 offen lds\n\t"
    "v_mfma_f32_16x16x32_bf16 v[196:199], v[12:15], v[136:139], v[196:199]\n\t"
    "ds_read_b64_tr_b16 v[8:9], v244 offset:8192\n\t"
    "s_setprio 2\n\t"
    "v_mfma_f32_16x16x32_bf16 v[204:207], v[12:15], v[140:143], v[204:207]\n\t"
    "ds_read_b64_tr_b16 v[10:11], v244 offset:12288\n\t"
    "s_waitcnt lgkmcnt(10)\n\t"
    "v_mfma_f32_16x16x32_bf16 v[208:211], v[24:27], v[136:139], v[208:211]\n\t"
    "ds_read_b64_tr_b16 v[12:13], v245 offset:8192\n\t"
    "v_mfma_f32_16x16x32_bf16 v[216:219], v[24:27], v[140:143], v[216:219]\n\t"
    "ds_read_b64_tr_b16 v[14:15], v245 offset:12288\n\t"
    "s_add_u32 m0, s61, 50176\n\t"
    "s_add_u32 s63, s76, 3473408\n\t"
    "buffer_load_dwordx4 v255, s[36:39], s63 offen lds\n\t"
    "v_mfma_f32_16x16x32_bf16 v[212:215], v[28:31], v[136:139], v[212:215]\n\t"
    "ds_read_b64_tr_b16 v[24:25], v246 offset:8192\n\t"
    "v_mfma_f32_16x16x32_bf16 v[220:223], v[28:31], v[140:143], v[220:223]\n\t"
    "ds_read_b64_tr_b16 v[26:27], v246 offset:12288\n\t"
    "s_waitcnt lgkmcnt(10)\n\t"
    "v_mfma_f32_16x16x32_bf16 v[224:227], v[40:43], v[136:139], v[224:227]\n\t"
    "ds_read_b64_tr_b16 v[28:29], v247 offset:8192\n\t"
    "v_mfma_f32_16x16x32_bf16 v[232:235], v[40:43], v[140:143], v[232:235]\n\t"
    "ds_read_b64_tr_b16 v[30:31], v247 offset:12288\n\t"
    "v_mfma_f32_16x16x32_bf16 v[228:231], v[44:47], v[136:139], v[228:231]\n\t"
    "ds_read_b64_tr_b16 v[40:41], v244 offset:9216\n\t"
    "s_setprio 1\n\t"
    "v_mfma_f32_16x16x32_bf16 v[236:239], v[44:47], v[140:143], v[236:239]\n\t"
    "ds_read_b64_tr_b16 v[42:43], v244 offset:13312\n\t"
    "s_waitcnt lgkmcnt(10)\n\t"
    "v_mfma_f32_16x16x32_bf16 v[176:179], v[56:59], v[136:139], v[176:179]\n\t"
    "ds_read_b64_tr_b16 v[44:45], v245 offset:9216\n\t"
    "v_mfma_f32_16x16x32_bf16 v[184:187], v[56:59], v[140:143], v[184:187]\n\t"
    "ds_read_b64_tr_b16 v[46:47], v245 offset:13312\n\t"
    "v_mfma_f32_16x16x32_bf16 v[180:183], v[60:63], v[136:139], v[180:183]\n\t"
    "ds_read_b64_tr_b16 v[56:57], v246 offset:9216\n\t"
    "v_mfma_f32_16x16x32_bf16 v[188:191], v[60:63], v[140:143], v[188:191]\n\t"
    "ds_read_b64_tr_b16 v[58:59], v246 offset:13312\n\t"
    "s_waitcnt lgkmcnt(10)\n\t"
    "v_mfma_f32_16x16x32_bf16 v[192:195], v[8:11], v[152:155], v[192:195]\n\t"
    "ds_read_b64_tr_b16 v[60:61], v247 offset:9216\n\t"
    "s_setprio 0\n\t"
    "v_mfma_f32_16x16x32_bf16 v[200:203], v[8:11], v[156:159], v[200:203]\n\t"
    "ds_read_b64_tr_b16 v[62:63], v247 offset:13312\n\t"
    "v_mfma_f32_16x16x32_bf16 v[196:199], v[12:15], v[152:155], v[196:199]\n\t"
    "v_mfma_f32_16x16x32_bf16 v[204:207], v[12:15], v[156:159], v[204:207]\n\t"
    "s_waitcnt lgkmcnt(8)\n\t"
    "v_mfma_f32_16x16x32_bf16 v[208:211], v[24:27], v[152:155], v[208:211]\n\t"
    "v_mfma_f32_16x16x32_bf16 v[216:219], v[24:27], v[156:159], v[216:219]\n\t"
    "s_waitcnt vmcnt(9) lgkmcnt(0)\n\t"
    "s_barrier\n\t"
    "s_setprio 3\n\t"
    "ds_read_b64_tr_b16 v[8:9], v240 offset:0\n\t"
    "ds_read_b64_tr_b16 v[10:11], v240 offset:4096\n\t"
    "ds_read_b64_tr_b16 v[12:13], v241 offset:0\n\t"
    "ds_read_b64_tr_b16 v[14:15], v241 offset:4096\n\t"
    "v_mfma_f32_16x16x32_bf16 v[212:215], v[28:31], v[152:155], v[212:215]\n\t"
    "ds_read_b64_tr_b16 v[24:25], v242 offset:0\n\t"
    "v_mfma_f32_16x16x32_bf16 v[220:223], v[28:31], v[156:159], v[220:223]\n\t"
    "ds_read_b64_tr_b16 v[26:27], v242 offset:4096\n\t"
    "v_mfma_f32_16x16x32_bf16 v[224:227], v[40:43], v[152:155], v[224:227]\n\t"
    "ds_read_b64_tr_b16 v[28:29], v243 offset:0\n\t"
    "s_waitcnt vmcnt(4)\n\t"
    "v_xor_b32 v108, v108, v126\n\t"
    "v_xor_b32 v109, v109, v127\n\t"
    "v_xor_b32 v110, v110, v126\n\t"
    "v_xor_b32 v111, v111, v127\n\t"
    "v_or3_b32 v108, v108, v109, v110\n\t"
    "v_or_b32 v108, v108, v111\n\t"
    "v_cmp_ne_u32 vcc, 0, v108\n\t"
    "s_nop 4\n\t"
    "s_cbranch_vccz PGO1_%=\n\t"
    "s_mov_b32 s70, 0x200000\n\t"
    "PSP1_%=:\n\t"
    "s_sleep 4\n\t"
    "s_add_u32 s63, %[pollfl], 4096\n\t"
    "buffer_load_dwordx4 v[108:111], v125, s[56:59], s63 offen sc1\n\t"
    "s_waitcnt vmcnt(0)\n\t"
    "v_xor_b32 v108, v108, v126\n\t"
    "v_xor_b32 v109, v109, v127\n\t"
    "v_xor_b32 v110, v110, v126\n\t"
    "v_xor_b32 v111, v111, v127\n\t"
    "v_or3_b32 v108, v108, v109, v110\n\t"
    "v_or_b32 v108, v108, v111\n\t"
    "v_cmp_ne_u32 vcc, 0, v108\n\t"
    "s_sub_u32 s70, s70, 1\n\t"
    "s_cbranch_scc1 PGO1_%=\n\t"
    "s_nop 4\n\t"
    "s_cbranch_vccnz PSP1_%=\n\t"
    "PGO1_%=:\n\t"
    "s_nop 1\n\t"
    "v_mfma_f32_16x16x32_bf16 v[232:235], v[40:43], v[156:159], v[232:235]\n\t"
    "ds_read_b64_tr_b16 v[30:31], v243 offset:4096\n\t"
    "s_add_u32 m0, s61, 98304\n\t"
    "s_add_u32 s63, s76, 131072\n\t"
    "buffer_load_dwordx4 v255, s[36:39], s63 offen lds\n\t"
    "v_mfma_f32_16x16x32_bf16 v[228:231], v[44:47], v[152:155], v[228:231]\n\t"
    "ds_read_b64_tr_b16 v[40:41], v240 offset:1024\n\t"
    "s_add_u32 s63, s74, 8192\n\t"
    "buffer_load_dwordx4 v[64:67], v252, s[40:43], s63 offen sc1\n\t"
    "v_mfma_f32_16x16x32_bf16 v[236:239], v[44:47], v[156:159], v[236:239]\n\t"
    "ds_read_b64_tr_b16 v[42:43], v240 offset:5120\n\t"
    "v_mfma_f32_16x16x32_bf16 v[176:179], v[56:59], v[152:155], v[176:179]\n\t"
    "ds_read_b64_tr_b16 v[44:45], v241 offset:1024\n\t"
    "v_mfma_f32_16x16x32_bf16 v[184:187], v[56:59], v[156:159], v[184:187]\n\t"
    "ds_read_b64_tr_b16 v[46:47], v241 offset:5120\n\t"
    "s_add_u32 m0, s61, 99328\n\t"
    "s_add_u32 s63, s76, 196608\n\t"
    "buffer_load_dwordx4 v255, s[36:39], s63 offen lds\n\t"
    "v_mfma_f32_16x16x32_bf16 v[180:183], v[60:63], v[152:155], v[180:183]\n\t"
    "ds_read_b64_tr_b16 v[56:57], v242 offset:1024\n\t"
    "v_mfma_f32_16x16x32_bf16 v[188:191], v[60:63], v[156:159], v[188:191]\n\t"
    "ds_read_b64_tr_b16 v[58:59], v242 offset:5120\n\t"
    "s_waitcnt lgkmcnt(10)\n\t"
    "v_mfma_f32_16x16x32_bf16 v[192:195], v[8:11], v[168:171], v[192:195]\n\t"
    "ds_read_b64_tr_b16 v[60:61], v243 offset:1024\n\t"
    "s_add_u32 s63, s74, 8192\n\t"
    "buffer_load_dwordx4 v[68:71], v252, s[40:43], s63 offen offset:1024 sc1\n\t"
    "v_mfma_f32_16x16x32_bf16 v[200:203], v[8:11], v[172:175], v[200:203]\n\t"
    "ds_read_b64_tr_b16 v[62:63], v243 offset:5120\n\t"
    "s_add_u32 m0, s61, 114688\n\t"
    "s_add_u32 s63, s76, 393216\n\t"
    "buffer_load_dwordx4 v255, s[36:39], s63 offen lds\n\t"
    "v_mfma_f32_16x16x32_bf16 v[196:199], v[12:15], v[168:171], v[196:199]\n\t"
    "ds_read_b64_tr_b16 v[8:9], v240 offset:8192\n\t"
    "s_setprio 2\n\t"
    "v_mfma_f32_16x16x32_bf16 v[204:207], v[12:15], v[172:175], v[204:207]\n\t"
    "ds_read_b64_tr_b16 v[10:11], v240 offset:12288\n\t"
    "s_waitcnt lgkmcnt(10)\n\t"
    "v_mfma_f32_16x16x32_bf16 v[208:211], v[24:27], v[168:171], v[208:211]\n\t"
    "ds_read_b64_tr_b16 v[12:13], v241 offset:8192\n\t"
    "v_mfma_f32_16x16x32_bf16 v[216:219], v[24:27], v[172:175], v[216:219]\n\t"
    "ds_read_b64_tr_b16 v[14:15], v241 offset:12288\n\t"
    "s_add_u32 m0, s61, 115712\n\t"
    "s_add_u32 s63, s76, 458752\n\t"
    "buffer_load_dwordx4 v255, s[36:39], s63 offen lds\n\t"
    "v_mfma_f32_16x16x32_bf16 v[212:215], v[28:31], v[168:171], v[212:215]\n\t"
    "ds_read_b64_tr_b16 v[24:25], v242 offset:8192\n\t"
    "s_add_u32 s63, s74, 8192\n\t"
    "buffer_load_dwordx4 v[80:83], v252, s[40:43], s63 offen offset:2048 sc1\n\t"
    "v_mfma_f32_16x16x32_bf16 v[220:223], v[28:31], v[172:175], v[220:223]\n\t"
    "ds_read_b64_tr_b16 v[26:27], v242 offset:12288\n\t"
    "s_waitcnt lgkmcnt(10)\n\t"
    "v_mfma_f32_16x16x32_bf16 v[224:227], v[40:43], v[168:171], v[224:227]\n\t"
    "ds_read_b64_tr_b16 v[28:29], v243 offset:8192\n\t"
    "v_mfma_f32_16x16x32_bf16 v[232:235], v[40:43], v[172:175], v[232:235]\n\t"
    "ds_read_b64_tr_b16 v[30:31], v243 offset:12288\n\t"
    "v_mfma_f32_16x16x32_bf16 v[228:231], v[44:47], v[168:171], v[228:231]\n\t"
    "ds_read_b64_tr_b16 v[40:41], v240 offset:9216\n\t"
    "s_setprio 1\n\t"
    "v_mfma_f32_16x16x32_bf16 v[236:239], v[44:47], v[172:175], v[236:239]\n\t"
    "ds_read_b64_tr_b16 v[42:43], v240 offset:13312\n\t"
    "s_waitcnt lgkmcnt(10)\n\t"
    "v_mfma_f32_16x16x32_bf16 v[176:179], v[56:59], v[168:171], v[176:179]\n\t"
    "ds_read_b64_tr_b16 v[44:45], v241 offset:9216\n\t"
    "s_add_u32 s63, s74, 8192\n\t"
    "buffer_load_dwordx4 v[84:87], v252, s[40:43], s63 offen offset:3072 sc1\n\t"
    "v_mfma_f32_16x16x32_bf16 v[184:187], v[56:59], v[172:175], v[184:187]\n\t"
    "ds_read_b64_tr_b16 v[46:47], v241 offset:13312\n\t"
    "v_mfma_f32_16x16x32_bf16 v[180:183], v[60:63], v[168:171], v[180:183]\n\t"
    "ds_read_b64_tr_b16 v[56:57], v242 offset:9216\n\t"
    "v_mfma_f32_16x16x32_bf16 v[188:191], v[60:63], v[172:175], v[188:191]\n\t"
    "ds_read_b64_tr_b16 v[58:59], v242 offset:13312\n\t"
    "s_waitcnt lgkmcnt(10)\n\t"
    "v_mfma_f32_16x16x32_bf16 v[192:195], v[8:11], v[128:131], v[192:195]\n\t"
    "ds_read_b64_tr_b16 v[60:61], v243 offset:9216\n\t"
    "s_setprio 0\n\t"
    "v_mfma_f32_16x16x32_bf16 v[200:203], v[8:11], v[132:135], v[200:203]\n\t"
    "ds_read_b64_tr_b16 v[62:63], v243 offset:13312\n\t"
    "v_mfma_f32_16x16x32_bf16 v[196:199], v[12:15], v[128:131], v[196:199]\n\t"
    "s_add_u32 s63, s74, 12288\n\t"
    "buffer_load_dwordx4 v[96:99], v252, s[40:43], s63 offen sc1\n\t"
    "v_mfma_f32_16x16x32_bf16 v[204:207], v[12:15], v[132:135], v[204:207]\n\t"
    "s_waitcnt lgkmcnt(8)\n\t"
    "v_mfma_f32_16x16x32_bf16 v[208:211], v[24:27], v[128:131], v[208:211]\n\t"
    "v_mfma_f32_16x16x32_bf16 v[216:219], v[24:27], v[132:135], v[216:219]\n\t"
    "s_waitcnt lgkmcnt(0)\n\t"
    "s_barrier\n\t"
    "s_setprio 3\n\t"
    "ds_read_b64_tr_b16 v[8:9], v104 offset:0\n\t"
    "ds_read_b64_tr_b16 v[10:11], v104 offset:4096\n\t"
    "ds_read_b64_tr_b16 v[12:13], v105 offset:0\n\t"
    "ds_read_b64_tr_b16 v[14:15], v105 offset:4096\n\t"
    "v_mfma_f32_16x16x32_bf16 v[212:215], v[28:31], v[128:131], v[212:215]\n\t"
    "ds_read_b64_tr_b16 v[24:25], v106 offset:0\n\t"
    "v_mfma_f32_16x16x32_bf16 v[220:223], v[28:31], v[132:135], v[220:223]\n\t"
    "ds_read_b64_tr_b16 v[26:27], v106 offset:4096\n\t"
    "v_mfma_f32_16x16x32_bf16 v[224:227], v[40:43], v[128:131], v[224:227]\n\t"
    "ds_read_b64_tr_b16 v[28:29], v107 offset:0\n\t"
    "s_add_u32 s63, s74, 12288\n\t"
    "buffer_load_dwordx4 v[100:103], v252, s[40:43], s63 offen offset:1024 sc1\n\t"
    "v_mfma_f32_16x16x32_bf16 v[232:235], v[40:43], v[132:135], v[232:235]\n\t"
    "ds_read_b64_tr_b16 v[30:31], v107 offset:4096\n\t"
    "s_add_u32 m0, s61, 65536\n\t"
    "s_add_u32 s63, s76, 1179648\n\t"
    "buffer_load_dwordx4 v255, s[36:39], s63 offen lds\n\t"
    "v_mfma_f32_16x16x32_bf16 v[228:231], v[44:47], v[128:131], v[228:231]\n\t"
    "ds_read_b64_tr_b16 v[40:41], v104 offset:1024\n\t"
    "v_mfma_f32_16x16x32_bf16 v[236:239], v[44:47], v[132:135], v[236:239]\n\t"
    "ds_read_b64_tr_b16 v[42:43], v104 offset:5120\n\t"
    "v_mfma_f32_16x16x32_bf16 v[176:179], v[56:59], v[128:131], v[176:179]\n\t"
    "ds_read_b64_tr_b16 v[44:45], v105 offset:1024\n\t"
    "v_mfma_f32_16x16x32_bf16 v[184:187], v[56:59], v[132:135], v[184:187]\n\t"
    "ds_read_b64_tr_b16 v[46:47], v105 offset:5120\n\t"
    "s_add_u32 m0, s61, 66560\n\t"
    "s_add_u32 s63, s76, 1245184\n\t"
    "buffer_load_dwordx4 v255, s[36:39], s63 offen lds\n\t"
    "v_mfma_f32_16x16x32_bf16 v[180:183], v[60:63], v[128:131], v[180:183]\n\t"
    "ds_read_b64_tr_b16 v[56:57], v106 offset:1024\n\t"
    "s_add_u32 s63, s74, 12288\n\t"
    "buffer_load_dwordx4 v[112:115], v252, s[40:43], s63 offen offset:2048 sc1\n\t"
    "v_mfma_f32_16x16x32_bf16 v[188:191], v[60:63], v[132:135], v[188:191]\n\t"
    "ds_read_b64_tr_b16 v[58:59], v106 offset:5120\n\t"
    "s_waitcnt lgkmcnt(10)\n\t"
    "v_mfma_f32_16x16x32_bf16 v[192:195], v[8:11], v[144:147], v[192:195]\n\t"
    "ds_read_b64_tr_b16 v[60:61], v107 offset:1024\n\t"
    "v_mfma_f32_16x16x32_bf16 v[200:203], v[8:11], v[148:151], v[200:203]\n\t"
    "ds_read_b64_tr_b16 v[62:63], v107 offset:5120\n\t"
    "s_add_u32 m0, s61, 81920\n\t"
    "s_add_u32 s63, s76, 1441792\n\t"
    "buffer_load_dwordx4 v255, s[36:39], s63 offen lds\n\t"
    "v_mfma_f32_16x16x32_bf16 v[196:199], v[12:15], v[144:147], v[196:199]\n\t"
    "ds_read_b64_tr_b16 v[8:9], v104 offset:8192\n\t"
    "s_setprio 2\n\t"
    "v_mfma_f32_16x16x32_bf16 v[204:207], v[12:15], v[148:151], v[204:207]\n\t"
    "ds_read_b64_tr_b16 v[10:11], v104 offset:12288\n\t"
    "s_waitcnt lgkmcnt(10)\n\t"
    "v_mfma_f32_16x16x32_bf16 v[208:211], v[24:27], v[144:147], v[208:211]\n\t"
    "ds_read_b64_tr_b16 v[12:13], v105 offset:8192\n\t"
    "s_add_u32 s63, s74, 12288\n\t"
    "buffer_load_dwordx4 v[116:119], v252, s[40:43], s63 offen offset:3072 sc1\n\t"
    "v_mfma_f32_16x16x32_bf16 v[216:219], v[24:27], v[148:151], v[216:219]\n\t"
    "ds_read_b64_tr_b16 v[14:15], v105 offset:12288\n\t"
    "s_add_u32 m0, s61, 82944\n\t"
    "s_add_u32 s63, s76, 1507328\n\t"
    "buffer_load_dwordx4 v255, s[36:39], s63 offen lds\n\t"
    "v_mfma_f32_16x16x32_bf16 v[212:215], v[28:31], v[144:147], v[212:215]\n\t"
    "ds_read_b64_tr_b16 v[24:25], v106 offset:8192\n\t"
    "v_mfma_f32_16x16x32_bf16 v[220:223], v[28:31], v[148:151], v[220:223]\n\t"
    "ds_read_b64_tr_b16 v[26:27], v106 offset:12288\n\t"
    "s_waitcnt lgkmcnt(10)\n\t"
    "v_mfma_f32_16x16x32_bf16 v[224:227], v[40:43], v[144:147], v[224:227]\n\t"
    "ds_read_b64_tr_b16 v[28:29], v107 offset:8192\n\t"
    "v_mfma_f32_16x16x32_bf16 v[232:235], v[40:43], v[148:151], v[232:235]\n\t"
    "ds_read_b64_tr_b16 v[30:31], v107 offset:12288\n\t"
    "v_mfma_f32_16x16x32_bf16 v[228:231], v[44:47], v[144:147], v[228:231]\n\t"
    "ds_read_b64_tr_b16 v[40:41], v104 offset:9216\n\t"
    "s_setprio 1\n\t"
    "v_mfma_f32_16x16x32_bf16 v[236:239], v[44:47], v[148:151], v[236:239]\n\t"
    "ds_read_b64_tr_b16 v[42:43], v104 offset:13312\n\t"
    "s_add_u32 s63, s74, 77824\n\t"
    "buffer_load_dwordx4 v[0:3], v252, s[40:43], s63 offen sc1\n\t"
    "s_waitcnt lgkmcnt(10)\n\t"
    "v_mfma_f32_16x16x32_bf16 v[176:179], v[56:59], v[144:147], v[176:179]\n\t"
    "ds_read_b64_tr_b16 v[44:45], v105 offset:9216\n\t"
    "v_mfma_f32_16x16x32_bf16 v[184:187], v[56:59], v[148:151], v[184:187]\n\t"
    "ds_read_b64_tr_b16 v[46:47], v105 offset:13312\n\t"
    "v_mfma_f32_16x16x32_bf16 v[180:183], v[60:63], v[144:147], v[180:183]\n\t"
    "ds_read_b64_tr_b16 v[56:57], v106 offset:9216\n\t"
    "v_mfma_f32_16x16x32_bf16 v[188:191], v[60:63], v[148:151], v[188:191]\n\t"
    "ds_read_b64_tr_b16 v[58:59], v106 offset:13312\n\t"
    "s_waitcnt lgkmcnt(10)\n\t"
    "v_mfma_f32_16x16x32_bf16 v[192:195], v[8:11], v[160:163], v[192:195]\n\t"
    "ds_read_b64_tr_b16 v[60:61], v107 offset:9216\n\t"
    "s_setprio 0\n\t"
    "v_mfma_f32_16x16x32_bf16 v[200:203], v[8:11], v[164:167], v[200:203]\n\t"
    "ds_read_b64_tr_b16 v[62:63], v107 offset:13312\n\t"
    "s_add_u32 s63, s74, 77824\n\t"
    "buffer_load_dwordx4 v[4:7], v252, s[40:43], s63 offen offset:1024 sc1\n\t"
    "v_mfma_f32_16x16x32_bf16 v[196:199], v[12:15], v[160:163], v[196:199]\n\t"
    "v_mfma_f32_16x16x32_bf16 v[204:207], v[12:15], v[164:167], v[204:207]\n\t"
    "s_waitcnt lgkmcnt(8)\n\t"
    "v_mfma_f32_16x16x32_bf16 v[208:211], v[24:27], v[160:163], v[208:211]\n\t"
    "v_mfma_f32_16x16x32_bf16 v[216:219], v[24:27], v[164:167], v[216:219]\n\t"
    "s_waitcnt vmcnt(18) lgkmcnt(0)\n\t"
    "s_barrier\n\t"
    "s_setprio 3\n\t"
    "ds_read_b64_tr_b16 v[8:9], v240 offset:32768\n\t"
    "ds_read_b64_tr_b16 v[10:11], v240 offset:36864\n\t"
    "ds_read_b64_tr_b16 v[12:13], v241 offset:32768\n\t"
    "ds_read_b64_tr_b16 v[14:15], v241 offset:36864\n\t"
    "v_mfma_f32_16x16x32_bf16 v[212:215], v[28:31], v[160:163], v[212:215]\n\t"
    "ds_read_b64_tr_b16 v[24:25], v242 offset:32768\n\t"
    "s_add_u32 s63, s74, 77824\n\t"
    "buffer_load_dwordx4 v[16:19], v252, s[40:43], s63 offen offset:2048 sc1\n\t"
    "v_mfma_f32_16x16x32_bf16 v[220:223], v[28:31], v[164:167], v[220:223]\n\t"
    "ds_read_b64_tr_b16 v[26:27], v242 offset:36864\n\t"
    "v_mfma_f32_16x16x32_bf16 v[224:227], v[40:43], v[160:163], v[224:227]\n\t"
    "ds_read_b64_tr_b16 v[28:29], v243 offset:32768\n\t"
    "v_mfma_f32_16x16x32_bf16 v[232:235], v[40:43], v[164:167], v[232:235]\n\t"
    "ds_read_b64_tr_b16 v[30:31], v243 offset:36864\n\t"
    "s_add_u32 m0, s61, 0\n\t"
    "s_add_u32 s63, s76, 2228224\n\t"
    "buffer_load_dwordx4 v255, s[36:39], s63 offen lds\n\t"
    "v_mfma_f32_16x16x32_bf16 v[228:231], v[44:47], v[160:163], v[228:231]\n\t"
    "ds_read_b64_tr_b16 v[40:41], v240 offset:33792\n\t"
    "v_mfma_f32_16x16x32_bf16 v[236:239], v[44:47], v[164:167], v[236:239]\n\t"
    "ds_read_b64_tr_b16 v[42:43], v240 offset:37888\n\t"
    "v_mfma_f32_16x16x32_bf16 v[176:179], v[56:59], v[160:163], v[176:179]\n\t"
    "ds_read_b64_tr_b16 v[44:45], v241 offset:33792\n\t"
    "s_add_u32 s63, s74, 77824\n\t"
    "buffer_load_dwordx4 v[20:23], v252, s[40:43], s63 offen offset:3072 sc1\n\t"
    "v_mfma_f32_16x16x32_bf16 v[184:187], v[56:59], v[164:167], v[184:187]\n\t"
    "ds_read_b64_tr_b16 v[46:47], v241 offset:37888\n\t"
    "s_add_u32 m0, s61, 1024\n\t"
    "s_add_u32 s63, s76, 2293760\n\t"
    "buffer_load_dwordx4 v255, s[36:39], s63 offen lds\n\t"
    "v_mfma_f32_16x16x32_bf16 v[180:183], v[60:63], v[160:163], v[180:183]\n\t"
    "ds_read_b64_tr_b16 v[56:57], v242 offset:33792\n\t"
    "v_mfma_f32_16x16x32_bf16 v[188:191], v[60:63], v[164:167], v[188:191]\n\t"
    "ds_read_b64_tr_b16 v[58:59], v242 offset:37888\n\t"
    "s_waitcnt lgkmcnt(10)\n\t"
    "v_mfma_f32_16x16x32_bf16 v[192:195], v[8:11], v[72:75], v[192:195]\n\t"
    "ds_read_b64_tr_b16 v[60:61], v243 offset:33792\n\t"
    "v_mfma_f32_16x16x32_bf16 v[200:203], v[8:11], v[76:79], v[200:203]\n\t"
    "ds_read_b64_tr_b16 v[62:63], v243 offset:37888\n\t"
    "s_add_u32 m0, s61, 16384\n\t"
    "s_add_u32 s63, s76, 2490368\n\t"
    "buffer_load_dwordx4 v255, s[36:39], s63 offen lds\n\t"
    "v_mfma_f32_16x16x32_bf16 v[196:199], v[12:15], v[72:75], v[196:199]\n\t"
    "ds_read_b64_tr_b16 v[8:9], v240 offset:40960\n\t"
    "s_setprio 2\n\t"
    "v_mfma_f32_16x16x32_bf16 v[204:207], v[12:15], v[76:79], v[204:207]\n\t"
    "ds_read_b64_tr_b16 v[10:11], v240 offset:45056\n\t"
    "s_add_u32 s63, s74, 81920\n\t"
    "buffer_load_dwordx4 v[32:35], v252, s[40:43], s63 offen sc1\n\t"
    "s_waitcnt lgkmcnt(10)\n\t"
    "v_mfma_f32_16x16x32_bf16 v[208:211], v[24:27], v[72:75], v[208:211]\n\t"
    "ds_read_b64_tr_b16 v[12:13], v241 offset:40960\n\t"
    "v_mfma_f32_16x16x32_bf16 v[216:219], v[24:27], v[76:79], v[216:219]\n\t"
    "ds_read_b64_tr_b16 v[14:15], v241 offset:45056\n\t"
    "s_add_u32 m0, s61, 17408\n\t"
    "s_add_u32 s63, s76, 2555904\n\t"
    "buffer_load_dwordx4 v255, s[36:39], s63 offen lds\n\t"
    "v_mfma_f32_16x16x32_bf16 v[212:215], v[28:31], v[72:75], v[212:215]\n\t"
    "ds_read_b64_tr_b16 v[24:25], v242 offset:40960\n\t"
    "v_mfma_f32_16x16x32_bf16 v[220:223], v[28:31], v[76:79], v[220:223]\n\t"
    "ds_read_b64_tr_b16 v[26:27], v242 offset:45056\n\t"
    "s_waitcnt lgkmcnt(10)\n\t"
    "v_mfma_f32_16x16x32_bf16 v[224:227], v[40:43], v[72:75], v[224:227]\n\t"
    "ds_read_b64_tr_b16 v[28:29], v243 offset:40960\n\t"
    "s_add_u32 s63, s74, 81920\n\t"
    "buffer_load_dwordx4 v[36:39], v252, s[40:43], s63 offen offset:1024 sc1\n\t"
    "v_mfma_f32_16x16x32_bf16 v[232:235], v[40:43], v[76:79], v[232:235]\n\t"
    "ds_read_b64_tr_b16 v[30:31], v243 offset:45056\n\t"
    "v_mfma_f32_16x16x32_bf16 v[228:231], v[44:47], v[72:75], v[228:231]\n\t"
    "ds_read_b64_tr_b16 v[40:41], v240 offset:41984\n\t"
    "s_setprio 1\n\t"
    "v_mfma_f32_16x16x32_bf16 v[236:239], v[44:47], v[76:79], v[236:239]\n\t"
    "ds_read_b64_tr_b16 v[42:43], v240 offset:46080\n\t"
    "s_waitcnt lgkmcnt(10)\n\t"
    "v_mfma_f32_16x16x32_bf16 v[176:179], v[56:59], v[72:75], v[176:179]\n\t"
    "ds_read_b64_tr_b16 v[44:45], v241 offset:41984\n\t"
    "v_mfma_f32_16x16x32_bf16 v[184:187], v[56:59], v[76:79], v[184:187]\n\t"
    "ds_read_b64_tr_b16 v[46:47], v241 offset:46080\n\t"
    "v_mfma_f32_16x16x32_bf16 v[180:183], v[60:63], v[72:75], v[180:183]\n\t"
    "ds_read_b64_tr_b16 v[56:57], v242 offset:41984\n\t"
    "s_add_u32 s63, s74, 81920\n\t"
    "buffer_load_dwordx4 v[48:51], v252, s[40:43], s63 offen offset:2048 sc1\n\t"
    "v_mfma_f32_16x16x32_bf16 v[188:191], v[60:63], v[76:79], v[188:191]\n\t"
    "ds_read_b64_tr_b16 v[58:59], v242 offset:46080\n\t"
    "s_waitcnt lgkmcnt(10)\n\t"
    "v_mfma_f32_16x16x32_bf16 v[192:195], v[8:11], v[88:91], v[192:195]\n\t"
    "ds_read_b64_tr_b16 v[60:61], v243 offset:41984\n\t"
    "s_setprio 0\n\t"
    "v_mfma_f32_16x16x32_bf16 v[200:203], v[8:11], v[92:95], v[200:203]\n\t"
    "ds_read_b64_tr_b16 v[62:63], v243 offset:46080\n\t"
    "v_mfma_f32_16x16x32_bf16 v[196:199], v[12:15], v[88:91], v[196:199]\n\t"
    "v_mfma_f32_16x16x32_bf16 v[204:207], v[12:15], v[92:95], v[204:207]\n\t"
    "s_waitcnt lgkmcnt(8)\n\t"
    "v_mfma_f32_16x16x32_bf16 v[208:211], v[24:27], v[88:91], v[208:211]\n\t"
    "s_add_u32 s63, s74, 81920\n\t"
    "buffer_load_dwordx4 v[52:55], v252, s[40:43], s63 offen offset:3072 sc1\n\t"
    "v_mfma_f32_16x16x32_bf16 v[216:219], v[24:27], v[92:95], v[216:219]\n\t"
    "s_waitcnt vmcnt(22) lgkmcnt(0)\n\t"
    "s_barrier\n\t"
    "s_setprio 3\n\t"
    "ds_read_b64_tr_b16 v[8:9], v244 offset:32768\n\t"
    "ds_read_b64_tr_b16 v[10:11], v244 offset:36864\n\t"
    "ds_read_b64_tr_b16 v[12:13], v245 offset:32768\n\t"
    "ds_read_b64_tr_b16 v[14:15], v245 offset:36864\n\t"
    "v_mfma_f32_16x16x32_bf16 v[212:215], v[28:31], v[88:91], v[212:215]\n\t"
    "ds_read_b64_tr_b16 v[24:25], v246 offset:32768\n\t"
    "v_mfma_f32_16x16x32_bf16 v[220:223], v[28:31], v[92:95], v[220:223]\n\t"
    "ds_read_b64_tr_b16 v[26:27], v246 offset:36864\n\t"
    "v_mfma_f32_16x16x32_bf16 v[224:227], v[40:43], v[88:91], v[224:227]\n\t"
    "ds_read_b64_tr_b16 v[28:29], v247 offset:32768\n\t"
    "v_mfma_f32_16x16x32_bf16 v[232:235], v[40:43], v[92:95], v[232:235]\n\t"
    "ds_read_b64_tr_b16 v[30:31], v247 offset:36864\n\t"
    "s_add_u32 m0, s61, 131072\n\t"
    "s_add_u32 s63, s76, 3276800\n\t"
    "buffer_load_dwordx4 v255, s[36:39], s63 offen lds\n\t"
    "v_mfma_f32_16x16x32_bf16 v[228:231], v[44:47], v[88:91], v[228:231]\n\t"
    "ds_read_b64_tr_b16 v[40:41], v244 offset:40960\n\t"
    "s_add_u32 s63, s74, 147456\n\t"
    "buffer_load_dwordx4 v[136:139], v252, s[40:43], s63 offen sc1\n\t"
    "v_mfma_f32_16x16x32_bf16 v[236:239], v[44:47], v[92:95], v[236:239]\n\t"
    "ds_read_b64_tr_b16 v[42:43], v244 offset:45056\n\t"
    "v_mfma_f32_16x16x32_bf16 v[176:179], v[56:59], v[88:91], v[176:179]\n\t"
    "ds_read_b64_tr_b16 v[44:45], v245 offset:40960\n\t"
    "v_mfma_f32_16x16x32_bf16 v[184:187], v[56:59], v[92:95], v[184:187]\n\t"
    "ds_read_b64_tr_b16 v[46:47], v245 offset:45056\n\t"
    "s_add_u32 m0, s61, 132096\n\t"
    "s_add_u32 s63, s76, 3342336\n\t"
    "buffer_load_dwordx4 v255, s[36:39], s63 offen lds\n\t"
    "v_mfma_f32_16x16x32_bf16 v[180:183], v[60:63], v[88:91], v[180:183]\n\t"
    "ds_read_b64_tr_b16 v[56:57], v246 offset:40960\n\t"
    "v_mfma_f32_16x16x32_bf16 v[188:191], v[60:63], v[92:95], v[188:191]\n\t"
    "ds_read_b64_tr_b16 v[58:59], v246 offset:45056\n\t"
    "s_waitcnt lgkmcnt(10)\n\t"
    "v_mfma_f32_16x16x32_bf16 v[224:227], v[8:11], v[64:67], v[224:227]\n\t"
    "ds_read_b64_tr_b16 v[60:61], v247 offset:40960\n\t"
    "s_add_u32 s63, s74, 147456\n\t"
    "buffer_load_dwordx4 v[140:143], v252, s[40:43], s63 offen offset:1024 sc1\n\t"
    "v_mfma_f32_16x16x32_bf16 v[232:235], v[8:11], v[68:71], v[232:235]\n\t"
    "ds_read_b64_tr_b16 v[62:63], v247 offset:45056\n\t"
    "s_add_u32 m0, s61, 147456\n\t"
    "s_add_u32 s63, s76, 3538944\n\t"
    "buffer_load_dwordx4 v255, s[36:39], s63 offen lds\n\t"
    "v_mfma_f32_16x16x32_bf16 v[228:231], v[12:15], v[64:67], v[228:231]\n\t"
    "ds_read_b64_tr_b16 v[8:9], v244 offset:33792\n\t"
    "s_setprio 2\n\t"
    "v_mfma_f32_16x16x32_bf16 v[236:239], v[12:15], v[68:71], v[236:239]\n\t"
    "ds_read_b64_tr_b16 v[10:11], v244 offset:37888\n\t"
    "s_waitcnt lgkmcnt(10)\n\t"
    "v_mfma_f32_16x16x32_bf16 v[176:179], v[24:27], v[64:67], v[176:179]\n\t"
    "ds_read_b64_tr_b16 v[12:13], v245 offset:33792\n\t"
    "v_bfe_u32 v253, v252, 4, 4\n\t"
    "v_mfma_f32_16x16x32_bf16 v[184:187], v[24:27], v[68:71], v[184:187]\n\t"
    "ds_read_b64_tr_b16 v[14:15], v245 offset:37888\n\t"
    "s_add_u32 m0, s61, 148480\n\t"
    "s_add_u32 s63, s76, 3604480\n\t"
    "buffer_load_dwordx4 v255, s[36:39], s63 offen lds\n\t"
    "v_mfma_f32_16x16x32_bf16 v[180:183], v[28:31], v[64:67], v[180:183]\n\t"
    "ds_read_b64_tr_b16 v[24:25], v246 offset:33792\n\t"
    "s_add_u32 s63, s74, 147456\n\t"
    "buffer_load_dwordx4 v[152:155], v252, s[40:43], s63 offen offset:2048 sc1\n\t"
    "v_mfma_f32_16x16x32_bf16 v[188:191], v[28:31], v[68:71], v[188:191]\n\t"
    "ds_read_b64_tr_b16 v[26:27], v246 offset:37888\n\t"
    "s_waitcnt vmcnt(28) lgkmcnt(10)\n\t"
    "v_mfma_f32_16x16x32_bf16 v[224:227], v[40:43], v[80:83], v[224:227]\n\t"
    "ds_read_b64_tr_b16 v[28:29], v247 offset:33792\n\t"
    "v_lshrrev_b32 v254, 8, v252\n\t"
    "s_waitcnt vmcnt(27)\n\t"
    "v_mfma_f32_16x16x32_bf16 v[232:235], v[40:43], v[84:87], v[232:235]\n\t"
    "ds_read_b64_tr_b16 v[30:31], v247 offset:37888\n\t"
    "v_mfma_f32_16x16x32_bf16 v[228:231], v[44:47], v[80:83], v[228:231]\n\t"
    "ds_read_b64_tr_b16 v[40:41], v244 offset:41984\n\t"
    "s_setprio 1\n\t"
    "v_mfma_f32_16x16x32_bf16 v[236:239], v[44:47], v[84:87], v[236:239]\n\t"
    "ds_read_b64_tr_b16 v[42:43], v244 offset:46080\n\t"
    "s_waitcnt lgkmcnt(10)\n\t"
    "v_mfma_f32_16x16x32_bf16 v[176:179], v[56:59], v[80:83], v[176:179]\n\t"
    "ds_read_b64_tr_b16 v[44:45], v245 offset:41984\n\t"
    "s_add_u32 s63, s74, 147456\n\t"
    "buffer_load_dwordx4 v[156:159], v252, s[40:43], s63 offen offset:3072 sc1\n\t"
    "v_mfma_f32_16x16x32_bf16 v[184:187], v[56:59], v[84:87], v[184:187]\n\t"
    "ds_read_b64_tr_b16 v[46:47], v245 offset:46080\n\t"
    "v_lshlrev_b32 v254, 4, v254\n\t"
    "v_mfma_f32_16x16x32_bf16 v[180:183], v[60:63], v[80:83], v[180:183]\n\t"
    "ds_read_b64_tr_b16 v[56:57], v246 offset:41984\n\t"
    "v_mfma_f32_16x16x32_bf16 v[188:191], v[60:63], v[84:87], v[188:191]\n\t"
    "ds_read_b64_tr_b16 v[58:59], v246 offset:46080\n\t"
    "s_waitcnt vmcnt(27) lgkmcnt(10)\n\t"
    "v_mfma_f32_16x16x32_bf16 v[224:227], v[8:11], v[96:99], v[224:227]\n\t"
    "ds_read_b64_tr_b16 v[60:61], v247 offset:41984\n\t"
    "s_setprio 0\n\t"
    "s_waitcnt vmcnt(26)\n\t"
    "v_mfma_f32_16x16x32_bf16 v[232:235], v[8:11], v[100:103], v[232:235]\n\t"
    "ds_read_b64_tr_b16 v[62:63], v247 offset:46080\n\t"
    "v_lshl_add_u32 v248, v253, 10, v254\n\t"
    "v_mfma_f32_16x16x32_bf16 v[228:231], v[12:15], v[96:99], v[228:231]\n\t"
    "s_add_u32 s63, s74, 151552\n\t"
    "buffer_load_dwordx4 v[168:171], v252, s[40:43], s63 offen sc1\n\t"
    "v_mfma_f32_16x16x32_bf16 v[236:239], v[12:15], v[100:103], v[236:239]\n\t"
    "s_waitcnt lgkmcnt(8)\n\t"
    "v_mfma_f32_16x16x32_bf16 v[176:179], v[24:27], v[96:99], v[176:179]\n\t"
    "s_xor_b32 s65, s66, 4\n\t"
    "s_lshl_b32 s65, s65, 12\n\t"
    "v_add_u32 v249, s65, v252\n\t"
    "v_mfma_f32_16x16x32_bf16 v[184:187], v[24:27], v[100:103], v[184:187]\n\t"
    "s_waitcnt vmcnt(21) lgkmcnt(0)\n\t"
    "s_barrier\n\t"
    "s_setprio 3\n\t"
    "ds_read_b64_tr_b16 v[8:9], v244 offset:0\n\t"
    "ds_read_b64_tr_b16 v[10:11], v244 offset:4096\n\t"
    "ds_read_b64_tr_b16 v[12:13], v245 offset:0\n\t"
    "ds_read_b64_tr_b16 v[14:15], v245 offset:4096\n\t"
    "v_mfma_f32_16x16x32_bf16 v[180:183], v[28:31], v[96:99], v[180:183]\n\t"
    "ds_read_b64_tr_b16 v[24:25], v246 offset:0\n\t"
    "v_mfma_f32_16x16x32_bf16 v[188:191], v[28:31], v[100:103], v[188:191]\n\t"
    "ds_read_b64_tr_b16 v[26:27], v246 offset:4096\n\t"
    "v_mfma_f32_16x16x32_bf16 v[224:227], v[40:43], v[112:115], v[224:227]\n\t"
    "ds_read_b64_tr_b16 v[28:29], v247 offset:0\n\t"
    "s_add_u32 s63, s74, 151552\n\t"
    "buffer_load_dwordx4 v[172:175], v252, s[40:43], s63 offen offset:1024 sc1\n\t"
    "v_mfma_f32_16x16x32_bf16 v[232:235], v[40:43], v[116:119], v[232:235]\n\t"
    "ds_read_b64_tr_b16 v[30:31], v247 offset:4096\n\t"
    "s_add_u32 m0, s61, 32768\n\t"
    "s_add_u32 s63, s75, 131072\n\t"
    "buffer_load_dwordx4 v255, s[36:39], s63 offen lds\n\t"
    "s_lshl_b32 s65, s66, 12\n\t"
    "v_add_u32 v250, s65, v252\n\t"
    "v_mfma_f32_16x16x32_bf16 v[228:231], v[44:47], v[112:115], v[228:231]\n\t"
    "ds_read_b64_tr_b16 v[40:41], v244 offset:8192\n\t"
    "v_mfma_f32_16x16x32_bf16 v[236:239], v[44:47], v[116:119], v[236:239]\n\t"
    "ds_read_b64_tr_b16 v[42:43], v244 offset:12288\n\t"
    "v_mfma_f32_16x16x32_bf16 v[176:179], v[56:59], v[112:115], v[176:179]\n\t"
    "ds_read_b64_tr_b16 v[44:45], v245 offset:8192\n\t"
    "s_mov_b64 s[44:45], %[out]\n\t"
    "s_and_b32 s45, s45, 0xffff\n\t"
    "s_mov_b32 s46, 0\n\t"
    "s_mov_b32 s47, 0x20000\n\t"
    "v_mfma_f32_16x16x32_bf16 v[184:187], v[56:59], v[116:119], v[184:187]\n\t"
    "ds_read_b64_tr_b16 v[46:47], v245 offset:12288\n\t"
    "s_add_u32 m0, s61, 33792\n\t"
    "s_add_u32 s63, s75, 196608\n\t"
    "buffer_load_dwordx4 v255, s[36:39], s63 offen lds\n\t"
    "v_mfma_f32_16x16x32_bf16 v[180:183], v[60:63], v[112:115], v[180:183]\n\t"
    "ds_read_b64_tr_b16 v[56:57], v246 offset:8192\n\t"
    "s_add_u32 s63, s74, 151552\n\t"
    "buffer_load_dwordx4 v[128:131], v252, s[40:43], s63 offen offset:2048 sc1\n\t"
    "v_mfma_f32_16x16x32_bf16 v[188:191], v[60:63], v[116:119], v[188:191]\n\t"
    "ds_read_b64_tr_b16 v[58:59], v246 offset:12288\n\t"
    "s_waitcnt vmcnt(24) lgkmcnt(10)\n\t"
    "v_mfma_f32_16x16x32_bf16 v[224:227], v[8:11], v[0:3], v[224:227]\n\t"
    "ds_read_b64_tr_b16 v[60:61], v247 offset:8192\n\t"
    "s_lshr_b32 s46, %[onrec], 4\n\t"
    "s_waitcnt vmcnt(23)\n\t"
    "v_mfma_f32_16x16x32_bf16 v[232:235], v[8:11], v[4:7], v[232:235]\n\t"
    "ds_read_b64_tr_b16 v[62:63], v247 offset:12288\n\t"
    "s_add_u32 m0, s61, 49152\n\t"
    "s_add_u32 s63, s75, 393216\n\t"
    "buffer_load_dwordx4 v255, s[36:39], s63 offen lds\n\t"
    "v_mfma_f32_16x16x32_bf16 v[228:231], v[12:15], v[0:3], v[228:231]\n\t"
    "ds_read_b64_tr_b16 v[8:9], v244 offset:1024\n\t"
    "s_setprio 2\n\t"
    "v_mfma_f32_16x16x32_bf16 v[236:239], v[12:15], v[4:7], v[236:239]\n\t"
    "ds_read_b64_tr_b16 v[10:11], v244 offset:5120\n\t"
    "s_waitcnt lgkmcnt(10)\n\t"
    "v_mfma_f32_16x16x32_bf16 v[176:179], v[24:27], v[0:3], v[176:179]\n\t"
    "ds_read_b64_tr_b16 v[12:13], v245 offset:1024\n\t"
    "s_add_u32 s63, s74, 151552\n\t"
    "buffer_load_dwordx4 v[132:135], v252, s[40:43], s63 offen offset:3072 sc1\n\t"
    "v_mfma_f32_16x16x32_bf16 v[184:187], v[24:27], v[4:7], v[184:187]\n\t"
    "ds_read_b64_tr_b16 v[14:15], v245 offset:5120\n\t"
    "s_add_u32 m0, s61, 50176\n\t"
    "s_add_u32 s63, s75, 458752\n\t"
    "buffer_load_dwordx4 v255, s[36:39], s63 offen lds\n\t"
    "v_mfma_f32_16x16x32_bf16 v[180:183], v[28:31], v[0:3], v[180:183]\n\t"
    "ds_read_b64_tr_b16 v[24:25], v246 offset:1024\n\t"
    "v_mfma_f32_16x16x32_bf16 v[188:191], v[28:31], v[4:7], v[188:191]\n\t"
    "ds_read_b64_tr_b16 v[26:27], v246 offset:5120\n\t"
    "s_waitcnt vmcnt(25) lgkmcnt(10)\n\t"
    "v_mfma_f32_16x16x32_bf16 v[224:227], v[40:43], v[16:19], v[224:227]\n\t"
    "ds_read_b64_tr_b16 v[28:29], v247 offset:1024\n\t"
    "s_waitcnt vmcnt(23)\n\t"
    "v_mfma_f32_16x16x32_bf16 v[232:235], v[40:43], v[20:23], v[232:235]\n\t"
    "ds_read_b64_tr_b16 v[30:31], v247 offset:5120\n\t"
    "v_mfma_f32_16x16x32_bf16 v[228:231], v[44:47], v[16:19], v[228:231]\n\t"
    "ds_read_b64_tr_b16 v[40:41], v244 offset:9216\n\t"
    "s_setprio 1\n\t"
    "v_mfma_f32_16x16x32_bf16 v[236:239], v[44:47], v[20:23], v[236:239]\n\t"
    "ds_read_b64_tr_b16 v[42:43], v244 offset:13312\n\t"
    "s_add_u32 s63, s74, 217088\n\t"
    "buffer_load_dwordx4 v[144:147], v252, s[40:43], s63 offen sc1\n\t"
    "s_waitcnt lgkmcnt(10)\n\t"
    "v_mfma_f32_16x16x32_bf16 v[176:179], v[56:59], v[16:19], v[176:179]\n\t"
    "ds_read_b64_tr_b16 v[44:45], v245 offset:9216\n\t"
    "v_mfma_f32_16x16x32_bf16 v[184:187], v[56:59], v[20:23], v[184:187]\n\t"
    "ds_read_b64_tr_b16 v[46:47], v245 offset:13312\n\t"
    "v_mfma_f32_16x16x32_bf16 v[180:183], v[60:63], v[16:19], v[180:183]\n\t"
    "ds_read_b64_tr_b16 v[56:57], v246 offset:9216\n\t"
    "v_mfma_f32_16x16x32_bf16 v[188:191], v[60:63], v[20:23], v[188:191]\n\t"
    "ds_read_b64_tr_b16 v[58:59], v246 offset:13312\n\t"
    "s_waitcnt vmcnt(21) lgkmcnt(10)\n\t"
    "v_mfma_f32_16x16x32_bf16 v[224:227], v[8:11], v[32:35], v[224:227]\n\t"
    "ds_read_b64_tr_b16 v[60:61], v247 offset:9216\n\t"
    "s_setprio 0\n\t"
    "s_waitcnt vmcnt(19)\n\t"
    "v_mfma_f32_16x16x32_bf16 v[232:235], v[8:11], v[36:39], v[232:235]\n\t"
    "ds_read_b64_tr_b16 v[62:63], v247 offset:13312\n\t"
    "s_add_u32 s63, s74, 217088\n\t"
    "buffer_load_dwordx4 v[148:151], v252, s[40:43], s63 offen offset:1024 sc1\n\t"
    "v_mfma_f32_16x16x32_bf16 v[228:231], v[12:15], v[32:35], v[228:231]\n\t"
    "v_mfma_f32_16x16x32_bf16 v[236:239], v[12:15], v[36:39], v[236:239]\n\t"
    "s_waitcnt lgkmcnt(8)\n\t"
    "v_mfma_f32_16x16x32_bf16 v[176:179], v[24:27], v[32:35], v[176:179]\n\t"
    "v_mfma_f32_16x16x32_bf16 v[184:187], v[24:27], v[36:39], v[184:187]\n\t"
    "s_waitcnt lgkmcnt(0)\n\t"
    "s_barrier\n\t"
    "s_setprio 3\n\t"
    "ds_read_b64_tr_b16 v[8:9], v240 offset:0\n\t"
    "ds_read_b64_tr_b16 v[10:11], v240 offset:4096\n\t"
    "ds_read_b64_tr_b16 v[12:13], v241 offset:0\n\t"
    "ds_read_b64_tr_b16 v[14:15], v241 offset:4096\n\t"
    "v_mfma_f32_16x16x32_bf16 v[180:183], v[28:31], v[32:35], v[180:183]\n\t"
    "ds_read_b64_tr_b16 v[24:25], v242 offset:0\n\t"
    "s_add_u32 s63, s74, 217088\n\t"
    "buffer_load_dwordx4 v[160:163], v252, s[40:43], s63 offen offset:2048 sc1\n\t"
    "v_mfma_f32_16x16x32_bf16 v[188:191], v[28:31], v[36:39], v[188:191]\n\t"
    "ds_read_b64_tr_b16 v[26:27], v242 offset:4096\n\t"
    "s_waitcnt vmcnt(20)\n\t"
    "v_mfma_f32_16x16x32_bf16 v[224:227], v[40:43], v[48:51], v[224:227]\n\t"
    "ds_read_b64_tr_b16 v[28:29], v243 offset:0\n\t"
    "s_waitcnt vmcnt(19)\n\t"
    "v_mfma_f32_16x16x32_bf16 v[232:235], v[40:43], v[52:55], v[232:235]\n\t"
    "ds_read_b64_tr_b16 v[30:31], v243 offset:4096\n\t"
    "s_add_u32 m0, s61, 98304\n\t"
    "s_add_u32 s63, s75, 1179648\n\t"
    "buffer_load_dwordx4 v255, s[36:39], s63 offen lds\n\t"
    "v_mfma_f32_16x16x32_bf16 v[228:231], v[44:47], v[48:51], v[228:231]\n\t"
    "ds_read_b64_tr_b16 v[40:41], v240 offset:8192\n\t"
    "v_mfma_f32_16x16x32_bf16 v[236:239], v[44:47], v[52:55], v[236:239]\n\t"
    "ds_read_b64_tr_b16 v[42:43], v240 offset:12288\n\t"
    "v_mfma_f32_16x16x32_bf16 v[176:179], v[56:59], v[48:51], v[176:179]\n\t"
    "ds_read_b64_tr_b16 v[44:45], v241 offset:8192\n\t"
    "s_add_u32 s63, s74, 217088\n\t"
    "buffer_load_dwordx4 v[164:167], v252, s[40:43], s63 offen offset:3072 sc1\n\t"
    "v_mfma_f32_16x16x32_bf16 v[184:187], v[56:59], v[52:55], v[184:187]\n\t"
    "ds_read_b64_tr_b16 v[46:47], v241 offset:12288\n\t"
    "s_add_u32 m0, s61, 99328\n\t"
    "s_add_u32 s63, s75, 1245184\n\t"
    "buffer_load_dwordx4 v255, s[36:39], s63 offen lds\n\t"
    "v_mfma_f32_16x16x32_bf16 v[180:183], v[60:63], v[48:51], v[180:183]\n\t"
    "ds_read_b64_tr_b16 v[56:57], v242 offset:8192\n\t"
    "v_mfma_f32_16x16x32_bf16 v[188:191], v[60:63], v[52:55], v[188:191]\n\t"
    "ds_read_b64_tr_b16 v[58:59], v242 offset:12288\n\t"
    "s_waitcnt vmcnt(20) lgkmcnt(10)\n\t"
    "v_mfma_f32_16x16x32_bf16 v[224:227], v[8:11], v[136:139], v[224:227]\n\t"
    "ds_read_b64_tr_b16 v[60:61], v243 offset:8192\n\t"
    "s_waitcnt vmcnt(18)\n\t"
    "v_mfma_f32_16x16x32_bf16 v[232:235], v[8:11], v[140:143], v[232:235]\n\t"
    "ds_read_b64_tr_b16 v[62:63], v243 offset:12288\n\t"
    "s_add_u32 m0, s61, 114688\n\t"
    "s_add_u32 s63, s75, 1441792\n\t"
    "buffer_load_dwordx4 v255, s[36:39], s63 offen lds\n\t"
    "v_mfma_f32_16x16x32_bf16 v[228:231], v[12:15], v[136:139], v[228:231]\n\t"
    "ds_read_b64_tr_b16 v[8:9], v240 offset:1024\n\t"
    "s_setprio 2\n\t"
    "v_mfma_f32_16x16x32_bf16 v[236:239], v[12:15], v[140:143], v[236:239]\n\t"
    "ds_read_b64_tr_b16 v[10:11], v240 offset:5120\n\t"
    "s_add_u32 s63, s74, 221184\n\t"
    "buffer_load_dwordx4 v[72:75], v252, s[40:43], s63 offen sc1\n\t"
    "s_waitcnt lgkmcnt(10)\n\t"
    "v_mfma_f32_16x16x32_bf16 v[176:179], v[24:27], v[136:139], v[176:179]\n\t"
    "ds_read_b64_tr_b16 v[12:13], v241 offset:1024\n\t"
    "v_mfma_f32_16x16x32_bf16 v[184:187], v[24:27], v[140:143], v[184:187]\n\t"
    "ds_read_b64_tr_b16 v[14:15], v241 offset:5120\n\t"
    "s_add_u32 m0, s61, 115712\n\t"
    "s_add_u32 s63, s75, 1507328\n\t"
    "buffer_load_dwordx4 v255, s[36:39], s63 offen lds\n\t"
    "v_mfma_f32_16x16x32_bf16 v[180:183], v[28:31], v[136:139], v[180:183]\n\t"
    "ds_read_b64_tr_b16 v[24:25], v242 offset:1024\n\t"
    "v_mfma_f32_16x16x32_bf16 v[188:191], v[28:31], v[140:143], v[188:191]\n\t"
    "ds_read_b64_tr_b16 v[26:27], v242 offset:5120\n\t"
    "s_waitcnt vmcnt(18) lgkmcnt(10)\n\t"
    "v_mfma_f32_16x16x32_bf16 v[224:227], v[40:43], v[152:155], v[224:227]\n\t"
    "ds_read_b64_tr_b16 v[28:29], v243 offset:1024\n\t"
    "s_add_u32 s63, s74, 221184\n\t"
    "buffer_load_dwordx4 v[76:79], v252, s[40:43], s63 offen offset:1024 sc1\n\t"
    "s_waitcnt vmcnt(18)\n\t"
    "v_mfma_f32_16x16x32_bf16 v[232:235], v[40:43], v[156:159], v[232:235]\n\t"
    "ds_read_b64_tr_b16 v[30:31], v243 offset:5120\n\t"
    "v_mfma_f32_16x16x32_bf16 v[228:231], v[44:47], v[152:155], v[228:231]\n\t"
    "ds_read_b64_tr_b16 v[40:41], v240 offset:9216\n\t"
    "s_setprio 1\n\t"
    "v_mfma_f32_16x16x32_bf16 v[236:239], v[44:47], v[156:159], v[236:239]\n\t"
    "ds_read_b64_tr_b16 v[42:43], v240 offset:13312\n\t"
    "s_waitcnt lgkmcnt(10)\n\t"
    "v_mfma_f32_16x16x32_bf16 v[176:179], v[56:59], v[152:155], v[176:179]\n\t"
    "ds_read_b64_tr_b16 v[44:45], v241 offset:9216\n\t"
    "v_mfma_f32_16x16x32_bf16 v[184:187], v[56:59], v[156:159], v[184:187]\n\t"
    "ds_read_b64_tr_b16 v[46:47], v241 offset:13312\n\t"
    "v_mfma_f32_16x16x32_bf16 v[180:183], v[60:63], v[152:155], v[180:183]\n\t"
    "ds_read_b64_tr_b16 v[56:57], v242 offset:9216\n\t"
    "s_add_u32 s63, s74, 221184\n\t"
    "buffer_load_dwordx4 v[88:91], v252, s[40:43], s63 offen offset:2048 sc1\n\t"
    "v_mfma_f32_16x16x32_bf16 v[188:191], v[60:63], v[156:159], v[188:191]\n\t"
    "ds_read_b64_tr_b16 v[58:59], v242 offset:13312\n\t"
    "s_waitcnt vmcnt(18) lgkmcnt(10)\n\t"
    "v_mfma_f32_16x16x32_bf16 v[224:227], v[8:11], v[168:171], v[224:227]\n\t"
    "ds_read_b64_tr_b16 v[60:61], v243 offset:9216\n\t"
    "s_setprio 0\n\t"
    "s_waitcnt vmcnt(17)\n\t"
    "v_mfma_f32_16x16x32_bf16 v[232:235], v[8:11], v[172:175], v[232:235]\n\t"
    "ds_read_b64_tr_b16 v[62:63], v243 offset:13312\n\t"
    "v_mfma_f32_16x16x32_bf16 v[228:231], v[12:15], v[168:171], v[228:231]\n\t"
    "v_mfma_f32_16x16x32_bf16 v[236:239], v[12:15], v[172:175], v[236:239]\n\t"
    "s_waitcnt lgkmcnt(8)\n\t"
    "v_mfma_f32_16x16x32_bf16 v[176:179], v[24:27], v[168:171], v[176:179]\n\t"
    "s_add_u32 s63, s74, 221184\n\t"
    "buffer_load_dwordx4 v[92:95], v252, s[40:43], s63 offen offset:3072 sc1\n\t"
    "v_mfma_f32_16x16x32_bf16 v[184:187], v[24:27], v[172:175], v[184:187]\n\t"
    "s_waitcnt lgkmcnt(0)\n\t"
    "s_barrier\n\t"
    "s_setprio 3\n\t"
    "ds_read_b64_tr_b16 v[8:9], v104 offset:0\n\t"
    "ds_read_b64_tr_b16 v[10:11], v104 offset:4096\n\t"
    "ds_read_b64_tr_b16 v[12:13], v105 offset:0\n\t"
    "ds_read_b64_tr_b16 v[14:15], v105 offset:4096\n\t"
    "v_mfma_f32_16x16x32_bf16 v[180:183], v[28:31], v[168:171], v[180:183]\n\t"
    "ds_read_b64_tr_b16 v[24:25], v106 offset:0\n\t"
    "v_mfma_f32_16x16x32_bf16 v[188:191], v[28:31], v[172:175], v[188:191]\n\t"
    "ds_read_b64_tr_b16 v[26:27], v106 offset:4096\n\t"
    "s_waitcnt vmcnt(15)\n\t"
    "v_mfma_f32_16x16x32_bf16 v[224:227], v[40:43], v[128:131], v[224:227]\n\t"
    "ds_read_b64_tr_b16 v[28:29], v107 offset:0\n\t"
    "s_waitcnt vmcnt(13)\n\t"
    "v_mfma_f32_16x16x32_bf16 v[232:235], v[40:43], v[132:135], v[232:235]\n\t"
    "ds_read_b64_tr_b16 v[30:31], v107 offset:4096\n\t"
    "s_add_u32 m0, s61, 65536\n\t"
    "s_add_u32 s63, s75, 2228224\n\t"
    "buffer_load_dwordx4 v255, s[36:39], s63 offen lds\n\t"
    "v_mfma_f32_16x16x32_bf16 v[228:231], v[44:47], v[128:131], v[228:231]\n\t"
    "ds_read_b64_tr_b16 v[40:41], v104 offset:8192\n\t"
    "v_mfma_f32_16x16x32_bf16 v[236:239], v[44:47], v[132:135], v[236:239]\n\t"
    "ds_read_b64_tr_b16 v[42:43], v104 offset:12288\n\t"
    "v_mfma_f32_16x16x32_bf16 v[176:179], v[56:59], v[128:131], v[176:179]\n\t"
    "ds_read_b64_tr_b16 v[44:45], v105 offset:8192\n\t"
    "v_mfma_f32_16x16x32_bf16 v[184:187], v[56:59], v[132:135], v[184:187]\n\t"
    "ds_read_b64_tr_b16 v[46:47], v105 offset:12288\n\t"
    "s_add_u32 m0, s61, 66560\n\t"
    "s_add_u32 s63, s75, 2293760\n\t"
    "buffer_load_dwordx4 v255, s[36:39], s63 offen lds\n\t"
    "v_mfma_f32_16x16x32_bf16 v[180:183], v[60:63], v[128:131], v[180:183]\n\t"
    "ds_read_b64_tr_b16 v[56:57], v106 offset:8192\n\t"
    "v_mfma_f32_16x16x32_bf16 v[188:191], v[60:63], v[132:135], v[188:191]\n\t"
    "ds_read_b64_tr_b16 v[58:59], v106 offset:12288\n\t"
    "s_waitcnt vmcnt(13) lgkmcnt(10)\n\t"
    "v_mfma_f32_16x16x32_bf16 v[224:227], v[8:11], v[144:147], v[224:227]\n\t"
    "ds_read_b64_tr_b16 v[60:61], v107 offset:8192\n\t"
    "s_mov_b64 s[48:49], %[part]\n\t"
    "s_and_b32 s49, s49, 0xffff\n\t"
    "s_mov_b32 s50, 131072\n\t"
    "s_mov_b32 s51, 0x20000\n\t"
    "s_waitcnt vmcnt(12)\n\t"
    "v_mfma_f32_16x16x32_bf16 v[232:235], v[8:11], v[148:151], v[232:235]\n\t"
    "ds_read_b64_tr_b16 v[62:63], v107 offset:12288\n\t"
    "s_add_u32 m0, s61, 81920\n\t"
    "s_add_u32 s63, s75, 2490368\n\t"
    "buffer_load_dwordx4 v255, s[36:39], s63 offen lds\n\t"
    "v_mfma_f32_16x16x32_bf16 v[228:231], v[12:15], v[144:147], v[228:231]\n\t"
    "ds_read_b64_tr_b16 v[8:9], v104 offset:1024\n\t"
    "s_setprio 2\n\t"
    "v_mfma_f32_16x16x32_bf16 v[236:239], v[12:15], v[148:151], v[236:239]\n\t"
    "ds_read_b64_tr_b16 v[10:11], v104 offset:5120\n\t"
    "s_waitcnt lgkmcnt(10)\n\t"
    "v_mfma_f32_16x16x32_bf16 v[176:179], v[24:27], v[144:147], v[176:179]\n\t"
    "ds_read_b64_tr_b16 v[12:13], v105 offset:1024\n\t"
    "v_mov_b32 v124, 0\n\t"
    "v_mfma_f32_16x16x32_bf16 v[184:187], v[24:27], v[148:151], v[184:187]\n\t"
    "ds_read_b64_tr_b16 v[14:15], v105 offset:5120\n\t"
    "s_add_u32 m0, s61, 82944\n\t"
    "s_add_u32 s63, s75, 2555904\n\t"
    "buffer_load_dwordx4 v255, s[36:39], s63 offen lds\n\t"
    "v_mfma_f32_16x16x32_bf16 v[180:183], v[28:31], v[144:147], v[180:183]\n\t"
    "ds_read_b64_tr_b16 v[24:25], v106 offset:1024\n\t"
    "v_mfma_f32_16x16x32_bf16 v[188:191], v[28:31], v[148:151], v[188:191]\n\t"
    "ds_read_b64_tr_b16 v[26:27], v106 offset:5120\n\t"
    "s_waitcnt vmcnt(13) lgkmcnt(10)\n\t"
    "v_mfma_f32_16x16x32_bf16 v[224:227], v[40:43], v[160:163], v[224:227]\n\t"
    "ds_read_b64_tr_b16 v[28:29], v107 offset:1024\n\t"
    "s_waitcnt vmcnt(11)\n\t"
    "v_mfma_f32_16x16x32_bf16 v[232:235], v[40:43], v[164:167], v[232:235]\n\t"
    "ds_read_b64_tr_b16 v[30:31], v107 offset:5120\n\t"
    "v_mfma_f32_16x16x32_bf16 v[228:231], v[44:47], v[160:163], v[228:231]\n\t"
    "ds_read_b64_tr_b16 v[40:41], v104 offset:9216\n\t"
    "s_setprio 1\n\t"
    "v_mfma_f32_16x16x32_bf16 v[236:239], v[44:47], v[164:167], v[236:239]\n\t"
    "ds_read_b64_tr_b16 v[42:43], v104 offset:13312\n\t"
    "v_bfe_u32 v123, v252, 4, 1\n\t"
    "s_waitcnt lgkmcnt(10)\n\t"
    "v_mfma_f32_16x16x32_bf16 v[176:179], v[56:59], v[160:163], v[176:179]\n\t"
    "ds_read_b64_tr_b16 v[44:45], v105 offset:9216\n\t"
    "v_mfma_f32_16x16x32_bf16 v[184:187], v[56:59], v[164:167], v[184:187]\n\t"
    "ds_read_b64_tr_b16 v[46:47], v105 offset:13312\n\t"
    "v_mfma_f32_16x16x32_bf16 v[180:183], v[60:63], v[160:163], v[180:183]\n\t"
    "ds_read_b64_tr_b16 v[56:57], v106 offset:9216\n\t"
    "v_mfma_f32_16x16x32_bf16 v[188:191], v[60:63], v[164:167], v[188:191]\n\t"
    "ds_read_b64_tr_b16 v[58:59], v106 offset:13312\n\t"
    "s_waitcnt vmcnt(8) lgkmcnt(10)\n\t"
    "v_mfma_f32_16x16x32_bf16 v[224:227], v[8:11], v[72:75], v[224:227]\n\t"
    "ds_read_b64_tr_b16 v[60:61], v107 offset:9216\n\t"
    "s_setprio 0\n\t"
    "s_waitcnt vmcnt(6)\n\t"
    "v_mfma_f32_16x16x32_bf16 v[232:235], v[8:11], v[76:79], v[232:235]\n\t"
    "ds_read_b64_tr_b16 v[62:63], v107 offset:13312\n\t"
    "v_lshlrev_b32 v123, 3, v123\n\t"
    "v_mfma_f32_16x16x32_bf16 v[228:231], v[12:15], v[72:75], v[228:231]\n\t"
    "v_mfma_f32_16x16x32_bf16 v[236:239], v[12:15], v[76:79], v[236:239]\n\t"
    "s_waitcnt lgkmcnt(8)\n\t"
    "v_mfma_f32_16x16x32_bf16 v[176:179], v[24:27], v[72:75], v[176:179]\n\t"
    "v_add_u32 v120, 131072, v249\n\t"
    "v_mfma_f32_16x16x32_bf16 v[184:187], v[24:27], v[76:79], v[184:187]\n\t"
    "s_waitcnt lgkmcnt(0)\n\t"
    "s_barrier\n\t"
    "s_setprio 3\n\t"
    "ds_read_b64_tr_b16 v[8:9], v240 offset:32768\n\t"
    "ds_read_b64_tr_b16 v[10:11], v240 offset:36864\n\t"
    "ds_read_b64_tr_b16 v[12:13], v241 offset:32768\n\t"
    "ds_read_b64_tr_b16 v[14:15], v241 offset:36864\n\t"
    "v_mfma_f32_16x16x32_bf16 v[180:183], v[28:31], v[72:75], v[180:183]\n\t"
    "ds_read_b64_tr_b16 v[24:25], v242 offset:32768\n\t"
    "v_mfma_f32_16x16x32_bf16 v[188:191], v[28:31], v[76:79], v[188:191]\n\t"
    "ds_read_b64_tr_b16 v[26:27], v242 offset:36864\n\t"
    "s_waitcnt vmcnt(5)\n\t"
    "v_mfma_f32_16x16x32_bf16 v[224:227], v[40:43], v[88:91], v[224:227]\n\t"
    "ds_read_b64_tr_b16 v[28:29], v243 offset:32768\n\t"
    "s_waitcnt vmcnt(4)\n\t"
    "v_mfma_f32_16x16x32_bf16 v[232:235], v[40:43], v[92:95], v[232:235]\n\t"
    "ds_read_b64_tr_b16 v[30:31], v243 offset:36864\n\t"
    "s_add_u32 m0, s61, 0\n\t"
    "s_add_u32 s63, s75, 3276800\n\t"
    "buffer_load_dwordx4 v255, s[36:39], s63 offen lds\n\t"
    "v_mfma_f32_16x16x32_bf16 v[228:231], v[44:47], v[88:91], v[228:231]\n\t"
    "ds_read_b64_tr_b16 v[40:41], v240 offset:40960\n\t"
    "v_add_u32 v121, 131072, v250\n\t"
    "v_mfma_f32_16x16x32_bf16 v[236:239], v[44:47], v[92:95], v[236:239]\n\t"
    "ds_read_b64_tr_b16 v[42:43], v240 offset:45056\n\t"
    "v_mfma_f32_16x16x32_bf16 v[176:179], v[56:59], v[88:91], v[176:179]\n\t"
    "ds_read_b64_tr_b16 v[44:45], v241 offset:40960\n\t"
    "v_mfma_f32_16x16x32_bf16 v[184:187], v[56:59], v[92:95], v[184:187]\n\t"
    "ds_read_b64_tr_b16 v[46:47], v241 offset:45056\n\t"
    "s_add_u32 m0, s61, 1024\n\t"
    "s_add_u32 s63, s75, 3342336\n\t"
    "buffer_load_dwordx4 v255, s[36:39], s63 offen lds\n\t"
    "v_mfma_f32_16x16x32_bf16 v[180:183], v[60:63], v[88:91], v[180:183]\n\t"
    "ds_read_b64_tr_b16 v[56:57], v242 offset:40960\n\t"
    "v_mfma_f32_16x16x32_bf16 v[188:191], v[60:63], v[92:95], v[188:191]\n\t"
    "ds_read_b64_tr_b16 v[58:59], v242 offset:45056\n\t"
    "s_waitcnt lgkmcnt(10)\n\t"
    "v_mfma_f32_16x16x32_bf16 v[192:195], v[8:11], v[64:67], v[192:195]\n\t"
    "ds_read_b64_tr_b16 v[60:61], v243 offset:40960\n\t"
    "v_mfma_f32_16x16x32_bf16 v[200:203], v[8:11], v[68:71], v[200:203]\n\t"
    "ds_read_b64_tr_b16 v[62:63], v243 offset:45056\n\t"
    "s_add_u32 m0, s61, 16384\n\t"
    "s_add_u32 s63, s75, 3538944\n\t"
    "buffer_load_dwordx4 v255, s[36:39], s63 offen lds\n\t"
    "v_mfma_f32_16x16x32_bf16 v[196:199], v[12:15], v[64:67], v[196:199]\n\t"
    "ds_read_b64_tr_b16 v[8:9], v240 offset:33792\n\t"
    "s_setprio 2\n\t"
    "v_mfma_f32_16x16x32_bf16 v[204:207], v[12:15], v[68:71], v[204:207]\n\t"
    "ds_read_b64_tr_b16 v[10:11], v240 offset:37888\n\t"
    "s_waitcnt lgkmcnt(10)\n\t"
    "v_mfma_f32_16x16x32_bf16 v[208:211], v[24:27], v[64:67], v[208:211]\n\t"
    "ds_read_b64_tr_b16 v[12:13], v241 offset:33792\n\t"
    "ds_write_b128 v120, v[176:179] offset:0\n\t"
    "v_mfma_f32_16x16x32_bf16 v[216:219], v[24:27], v[68:71], v[216:219]\n\t"
    "ds_read_b64_tr_b16 v[14:15], v241 offset:37888\n\t"
    "s_add_u32 m0, s61, 17408\n\t"
    "s_add_u32 s63, s75, 3604480\n\t"
    "buffer_load_dwordx4 v255, s[36:39], s63 offen lds\n\t"
    "v_mfma_f32_16x16x32_bf16 v[212:215], v[28:31], v[64:67], v[212:215]\n\t"
    "ds_read_b64_tr_b16 v[24:25], v242 offset:33792\n\t"
    "ds_write_b128 v120, v[180:183] offset:1024\n\t"
    "v_mfma_f32_16x16x32_bf16 v[220:223], v[28:31], v[68:71], v[220:223]\n\t"
    "ds_read_b64_tr_b16 v[26:27], v242 offset:37888\n\t"
    "s_waitcnt lgkmcnt(12)\n\t"
    "v_mfma_f32_16x16x32_bf16 v[192:195], v[40:43], v[80:83], v[192:195]\n\t"
    "ds_read_b64_tr_b16 v[28:29], v243 offset:33792\n\t"
    "ds_write_b128 v120, v[184:187] offset:2048\n\t"
    "v_mfma_f32_16x16x32_bf16 v[200:203], v[40:43], v[84:87], v[200:203]\n\t"
    "ds_read_b64_tr_b16 v[30:31], v243 offset:37888\n\t"
    "v_mfma_f32_16x16x32_bf16 v[196:199], v[44:47], v[80:83], v[196:199]\n\t"
    "ds_read_b64_tr_b16 v[40:41], v240 offset:41984\n\t"
    "s_setprio 1\n\t"
    "v_mfma_f32_16x16x32_bf16 v[204:207], v[44:47], v[84:87], v[204:207]\n\t"
    "ds_read_b64_tr_b16 v[42:43], v240 offset:46080\n\t"
    "ds_write_b128 v120, v[188:191] offset:3072\n\t"
    "s_waitcnt lgkmcnt(14)\n\t"
    "v_mfma_f32_16x16x32_bf16 v[208:211], v[56:59], v[80:83], v[208:211]\n\t"
    "ds_read_b64_tr_b16 v[44:45], v241 offset:41984\n\t"
    "v_mfma_f32_16x16x32_bf16 v[216:219], v[56:59], v[84:87], v[216:219]\n\t"
    "ds_read_b64_tr_b16 v[46:47], v241 offset:46080\n\t"
    "v_mfma_f32_16x16x32_bf16 v[212:215], v[60:63], v[80:83], v[212:215]\n\t"
    "ds_read_b64_tr_b16 v[56:57], v242 offset:41984\n\t"
    "v_mfma_f32_16x16x32_bf16 v[220:223], v[60:63], v[84:87], v[220:223]\n\t"
    "ds_read_b64_tr_b16 v[58:59], v242 offset:46080\n\t"
    "s_waitcnt lgkmcnt(13)\n\t"
    "v_mfma_f32_16x16x32_bf16 v[192:195], v[8:11], v[96:99], v[192:195]\n\t"
    "ds_read_b64_tr_b16 v[60:61], v243 offset:41984\n\t"
    "s_setprio 0\n\t"
    "v_mfma_f32_16x16x32_bf16 v[200:203], v[8:11], v[100:103], v[200:203]\n\t"
    "ds_read_b64_tr_b16 v[62:63], v243 offset:46080\n\t"
    "v_mfma_f32_16x16x32_bf16 v[196:199], v[12:15], v[96:99], v[196:199]\n\t"
    "v_mfma_f32_16x16x32_bf16 v[204:207], v[12:15], v[100:103], v[204:207]\n\t"
    "s_waitcnt lgkmcnt(9)\n\t"
    "v_mfma_f32_16x16x32_bf16 v[208:211], v[24:27], v[96:99], v[208:211]\n\t"
    "v_mfma_f32_16x16x32_bf16 v[216:219], v[24:27], v[100:103], v[216:219]\n\t"
    "s_waitcnt lgkmcnt(0)\n\t"
    "s_barrier\n\t"
    "s_setprio 3\n\t"
    "ds_read_b64_tr_b16 v[8:9], v244 offset:32768\n\t"
    "ds_read_b64_tr_b16 v[10:11], v244 offset:36864\n\t"
    "ds_read_b64_tr_b16 v[12:13], v245 offset:32768\n\t"
    "ds_read_b64_tr_b16 v[14:15], v245 offset:36864\n\t"
    "v_mfma_f32_16x16x32_bf16 v[212:215], v[28:31], v[96:99], v[212:215]\n\t"
    "ds_read_b64_tr_b16 v[24:25], v246 offset:32768\n\t"
    "v_mfma_f32_16x16x32_bf16 v[220:223], v[28:31], v[100:103], v[220:223]\n\t"
    "ds_read_b64_tr_b16 v[26:27], v246 offset:36864\n\t"
    "v_mfma_f32_16x16x32_bf16 v[192:195], v[40:43], v[112:115], v[192:195]\n\t"
    "ds_read_b64_tr_b16 v[28:29], v247 offset:32768\n\t"
    "v_mfma_f32_16x16x32_bf16 v[200:203], v[40:43], v[116:119], v[200:203]\n\t"
    "ds_read_b64_tr_b16 v[30:31], v247 offset:36864\n\t"
    "v_mfma_f32_16x16x32_bf16 v[196:199], v[44:47], v[112:115], v[196:199]\n\t"
    "ds_read_b64_tr_b16 v[40:41], v244 offset:40960\n\t"
    "v_mfma_f32_16x16x32_bf16 v[204:207], v[44:47], v[116:119], v[204:207]\n\t"
    "ds_read_b64_tr_b16 v[42:43], v244 offset:45056\n\t"
    "v_mfma_f32_16x16x32_bf16 v[208:211], v[56:59], v[112:115], v[208:211]\n\t"
    "ds_read_b64_tr_b16 v[44:45], v245 offset:40960\n\t"
    "ds_read_b128 v[176:179], v121 offset:0\n\t"
    "v_mfma_f32_16x16x32_bf16 v[216:219], v[56:59], v[116:119], v[216:219]\n\t"
    "ds_read_b64_tr_b16 v[46:47], v245 offset:45056\n\t"
    "ds_read_b128 v[180:183], v121 offset:1024\n\t"
    "v_mfma_f32_16x16x32_bf16 v[212:215], v[60:63], v[112:115], v[212:215]\n\t"
    "ds_read_b64_tr_b16 v[56:57], v246 offset:40960\n\t"
    "ds_read_b128 v[184:187], v121 offset:2048\n\t"
    "v_mfma_f32_16x16x32_bf16 v[220:223], v[60:63], v[116:119], v[220:223]\n\t"
    "ds_read_b64_tr_b16 v[58:59], v246 offset:45056\n\t"
    "ds_read_b128 v[188:191], v121 offset:3072\n\t"
    "s_waitcnt lgkmcnt(14)\n\t"
    "v_mfma_f32_16x16x32_bf16 v[192:195], v[8:11], v[0:3], v[192:195]\n\t"
    "ds_read_b64_tr_b16 v[60:61], v247 offset:40960\n\t"
    "v_mfma_f32_16x16x32_bf16 v[200:203], v[8:11], v[4:7], v[200:203]\n\t"
    "ds_read_b64_tr_b16 v[62:63], v247 offset:45056\n\t"
    "v_mfma_f32_16x16x32_bf16 v[196:199], v[12:15], v[0:3], v[196:199]\n\t"
    "ds_read_b64_tr_b16 v[8:9], v244 offset:33792\n\t"
    "s_setprio 2\n\t"
    "s_waitcnt lgkmcnt(9)\n\t"
    "v_add_f32 v224, v224, v176\n\t"
    "v_add_f32 v225, v225, v177\n\t"
    "v_mfma_f32_16x16x32_bf16 v[204:207], v[12:15], v[4:7], v[204:207]\n\t"
    "ds_read_b64_tr_b16 v[10:11], v244 offset:37888\n\t"
    "v_add_f32 v226, v226, v178\n\t"
    "v_add_f32 v227, v227, v179\n\t"
    "v_mfma_f32_16x16x32_bf16 v[208:211], v[24:27], v[0:3], v[208:211]\n\t"
    "ds_read_b64_tr_b16 v[12:13], v245 offset:33792\n\t"
    "s_waitcnt lgkmcnt(9)\n\t"
    "v_add_f32 v228, v228, v180\n\t"
    "v_add_f32 v229, v229, v181\n\t"
    "v_mfma_f32_16x16x32_bf16 v[216:219], v[24:27], v[4:7], v[216:219]\n\t"
    "ds_read_b64_tr_b16 v[14:15], v245 offset:37888\n\t"
    "v_add_f32 v230, v230, v182\n\t"
    "v_add_f32 v231, v231, v183\n\t"
    "v_mfma_f32_16x16x32_bf16 v[212:215], v[28:31], v[0:3], v[212:215]\n\t"
    "ds_read_b64_tr_b16 v[24:25], v246 offset:33792\n\t"
    "s_waitcnt lgkmcnt(9)\n\t"
    "v_add_f32 v232, v232, v184\n\t"
    "v_add_f32 v233, v233, v185\n\t"
    "v_mfma_f32_16x16x32_bf16 v[220:223], v[28:31], v[4:7], v[220:223]\n\t"
    "ds_read_b64_tr_b16 v[26:27], v246 offset:37888\n\t"
    "v_add_f32 v234, v234, v186\n\t"
    "v_add_f32 v235, v235, v187\n\t"
    "v_mfma_f32_16x16x32_bf16 v[192:195], v[40:43], v[16:19], v[192:195]\n\t"
    "ds_read_b64_tr_b16 v[28:29], v247 offset:33792\n\t"
    "s_waitcnt lgkmcnt(9)\n\t"
    "v_add_f32 v236, v236, v188\n\t"
    "v_add_f32 v237, v237, v189\n\t"
    "v_mfma_f32_16x16x32_bf16 v[200:203], v[40:43], v[20:23], v[200:203]\n\t"
    "ds_read_b64_tr_b16 v[30:31], v247 offset:37888\n\t"
    "v_add_f32 v238, v238, v190\n\t"
    "v_add_f32 v239, v239, v191\n\t"
    "v_mfma_f32_16x16x32_bf16 v[196:199], v[44:47], v[16:19], v[196:199]\n\t"
    "ds_read_b64_tr_b16 v[40:41], v244 offset:41984\n\t"
    "s_setprio 1\n\t"
    "v_mfma_f32_16x16x32_bf16 v[204:207], v[44:47], v[20:23], v[204:207]\n\t"
    "ds_read_b64_tr_b16 v[42:43], v244 offset:46080\n\t"
    "s_lshl_b32 s63, s77, 9\n\t"
    "s_xor_b32 s63, s63, 65536\n\t"
    "buffer_store_dwordx4 v[224:227], v252, s[48:51], s63 offen sc1\n\t"
    "s_waitcnt lgkmcnt(10)\n\t"
    "v_mfma_f32_16x16x32_bf16 v[208:211], v[56:59], v[16:19], v[208:211]\n\t"
    "ds_read_b64_tr_b16 v[44:45], v245 offset:41984\n\t"
    "s_lshl_b32 s63, s77, 9\n\t"
    "s_xor_b32 s63, s63, 65536\n\t"
    "buffer_store_dwordx4 v[228:231], v252, s[48:51], s63 offen offset:1024 sc1\n\t"
    "v_mfma_f32_16x16x32_bf16 v[216:219], v[56:59], v[20:23], v[216:219]\n\t"
    "ds_read_b64_tr_b16 v[46:47], v245 offset:46080\n\t"
    "v_mfma_f32_16x16x32_bf16 v[212:215], v[60:63], v[16:19], v[212:215]\n\t"
    "ds_read_b64_tr_b16 v[56:57], v246 offset:41984\n\t"
    "s_lshl_b32 s63, s77, 9\n\t"
    "s_xor_b32 s63, s63, 65536\n\t"
    "buffer_store_dwordx4 v[232:235], v252, s[48:51], s63 offen offset:2048 sc1\n\t"
    "v_mfma_f32_16x16x32_bf16 v[220:223], v[60:63], v[20:23], v[220:223]\n\t"
    "ds_read_b64_tr_b16 v[58:59], v246 offset:46080\n\t"
    "s_waitcnt lgkmcnt(10)\n\t"
    "v_mfma_f32_16x16x32_bf16 v[192:195], v[8:11], v[32:35], v[192:195]\n\t"
    "ds_read_b64_tr_b16 v[60:61], v247 offset:41984\n\t"
    "s_setprio 0\n\t"
    "v_mfma_f32_16x16x32_bf16 v[200:203], v[8:11], v[36:39], v[200:203]\n\t"
    "ds_read_b64_tr_b16 v[62:63], v247 offset:46080\n\t"
    "s_lshl_b32 s63, s77, 9\n\t"
    "s_xor_b32 s63, s63, 65536\n\t"
    "buffer_store_dwordx4 v[236:239], v252, s[48:51], s63 offen offset:3072 sc1\n\t"
    "v_mfma_f32_16x16x32_bf16 v[196:199], v[12:15], v[32:35], v[196:199]\n\t"
    "v_mfma_f32_16x16x32_bf16 v[204:207], v[12:15], v[36:39], v[204:207]\n\t"
    "s_waitcnt lgkmcnt(8)\n\t"
    "v_mfma_f32_16x16x32_bf16 v[208:211], v[24:27], v[32:35], v[208:211]\n\t"
    "v_mfma_f32_16x16x32_bf16 v[216:219], v[24:27], v[36:39], v[216:219]\n\t"
    "s_waitcnt vmcnt(8) lgkmcnt(0)\n\t"
    "s_barrier\n\t"
    "s_setprio 3\n\t"
    "ds_read_b64_tr_b16 v[8:9], v244 offset:0\n\t"
    "ds_read_b64_tr_b16 v[10:11], v244 offset:4096\n\t"
    "ds_read_b64_tr_b16 v[12:13], v245 offset:0\n\t"
    "ds_read_b64_tr_b16 v[14:15], v245 offset:4096\n\t"
    "v_mfma_f32_16x16x32_bf16 v[212:215], v[28:31], v[32:35], v[212:215]\n\t"
    "ds_read_b64_tr_b16 v[24:25], v246 offset:0\n\t"
    "v_mfma_f32_16x16x32_bf16 v[220:223], v[28:31], v[36:39], v[220:223]\n\t"
    "ds_read_b64_tr_b16 v[26:27], v246 offset:4096\n\t"
    "v_mfma_f32_16x16x32_bf16 v[192:195], v[40:43], v[48:51], v[192:195]\n\t"
    "ds_read_b64_tr_b16 v[28:29], v247 offset:0\n\t"
    "v_mfma_f32_16x16x32_bf16 v[200:203], v[40:43], v[52:55], v[200:203]\n\t"
    "ds_read_b64_tr_b16 v[30:31], v247 offset:4096\n\t"
    "v_mfma_f32_16x16x32_bf16 v[196:199], v[44:47], v[48:51], v[196:199]\n\t"
    "ds_read_b64_tr_b16 v[40:41], v244 offset:8192\n\t"
    "v_mfma_f32_16x16x32_bf16 v[204:207], v[44:47], v[52:55], v[204:207]\n\t"
    "ds_read_b64_tr_b16 v[42:43], v244 offset:12288\n\t"
    "v_mfma_f32_16x16x32_bf16 v[208:211], v[56:59], v[48:51], v[208:211]\n\t"
    "ds_read_b64_tr_b16 v[44:45], v245 offset:8192\n\t"
    "v_mfma_f32_16x16x32_bf16 v[216:219], v[56:59], v[52:55], v[216:219]\n\t"
    "ds_read_b64_tr_b16 v[46:47], v245 offset:12288\n\t"
    "v_mfma_f32_16x16x32_bf16 v[212:215], v[60:63], v[48:51], v[212:215]\n\t"
    "ds_read_b64_tr_b16 v[56:57], v246 offset:8192\n\t"
    "v_mfma_f32_16x16x32_bf16 v[220:223], v[60:63], v[52:55], v[220:223]\n\t"
    "ds_read_b64_tr_b16 v[58:59], v246 offset:12288\n\t"
    "s_waitcnt lgkmcnt(10)\n\t"
    "v_mfma_f32_16x16x32_bf16 v[192:195], v[8:11], v[136:139], v[192:195]\n\t"
    "ds_read_b64_tr_b16 v[60:61], v247 offset:8192\n\t"
    "v_mfma_f32_16x16x32_bf16 v[200:203], v[8:11], v[140:143], v[200:203]\n\t"
    "ds_read_b64_tr_b16 v[62:63], v247 offset:12288\n\t"
    "v_mfma_f32_16x16x32_bf16 v[196:199], v[12:15], v[136:139], v[196:199]\n\t"
    "ds_read_b64_tr_b16 v[8:9], v244 offset:1024\n\t"
    "s_setprio 2\n\t"
    "v_mfma_f32_16x16x32_bf16 v[204:207], v[12:15], v[140:143], v[204:207]\n\t"
    "ds_read_b64_tr_b16 v[10:11], v244 offset:5120\n\t"
    "s_waitcnt lgkmcnt(10)\n\t"
    "v_mfma_f32_16x16x32_bf16 v[208:211], v[24:27], v[136:139], v[208:211]\n\t"
    "ds_read_b64_tr_b16 v[12:13], v245 offset:1024\n\t"
    "v_mfma_f32_16x16x32_bf16 v[216:219], v[24:27], v[140:143], v[216:219]\n\t"
    "ds_read_b64_tr_b16 v[14:15], v245 offset:5120\n\t"
    "v_mfma_f32_16x16x32_bf16 v[212:215], v[28:31], v[136:139], v[212:215]\n\t"
    "ds_read_b64_tr_b16 v[24:25], v246 offset:1024\n\t"
    "v_mfma_f32_16x16x32_bf16 v[220:223], v[28:31], v[140:143], v[220:223]\n\t"
    "ds_read_b64_tr_b16 v[26:27], v246 offset:5120\n\t"
    "s_waitcnt lgkmcnt(10)\n\t"
    "v_mfma_f32_16x16x32_bf16 v[192:195], v[40:43], v[152:155], v[192:195]\n\t"
    "ds_read_b64_tr_b16 v[28:29], v247 offset:1024\n\t"
    "s_waitcnt vmcnt(0)\n\t"
    "s_xor_b32 s63, %[wgi], 128\n\t"
    "buffer_store_dwordx2 v[126:127], v123, s[56:59], s63 offen sc1\n\t"
    "v_mfma_f32_16x16x32_bf16 v[200:203], v[40:43], v[156:159], v[200:203]\n\t"
    "ds_read_b64_tr_b16 v[30:31], v247 offset:5120\n\t"
    "v_mfma_f32_16x16x32_bf16 v[196:199], v[44:47], v[152:155], v[196:199]\n\t"
    "ds_read_b64_tr_b16 v[40:41], v244 offset:9216\n\t"
    "s_setprio 1\n\t"
    "v_mfma_f32_16x16x32_bf16 v[204:207], v[44:47], v[156:159], v[204:207]\n\t"
    "ds_read_b64_tr_b16 v[42:43], v244 offset:13312\n\t"
    "s_waitcnt lgkmcnt(10)\n\t"
    "v_mfma_f32_16x16x32_bf16 v[208:211], v[56:59], v[152:155], v[208:211]\n\t"
    "ds_read_b64_tr_b16 v[44:45], v245 offset:9216\n\t"
    "v_mfma_f32_16x16x32_bf16 v[216:219], v[56:59], v[156:159], v[216:219]\n\t"
    "ds_read_b64_tr_b16 v[46:47], v245 offset:13312\n\t"
    "v_mfma_f32_16x16x32_bf16 v[212:215], v[60:63], v[152:155], v[212:215]\n\t"
    "ds_read_b64_tr_b16 v[56:57], v246 offset:9216\n\t"
    "v_mfma_f32_16x16x32_bf16 v[220:223], v[60:63], v[156:159], v[220:223]\n\t"
    "ds_read_b64_tr_b16 v[58:59], v246 offset:13312\n\t"
    "s_waitcnt lgkmcnt(10)\n\t"
    "v_mfma_f32_16x16x32_bf16 v[192:195], v[8:11], v[168:171], v[192:195]\n\t"
    "ds_read_b64_tr_b16 v[60:61], v247 offset:9216\n\t"
    "s_setprio 0\n\t"
    "v_mfma_f32_16x16x32_bf16 v[200:203], v[8:11], v[172:175], v[200:203]\n\t"
    "ds_read_b64_tr_b16 v[62:63], v247 offset:13312\n\t"
    "v_mfma_f32_16x16x32_bf16 v[196:199], v[12:15], v[168:171], v[196:199]\n\t"
    "v_mfma_f32_16x16x32_bf16 v[204:207], v[12:15], v[172:175], v[204:207]\n\t"
    "s_waitcnt lgkmcnt(8)\n\t"
    "v_mfma_f32_16x16x32_bf16 v[208:211], v[24:27], v[168:171], v[208:211]\n\t"
    "v_mfma_f32_16x16x32_bf16 v[216:219], v[24:27], v[172:175], v[216:219]\n\t"
    "s_waitcnt lgkmcnt(0)\n\t"
    "s_barrier\n\t"
    "s_setprio 3\n\t"
    "ds_read_b64_tr_b16 v[8:9], v240 offset:0\n\t"
    "ds_read_b64_tr_b16 v[10:11], v240 offset:4096\n\t"
    "ds_read_b64_tr_b16 v[12:13], v241 offset:0\n\t"
    "ds_read_b64_tr_b16 v[14:15], v241 offset:4096\n\t"
    "v_mfma_f32_16x16x32_bf16 v[212:215], v[28:31], v[168:171], v[212:215]\n\t"
    "ds_read_b64_tr_b16 v[24:25], v242 offset:0\n\t"
    "v_mfma_f32_16x16x32_bf16 v[220:223], v[28:31], v[172:175], v[220:223]\n\t"
    "ds_read_b64_tr_b16 v[26:27], v242 offset:4096\n\t"
    "v_mfma_f32_16x16x32_bf16 v[192:195], v[40:43], v[128:131], v[192:195]\n\t"
    "ds_read_b64_tr_b16 v[28:29], v243 offset:0\n\t"
    "v_mfma_f32_16x16x32_bf16 v[200:203], v[40:43], v[132:135], v[200:203]\n\t"
    "ds_read_b64_tr_b16 v[30:31], v243 offset:4096\n\t"
    "v_mfma_f32_16x16x32_bf16 v[196:199], v[44:47], v[128:131], v[196:199]\n\t"
    "ds_read_b64_tr_b16 v[40:41], v240 offset:8192\n\t"
    "v_mfma_f32_16x16x32_bf16 v[204:207], v[44:47], v[132:135], v[204:207]\n\t"
    "ds_read_b64_tr_b16 v[42:43], v240 offset:12288\n\t"
    "v_mfma_f32_16x16x32_bf16 v[208:211], v[56:59], v[128:131], v[208:211]\n\t"
    "ds_read_b64_tr_b16 v[44:45], v241 offset:8192\n\t"
    "v_mfma_f32_16x16x32_bf16 v[216:219], v[56:59], v[132:135], v[216:219]\n\t"
    "ds_read_b64_tr_b16 v[46:47], v241 offset:12288\n\t"
    "v_mfma_f32_16x16x32_bf16 v[212:215], v[60:63], v[128:131], v[212:215]\n\t"
    "ds_read_b64_tr_b16 v[56:57], v242 offset:8192\n\t"
    "v_mfma_f32_16x16x32_bf16 v[220:223], v[60:63], v[132:135], v[220:223]\n\t"
    "ds_read_b64_tr_b16 v[58:59], v242 offset:12288\n\t"
    "s_waitcnt lgkmcnt(10)\n\t"
    "v_mfma_f32_16x16x32_bf16 v[192:195], v[8:11], v[144:147], v[192:195]\n\t"
    "ds_read_b64_tr_b16 v[60:61], v243 offset:8192\n\t"
    "v_mfma_f32_16x16x32_bf16 v[200:203], v[8:11], v[148:151], v[200:203]\n\t"
    "ds_read_b64_tr_b16 v[62:63], v243 offset:12288\n\t"
    "v_mfma_f32_16x16x32_bf16 v[196:199], v[12:15], v[144:147], v[196:199]\n\t"
    "ds_read_b64_tr_b16 v[8:9], v240 offset:1024\n\t"
    "s_setprio 2\n\t"
    "v_mfma_f32_16x16x32_bf16 v[204:207], v[12:15], v[148:151], v[204:207]\n\t"
    "ds_read_b64_tr_b16 v[10:11], v240 offset:5120\n\t"
    "s_waitcnt lgkmcnt(10)\n\t"
    "v_mfma_f32_16x16x32_bf16 v[208:211], v[24:27], v[144:147], v[208:211]\n\t"
    "ds_read_b64_tr_b16 v[12:13], v241 offset:1024\n\t"
    "s_mov_b32 s63, %[wgi]\n\t"
    "buffer_load_dwordx4 v[108:111], v124, s[56:59], s63 offen sc1\n\t"
    "v_mfma_f32_16x16x32_bf16 v[216:219], v[24:27], v[148:151], v[216:219]\n\t"
    "ds_read_b64_tr_b16 v[14:15], v241 offset:5120\n\t"
    "v_mfma_f32_16x16x32_bf16 v[212:215], v[28:31], v[144:147], v[212:215]\n\t"
    "ds_read_b64_tr_b16 v[24:25], v242 offset:1024\n\t"
    "v_mfma_f32_16x16x32_bf16 v[220:223], v[28:31], v[148:151], v[220:223]\n\t"
    "ds_read_b64_tr_b16 v[26:27], v242 offset:5120\n\t"
    "s_waitcnt lgkmcnt(10)\n\t"
    "v_mfma_f32_16x16x32_bf16 v[192:195], v[40:43], v[160:163], v[192:195]\n\t"
    "ds_read_b64_tr_b16 v[28:29], v243 offset:1024\n\t"
    "v_mfma_f32_16x16x32_bf16 v[200:203], v[40:43], v[164:167], v[200:203]\n\t"
    "ds_read_b64_tr_b16 v[30:31], v243 offset:5120\n\t"
    "v_mfma_f32_16x16x32_bf16 v[196:199], v[44:47], v[160:163], v[196:199]\n\t"
    "ds_read_b64_tr_b16 v[40:41], v240 offset:9216\n\t"
    "s_setprio 1\n\t"
    "v_mfma_f32_16x16x32_bf16 v[204:207], v[44:47], v[164:167], v[204:207]\n\t"
    "ds_read_b64_tr_b16 v[42:43], v240 offset:13312\n\t"
    "s_waitcnt lgkmcnt(10)\n\t"
    "v_mfma_f32_16x16x32_bf16 v[208:211], v[56:59], v[160:163], v[208:211]\n\t"
    "ds_read_b64_tr_b16 v[44:45], v241 offset:9216\n\t"
    "v_mfma_f32_16x16x32_bf16 v[216:219], v[56:59], v[164:167], v[216:219]\n\t"
    "ds_read_b64_tr_b16 v[46:47], v241 offset:13312\n\t"
    "v_mfma_f32_16x16x32_bf16 v[212:215], v[60:63], v[160:163], v[212:215]\n\t"
    "ds_read_b64_tr_b16 v[56:57], v242 offset:9216\n\t"
    "v_mfma_f32_16x16x32_bf16 v[220:223], v[60:63], v[164:167], v[220:223]\n\t"
    "ds_read_b64_tr_b16 v[58:59], v242 offset:13312\n\t"
    "s_waitcnt lgkmcnt(10)\n\t"
    "v_mfma_f32_16x16x32_bf16 v[192:195], v[8:11], v[72:75], v[192:195]\n\t"
    "ds_read_b64_tr_b16 v[60:61], v243 offset:9216\n\t"
    "s_setprio 0\n\t"
    "v_mfma_f32_16x16x32_bf16 v[200:203], v[8:11], v[76:79], v[200:203]\n\t"
    "ds_read_b64_tr_b16 v[62:63], v243 offset:13312\n\t"
    "v_mfma_f32_16x16x32_bf16 v[196:199], v[12:15], v[72:75], v[196:199]\n\t"
    "v_mfma_f32_16x16x32_bf16 v[204:207], v[12:15], v[76:79], v[204:207]\n\t"
    "s_waitcnt lgkmcnt(8)\n\t"
    "v_mfma_f32_16x16x32_bf16 v[208:211], v[24:27], v[72:75], v[208:211]\n\t"
    "v_mfma_f32_16x16x32_bf16 v[216:219], v[24:27], v[76:79], v[216:219]\n\t"
    "v_mfma_f32_16x16x32_bf16 v[212:215], v[28:31], v[72:75], v[212:215]\n\t"
    "v_mfma_f32_16x16x32_bf16 v[220:223], v[28:31], v[76:79], v[220:223]\n\t"
    "s_waitcnt lgkmcnt(4)\n\t"
    "v_mfma_f32_16x16x32_bf16 v[192:195], v[40:43], v[88:91], v[192:195]\n\t"
    "v_mfma_f32_16x16x32_bf16 v[200:203], v[40:43], v[92:95], v[200:203]\n\t"
    "v_mfma_f32_16x16x32_bf16 v[196:199], v[44:47], v[88:91], v[196:199]\n\t"
    "v_mfma_f32_16x16x32_bf16 v[204:207], v[44:47], v[92:95], v[204:207]\n\t"
    "s_waitcnt lgkmcnt(0)\n\t"
    "v_mfma_f32_16x16x32_bf16 v[208:211], v[56:59], v[88:91], v[208:211]\n\t"
    "s_waitcnt vmcnt(0)\n\t"
    "v_xor_b32 v108, v108, v126\n\t"
    "v_xor_b32 v109, v109, v127\n\t"
    "v_xor_b32 v110, v110, v126\n\t"
    "v_xor_b32 v111, v111, v127\n\t"
    "v_or3_b32 v108, v108, v109, v110\n\t"
    "v_or_b32 v108, v108, v111\n\t"
    "v_cmp_ne_u32 vcc, 0, v108\n\t"
    "s_nop 4\n\t"
    "s_cbranch_vccz PGO2_%=\n\t"
    "s_mov_b32 s70, 0x200000\n\t"
    "PSP2_%=:\n\t"
    "s_sleep 4\n\t"
    "s_mov_b32 s63, %[wgi]\n\t"
    "buffer_load_dwordx4 v[108:111], v124, s[56:59], s63 offen sc1\n\t"
    "s_waitcnt vmcnt(0)\n\t"
    "v_xor_b32 v108, v108, v126\n\t"
    "v_xor_b32 v109, v109, v127\n\t"
    "v_xor_b32 v110, v110, v126\n\t"
    "v_xor_b32 v111, v111, v127\n\t"
    "v_or3_b32 v108, v108, v109, v110\n\t"
    "v_or_b32 v108, v108, v111\n\t"
    "v_cmp_ne_u32 vcc, 0, v108\n\t"
    "s_sub_u32 s70, s70, 1\n\t"
    "s_cbranch_scc1 PGO2_%=\n\t"
    "s_nop 4\n\t"
    "s_cbranch_vccnz PSP2_%=\n\t"
    "PGO2_%=:\n\t"
    "s_nop 1\n\t"
    "s_lshl_b32 s63, s77, 9\n\t"
    "buffer_load_dwordx4 v[224:227], v252, s[48:51], s63 offen sc1\n\t"
    "s_lshl_b32 s63, s77, 9\n\t"
    "buffer_load_dwordx4 v[228:231], v252, s[48:51], s63 offen offset:1024 sc1\n\t"
    "v_mfma_f32_16x16x32_bf16 v[216:219], v[56:59], v[92:95], v[216:219]\n\t"
    "s_lshl_b32 s63, s77, 9\n\t"
    "buffer_load_dwordx4 v[232:235], v252, s[48:51], s63 offen offset:2048 sc1\n\t"
    "s_lshl_b32 s63, s77, 9\n\t"
    "buffer_load_dwordx4 v[236:239], v252, s[48:51], s63 offen offset:3072 sc1\n\t"
    "v_mfma_f32_16x16x32_bf16 v[212:215], v[60:63], v[88:91], v[212:215]\n\t"
    "v_mfma_f32_16x16x32_bf16 v[220:223], v[60:63], v[92:95], v[220:223]\n\t"
    "s_setprio 0\n\t"
    "ds_write_b128 v249, v[208:211] offset:32768\n\t"
    "s_nop 4\n\t"
    "ds_write_b128 v249, v[212:215] offset:33792\n\t"
    "ds_write_b128 v249, v[216:219] offset:34816\n\t"
    "ds_write_b128 v249, v[220:223] offset:35840\n\t"
    "s_waitcnt lgkmcnt(0)\n\t"
    "s_barrier\n\t"
    "ds_read_b128 v[208:211], v250 offset:32768\n\t"
    "ds_read_b128 v[212:215], v250 offset:33792\n\t"
    "ds_read_b128 v[216:219], v250 offset:34816\n\t"
    "ds_read_b128 v[220:223], v250 offset:35840\n\t"
    "s_waitcnt lgkmcnt(3)\n\t"
    "v_add_f32 v192, v192, v208\n\t"
    "v_add_f32 v193, v193, v209\n\t"
    "v_add_f32 v194, v194, v210\n\t"
    "v_add_f32 v195, v195, v211\n\t"
    "s_waitcnt lgkmcnt(2)\n\t"
    "v_add_f32 v196, v196, v212\n\t"
    "v_add_f32 v197, v197, v213\n\t"
    "v_add_f32 v198, v198, v214\n\t"
    "v_add_f32 v199, v199, v215\n\t"
    "s_waitcnt lgkmcnt(1)\n\t"
    "v_add_f32 v200, v200, v216\n\t"
    "v_add_f32 v201, v201, v217\n\t"
    "v_add_f32 v202, v202, v218\n\t"
    "v_add_f32 v203, v203, v219\n\t"
    "s_waitcnt lgkmcnt(0)\n\t"
    "v_add_f32 v204, v204, v220\n\t"
    "v_add_f32 v205, v205, v221\n\t"
    "v_add_f32 v206, v206, v222\n\t"
    "v_add_f32 v207, v207, v223\n\t"
    "s_waitcnt vmcnt(3)\n\t"
    "v_add_f32 v192, v192, v224\n\t"
    "v_add_f32 v193, v193, v225\n\t"
    "v_add_f32 v194, v194, v226\n\t"
    "v_add_f32 v195, v195, v227\n\t"
    "s_waitcnt vmcnt(2)\n\t"
    "v_add_f32 v196, v196, v228\n\t"
    "v_add_f32 v197, v197, v229\n\t"
    "v_add_f32 v198, v198, v230\n\t"
    "v_add_f32 v199, v199, v231\n\t"
    "s_waitcnt vmcnt(1)\n\t"
    "v_add_f32 v200, v200, v232\n\t"
    "v_add_f32 v201, v201, v233\n\t"
    "v_add_f32 v202, v202, v234\n\t"
    "v_add_f32 v203, v203, v235\n\t"
    "s_waitcnt vmcnt(0)\n\t"
    "v_add_f32 v204, v204, v236\n\t"
    "v_add_f32 v205, v205, v237\n\t"
    "v_add_f32 v206, v206, v238\n\t"
    "v_add_f32 v207, v207, v239\n\t"
    "v_cvt_pk_bf16_f32 v192, v192, v193\n\t"
    "v_cvt_pk_bf16_f32 v193, v194, v195\n\t"
    "v_cvt_pk_bf16_f32 v194, v196, v197\n\t"
    "v_cvt_pk_bf16_f32 v195, v198, v199\n\t"
    "v_cvt_pk_bf16_f32 v196, v200, v201\n\t"
    "v_cvt_pk_bf16_f32 v197, v202, v203\n\t"
    "v_cvt_pk_bf16_f32 v198, v204, v205\n\t"
    "v_cvt_pk_bf16_f32 v199, v206, v207\n\t"
    "v_permlane32_swap_b32 v192, v194\n\t"
    "v_permlane32_swap_b32 v193, v195\n\t"
    "v_permlane32_swap_b32 v196, v198\n\t"
    "v_permlane32_swap_b32 v197, v199\n\t"
    "v_permlane16_swap_b32 v192, v194\n\t"
    "v_permlane16_swap_b32 v193, v195\n\t"
    "v_permlane16_swap_b32 v196, v198\n\t"
    "v_permlane16_swap_b32 v197, v199\n\t"
    "buffer_store_dwordx4 v[192:195], v248, s[44:47], 0 offen\n\t"
    "s_movk_i32 s63, 0x4000\n\t"
    "buffer_store_dwordx4 v[196:199], v248, s[44:47], s63 offen\n\t"
    : "+{s[56:57]}"(fl2)
    : [w]"s"(w), [part]"s"(pp), [wgi]"s"(wgi), [wo]"s"(wop), [ofr]"s"(ofr), [out]"s"(outp), [pollfl]"s"(pollfl), [onrec]"s"(onrec), [bump]"s"(bumpo), [tagin]"s"(tag)
    : "v0", "v1", "v2", "v3", "v4", "v5", "v6", "v7", "v8", "v9", "v10", "v11", "v12", "v13", "v14", "v15", "v16", "v17", "v18", "v19", "v20", "v21", "v22", "v23", "v24", "v25", "v26", "v27", "v28", "v29", "v30", "v31", "v32", "v33", "v34", "v35", "v36", "v37", "v38", "v39", "v40", "v41", "v42", "v43", "v44", "v45", "v46", "v47", "v48", "v49", "v50", "v51", "v52", "v53", "v54", "v55", "v56", "v57", "v58", "v59", "v60", "v61", "v62", "v63", "v64", "v65", "v66", "v67", "v68", "v69", "v70", "v71", "v72", "v73", "v74", "v75", "v76", "v77", "v78", "v79", "v80", "v81", "v82", "v83", "v84", "v85", "v86", "v87", "v88", "v89", "v90", "v91", "v92", "v93", "v94", "v95", "v96", "v97", "v98", "v99", "v100", "v101", "v102", "v103", "v104", "v105", "v106", "v107", "v108", "v109", "v110", "v111", "v112", "v113", "v114", "v115", "v116", "v117", "v118", "v119", "v120", "v121", "v122", "v123", "v124", "v125", "v126", "v127", "v128", "v129", "v130", "v131", "v132", "v133", "v134", "v135", "v136", "v137", "v138", "v139", "v140", "v141", "v142", "v143", "v144", "v145", "v146", "v147", "v148", "v149", "v150", "v151", "v152", "v153", "v154", "v155", "v156", "v157", "v158", "v159", "v160", "v161", "v162", "v163", "v164", "v165", "v166", "v167", "v168", "v169", "v170", "v171", "v172", "v173", "v174", "v175", "v176", "v177", "v178", "v179", "v180", "v181", "v182", "v183", "v184", "v185", "v186", "v187", "v188", "v189", "v190", "v191", "v192", "v193", "v194", "v195", "v196", "v197", "v198", "v199", "v200", "v201", "v202", "v203", "v204", "v205", "v206", "v207", "v208", "v209", "v210", "v211", "v212", "v213", "v214", "v215", "v216", "v217", "v218", "v219", "v220", "v221", "v222", "v223", "v224", "v225", "v226", "v227", "v228", "v229", "v230", "v231", "v232", "v233", "v234", "v235", "v236", "v237", "v238", "v239", "v240", "v241", "v242", "v243", "v244", "v245", "v246", "v247", "v248", "v249", "v250", "v251", "v252", "v253", "v254", "v255", "s36", "s37", "s38", "s39", "s40", "s41", "s42", "s43", "s44", "s45", "s46", "s47", "s48", "s49", "s50", "s51", "s52", "s53", "s54", "s55", "s58", "s59", "s60", "s61", "s62", "s63", "s64", "s65", "s66", "s67", "s68", "s69", "s70", "s71", "s72", "s73", "s74", "s75", "s76", "s77", "s78", "s79", "s80", "s81", "s82", "s83", "vcc", "scc", "memory");
  }
}
extern "C" void kernel_launch(void* const* d_in, const int* in_sizes, int n_in, void* d_out, int out_size, void* d_ws, size_t ws_size, hipStream_t stream){
  const u16* x=(const u16*)d_in[0]; const u16* kc=(const u16*)d_in[1]; const u16* vc=(const u16*)d_in[2];
  const u16* wq=(const u16*)d_in[3]; const u16* wo=(const u16*)d_in[4];
  char* base=(char*)d_ws + 0;
  u16* ows=(u16*)base;
  char* flags=base+(56u<<20);
  (void)hipFuncSetAttribute((const void*)gqa_fused, hipFuncAttributeMaxDynamicSharedMemorySize, 163840);
  gqa_fused<<<256,512,163840,stream>>>(x,kc,vc,wq,wo,(u16*)d_out,ows,flags);
}
```
